```python
import torch

def _softmax(x, axis=-1):
    return torch.softmax(x, dim=axis)
def _gelu(x):
    return torch.nn.functional.gelu(x, approximate="tanh")
def _silu(x):
    return torch.nn.functional.silu(x)
def _sigmoid(x):
    return torch.sigmoid(x)
def _layernorm(x, axis=-1, eps=1e-5):
    m = x.mean(dim=axis, keepdim=True)
    v = x.var(dim=axis, keepdim=True, unbiased=False)
    return (x - m) / torch.sqrt(v + eps)
def _scatter_add(tgt, idx, src, axis=0):
    return tgt.clone().index_add_(axis, idx, src)
def _segment_sum(src, seg, n):
    out = torch.zeros((n,) + src.shape[1:], dtype=src.dtype)
    return out.index_add_(0, seg, src)
def _pad(x, pw):
    flat = tuple(v for pair in reversed(pw) for v in pair)
    return torch.nn.functional.pad(x, flat)

kernel_name = 'parallel_sum__sliding_local_dilated__geglu_ffn__prompt'

def setup_inputs():

    _w = (lambda a: (lambda t: t.to(torch.bfloat16) if t.is_floating_point() else t)(torch.as_tensor(a)))
    def _A_setup():
        B, S, D = 1, 2048, 448
        return {
            "x": _w((lambda *s: torch.randn(*s, dtype=torch.bfloat16) * 0.05)(B, S, D)),
            "wqkv": _w((lambda *s: torch.randn(*s, dtype=torch.bfloat16) * 0.05)(D, 3 * D)),
            "wo": _w((lambda *s: torch.randn(*s, dtype=torch.bfloat16) * 0.05)(D, D)),
        }
    _A_in = _A_setup()

    def _B_setup():
        B, S, D, F = 1, 2048, 448, 1792
        return {
            "x": _w((lambda *s: torch.randn(*s, dtype=torch.bfloat16) * 0.05)(B, S, D)),
            "w_gate": _w((lambda *s: torch.randn(*s, dtype=torch.bfloat16) * 0.05)(D, F)),
            "w_up": _w((lambda *s: torch.randn(*s, dtype=torch.bfloat16) * 0.05)(D, F)),
            "w_down": _w((lambda *s: torch.randn(*s, dtype=torch.bfloat16) * 0.05)(F, D)),
        }
    _B_in = _B_setup()

    return {
        "A_x": _A_in["x"],
        "A_wqkv": _A_in["wqkv"],
        "A_wo": _A_in["wo"],
        "B_x": _B_in["x"],
        "B_w_gate": _B_in["w_gate"],
        "B_w_up": _B_in["w_up"],
        "B_w_down": _B_in["w_down"],
    }

def reference(A_x, A_wqkv, A_wo, B_x, B_w_gate, B_w_up, B_w_down):
    def _A_ref(x, wqkv, wo):
        B, S, D = x.shape
        H = 16; Dh = D // H
        W, dil = 8, 2
        qkv = (lambda x, *d: x.permute(*d))((x @ wqkv).reshape(B, S, 3, H, Dh), 2, 0, 3, 1, 4)
        q, k, v = qkv[0], qkv[1], qkv[2]
        scale = Dh ** -0.5
        pos = torch.arange(S)
        off = torch.arange(W) * dil
        idx = pos[:, None] - off[None, :]
        mask = idx >= 0
        idx_c = torch.where(mask, idx, 0).reshape(-1)
        kg = k[:, :, idx_c, :].reshape(B, H, S, W, Dh)
        vg = v[:, :, idx_c, :].reshape(B, H, S, W, Dh)
        logits = torch.einsum("bhsd,bhswd->bhsw", q, kg) * scale
        logits = torch.where(mask[None, None, :, :], logits, -1e9)
        attn = _softmax(logits, axis=-1)
        o = torch.einsum("bhsw,bhswd->bhsd", attn, vg)
        return (lambda x, *d: x.permute(*d))(o, 0, 2, 1, 3).reshape(B, S, D) @ wo

    def _B_ref(x, w_gate, w_up, w_down):
        return (_gelu(x @ w_gate) * (x @ w_up)) @ w_down

    x = A_x
    xn = _layernorm(x, axis=-1)
    return x + _A_ref(xn, A_wqkv, A_wo) + _B_ref(xn, B_w_gate, B_w_up, B_w_down)
```

```cpp
#include <hip/hip_runtime.h>
#include <hip/hip_bf16.h>
#include <stdint.h>
typedef unsigned short u16;
constexpr int S=2048, D=448, F=1792, H=16, DH=28, W=8, DIL=2;
__device__ __forceinline__ float bf2f(u16 v){ return __uint_as_float(((unsigned)v)<<16); }
__device__ __forceinline__ u16 f2bf(float f){ unsigned u=__float_as_uint(f); u += 0x7fffu+((u>>16)&1u); return (u16)(u>>16); }
__device__ __forceinline__ float rbf(float f){ return bf2f(f2bf(f)); }
#define MIMIC 1
__global__ void k_ln(const u16* x, float* xn){
  int row=blockIdx.x; int t=threadIdx.x; float v[7]; float s=0;
  for(int i=0;i<7;i++){ v[i]=bf2f(x[row*D+t+64*i]); s+=v[i]; }
  for(int o=32;o;o>>=1) s+=__shfl_xor(s,o);
  float m=s/D; float q=0;
  for(int i=0;i<7;i++){ float d=v[i]-m; q+=d*d; }
  for(int o=32;o;o>>=1) q+=__shfl_xor(q,o);
  float var=q/D;
  if(MIMIC){ float mb=rbf(m), vb=rbf(var); float sd=rbf(sqrtf(rbf(vb+1e-5f)));
    for(int i=0;i<7;i++) xn[row*D+t+64*i]=rbf(rbf(v[i]-mb)/sd);
  } else { float r=rsqrtf(var+1e-5f); for(int i=0;i<7;i++) xn[row*D+t+64*i]=(v[i]-m)*r; }
}
__global__ void k_gemm(const float* A,const u16* B,float* C,int M,int N,int K){
  __shared__ float As[16][17];
  int n=blockIdx.x*16+threadIdx.x, m=blockIdx.y*16+threadIdx.y; float acc=0;
  for(int k0=0;k0<K;k0+=16){ As[threadIdx.y][threadIdx.x]=A[m*K+k0+threadIdx.x]; __syncthreads();
    for(int k=0;k<16;k++) acc+=As[threadIdx.y][k]*bf2f(B[(k0+k)*N+n]); __syncthreads(); }
  C[m*N+n]= MIMIC? rbf(acc):acc;
}
__global__ void k_attn(const float* qkv,float* o){
  int idx=blockIdx.x*blockDim.x+threadIdx.x; int s=idx/H,h=idx%H; if(s>=S) return;
  float l[W]; float mx=-1e30f; const float sc=rsqrtf((float)DH);
  for(int w=0;w<W;w++){ int p=s-w*DIL; float a=-1e9f; if(p>=0){ a=0; for(int d=0;d<DH;d++) a+=qkv[s*3*D+h*DH+d]*qkv[p*3*D+D+h*DH+d]; a*=sc; } l[w]=a; mx=fmaxf(mx,a);}
  float sum=0; for(int w=0;w<W;w++){ l[w]=__expf(l[w]-mx); sum+=l[w]; }
  for(int d=0;d<DH;d++){ float a=0; for(int w=0;w<W;w++){ int p=s-w*DIL; if(p<0)p=0; a+=l[w]*qkv[p*3*D+2*D+h*DH+d]; } o[s*D+h*DH+d]= MIMIC? rbf(a/sum):a/sum; }
}
__global__ void k_glu(const float* g,const float* u,float* h){
  int i=blockIdx.x*blockDim.x+threadIdx.x; float x=g[i]; float t=tanhf(0.7978845608f*(x+0.044715f*x*x*x)); float ge=0.5f*x*(1.f+t);
  h[i]= MIMIC? rbf(rbf(ge)*u[i]) : ge*u[i];
}
__global__ void k_fin(const u16* x,const float* a,const float* b,u16* out){
  int i=blockIdx.x*blockDim.x+threadIdx.x; out[i]= MIMIC? f2bf(rbf(bf2f(x[i])+a[i])+b[i]) : f2bf(bf2f(x[i])+a[i]+b[i]);
}
extern "C" void kernel_launch(void* const* d_in,const int* in_sizes,int n_in,void* d_out,int out_size,void* d_ws,size_t ws_size,hipStream_t stream){
  const u16* x=(const u16*)d_in[0]; const u16* wqkv=(const u16*)d_in[1]; const u16* wo=(const u16*)d_in[2];
  const u16* wg=(const u16*)d_in[4]; const u16* wu=(const u16*)d_in[5]; const u16* wd=(const u16*)d_in[6];
  float* ws=(float*)d_ws; float* xn=ws; float* qkv=xn+S*D; float* o=qkv+S*3*D; float* g=o+S*D; float* u=g+S*F; float* h=u+S*F; float* a=h+S*F; float* b=a+S*D;
  k_ln<<<S,64,0,stream>>>(x,xn);
  dim3 t(16,16);
  k_gemm<<<dim3(3*D/16,S/16),t,0,stream>>>(xn,wqkv,qkv,S,3*D,D);
  k_gemm<<<dim3(F/16,S/16),t,0,stream>>>(xn,wg,g,S,F,D);
  k_gemm<<<dim3(F/16,S/16),t,0,stream>>>(xn,wu,u,S,F,D);
  k_attn<<<S*H/256,256,0,stream>>>(qkv,o);
  k_glu<<<S*F/256,256,0,stream>>>(g,u,h);
  k_gemm<<<dim3(D/16,S/16),t,0,stream>>>(o,wo,a,S,D,D);
  k_gemm<<<dim3(D/16,S/16),t,0,stream>>>(h,wd,b,S,D,F);
  k_fin<<<S*D/256,256,0,stream>>>(x,a,b,(u16*)d_out);
}
```

```cpp
#include <hip/hip_runtime.h>
#include <hip/hip_bf16.h>
#include <stdint.h>
typedef unsigned short u16;
constexpr int S=2048, D=448, F=1792, H=16, DH=28, W=8, DIL=2;
__device__ __forceinline__ float bf2f(u16 v){ return __uint_as_float(((unsigned)v)<<16); }
__device__ __forceinline__ u16 f2bf(float f){ unsigned u=__float_as_uint(f); u += 0x7fffu+((u>>16)&1u); return (u16)(u>>16); }
__device__ __forceinline__ float rbf(float f){ return bf2f(f2bf(f)); }
#define MIMIC 0
__global__ void k_ln(const u16* x, float* xn){
  int row=blockIdx.x; int t=threadIdx.x; float v[7]; float s=0;
  for(int i=0;i<7;i++){ v[i]=bf2f(x[row*D+t+64*i]); s+=v[i]; }
  for(int o=32;o;o>>=1) s+=__shfl_xor(s,o);
  float m=s/D; float q=0;
  for(int i=0;i<7;i++){ float d=v[i]-m; q+=d*d; }
  for(int o=32;o;o>>=1) q+=__shfl_xor(q,o);
  float var=q/D;
  if(MIMIC){ float mb=rbf(m), vb=rbf(var); float sd=rbf(sqrtf(rbf(vb+1e-5f)));
    for(int i=0;i<7;i++) xn[row*D+t+64*i]=rbf(rbf(v[i]-mb)/sd);
  } else { float r=rsqrtf(var+1e-5f); for(int i=0;i<7;i++) xn[row*D+t+64*i]=(v[i]-m)*r; }
}
__global__ void k_gemm(const float* A,const u16* B,float* C,int M,int N,int K){
  __shared__ float As[16][17];
  int n=blockIdx.x*16+threadIdx.x, m=blockIdx.y*16+threadIdx.y; float acc=0;
  for(int k0=0;k0<K;k0+=16){ As[threadIdx.y][threadIdx.x]=A[m*K+k0+threadIdx.x]; __syncthreads();
    for(int k=0;k<16;k++) acc+=As[threadIdx.y][k]*bf2f(B[(k0+k)*N+n]); __syncthreads(); }
  C[m*N+n]= MIMIC? rbf(acc):acc;
}
__global__ void k_attn(const float* qkv,float* o){
  int idx=blockIdx.x*blockDim.x+threadIdx.x; int s=idx/H,h=idx%H; if(s>=S) return;
  float l[W]; float mx=-1e30f; const float sc=rsqrtf((float)DH);
  for(int w=0;w<W;w++){ int p=s-w*DIL; float a=-1e9f; if(p>=0){ a=0; for(int d=0;d<DH;d++) a+=qkv[s*3*D+h*DH+d]*qkv[p*3*D+D+h*DH+d]; a*=sc; } l[w]=a; mx=fmaxf(mx,a);}
  float sum=0; for(int w=0;w<W;w++){ l[w]=__expf(l[w]-mx); sum+=l[w]; }
  for(int d=0;d<DH;d++){ float a=0; for(int w=0;w<W;w++){ int p=s-w*DIL; if(p<0)p=0; a+=l[w]*qkv[p*3*D+2*D+h*DH+d]; } o[s*D+h*DH+d]= MIMIC? rbf(a/sum):a/sum; }
}
__global__ void k_glu(const float* g,const float* u,float* h){
  int i=blockIdx.x*blockDim.x+threadIdx.x; float x=g[i]; float t=tanhf(0.7978845608f*(x+0.044715f*x*x*x)); float ge=0.5f*x*(1.f+t);
  h[i]= MIMIC? rbf(rbf(ge)*u[i]) : ge*u[i];
}
__global__ void k_fin(const u16* x,const float* a,const float* b,u16* out){
  int i=blockIdx.x*blockDim.x+threadIdx.x; out[i]= MIMIC? f2bf(rbf(bf2f(x[i])+a[i])+b[i]) : f2bf(bf2f(x[i])+a[i]+b[i]);
}

typedef unsigned v4u __attribute__((ext_vector_type(4)));
typedef unsigned long long u64;
__global__ void k_enc(const u64* st,int n,int mode){
  __shared__ u64 v[256];
  int t=threadIdx.x;
  u64 d = t<n ? st[4*t+1]-st[4*t] : 0; v[t]=d; __syncthreads();
  if(t==0){
    u64 stat=0;
    if(mode==0){ for(int i=0;i<n;i++){ int c=0; for(int j=0;j<n;j++) c+= (v[j]<v[i])||(v[j]==v[i]&&j<i); if(c==n/2) stat=v[i]; } }
    if(mode==1){ for(int i=0;i<n;i++) stat=v[i]>stat?v[i]:stat; }
    if(mode>=2){ u64 mn=~0ull,mx=0; for(int i=0;i<n;i++){ u64 a=st[4*i+2], b=st[4*i+(mode==2?2:3)]; mn=a<mn?a:mn; mx=b>mx?b:mx; } stat=(mx-mn)*24; }
    if(stat>40000) stat=40000;
    u64 r0=__builtin_amdgcn_s_memrealtime();
    while(__builtin_amdgcn_s_memrealtime()-r0 < stat*10) __builtin_amdgcn_s_sleep(4);
  }
}
#define STAMP(t) asm volatile("s_memtime %0\n\ts_waitcnt lgkmcnt(0)":"=s"(t)::"memory")
__global__ __launch_bounds__(256) void k_probe(const char* x,const char* w,u64* st){
  u64 t0,t1; u64 r0=__builtin_amdgcn_s_memrealtime();
  STAMP(t0);
  int g=blockIdx.x>>4; const v4u* p=(const v4u*)(x+g*114688)+threadIdx.x;
  v4u r[28];
  #pragma unroll
  for(int i=0;i<28;i++) r[i]=p[i*256];
  v4u a=r[0];
  #pragma unroll
  for(int i=1;i<28;i++) a^=r[i];
  asm volatile(""::"v"(a));
  __syncthreads();
  STAMP(t1); u64 r1=__builtin_amdgcn_s_memrealtime();
  if(threadIdx.x==0){ st[4*blockIdx.x]=t0; st[4*blockIdx.x+1]=t1; st[4*blockIdx.x+2]=r0; st[4*blockIdx.x+3]=r1; } }
extern "C" void kernel_launch(void* const* d_in,const int* in_sizes,int n_in,void* d_out,int out_size,void* d_ws,size_t ws_size,hipStream_t stream){
  const u16* x=(const u16*)d_in[0]; const u16* wqkv=(const u16*)d_in[1]; const u16* wo=(const u16*)d_in[2];
  const u16* wg=(const u16*)d_in[4]; const u16* wu=(const u16*)d_in[5]; const u16* wd=(const u16*)d_in[6];
  float* ws=(float*)d_ws; float* xn=ws; float* qkv=xn+S*D; float* o=qkv+S*3*D; float* g=o+S*D; float* u=g+S*F; float* h=u+S*F; float* a=h+S*F; float* b=a+S*D;
  u64* st=(u64*)((char*)d_ws+200*1024*1024);
  k_probe<<<256,256,0,stream>>>((const char*)x,(const char*)wg,st);
  k_enc<<<1,256,0,stream>>>(st,256,2);
  k_ln<<<S,64,0,stream>>>(x,xn);
  dim3 t(16,16);
  k_gemm<<<dim3(3*D/16,S/16),t,0,stream>>>(xn,wqkv,qkv,S,3*D,D);
  k_gemm<<<dim3(F/16,S/16),t,0,stream>>>(xn,wg,g,S,F,D);
  k_gemm<<<dim3(F/16,S/16),t,0,stream>>>(xn,wu,u,S,F,D);
  k_attn<<<S*H/256,256,0,stream>>>(qkv,o);
  k_glu<<<S*F/256,256,0,stream>>>(g,u,h);
  k_gemm<<<dim3(D/16,S/16),t,0,stream>>>(o,wo,a,S,D,D);
  k_gemm<<<dim3(D/16,S/16),t,0,stream>>>(h,wd,b,S,D,F);
  k_fin<<<S*D/256,256,0,stream>>>(x,a,b,(u16*)d_out);
}
```

```cpp
#include <hip/hip_runtime.h>
#include <hip/hip_bf16.h>
#include <stdint.h>
typedef unsigned short u16;
constexpr int S=2048, D=448, F=1792, H=16, DH=28, W=8, DIL=2;
__device__ __forceinline__ float bf2f(u16 v){ return __uint_as_float(((unsigned)v)<<16); }
__device__ __forceinline__ u16 f2bf(float f){ unsigned u=__float_as_uint(f); u += 0x7fffu+((u>>16)&1u); return (u16)(u>>16); }
__device__ __forceinline__ float rbf(float f){ return bf2f(f2bf(f)); }
#define MIMIC 0
__global__ void k_ln(const u16* x, float* xn){
  int row=blockIdx.x; int t=threadIdx.x; float v[7]; float s=0;
  for(int i=0;i<7;i++){ v[i]=bf2f(x[row*D+t+64*i]); s+=v[i]; }
  for(int o=32;o;o>>=1) s+=__shfl_xor(s,o);
  float m=s/D; float q=0;
  for(int i=0;i<7;i++){ float d=v[i]-m; q+=d*d; }
  for(int o=32;o;o>>=1) q+=__shfl_xor(q,o);
  float var=q/D;
  if(MIMIC){ float mb=rbf(m), vb=rbf(var); float sd=rbf(sqrtf(rbf(vb+1e-5f)));
    for(int i=0;i<7;i++) xn[row*D+t+64*i]=rbf(rbf(v[i]-mb)/sd);
  } else { float r=rsqrtf(var+1e-5f); for(int i=0;i<7;i++) xn[row*D+t+64*i]=(v[i]-m)*r; }
}
__global__ void k_gemm(const float* A,const u16* B,float* C,int M,int N,int K){
  __shared__ float As[16][17];
  int n=blockIdx.x*16+threadIdx.x, m=blockIdx.y*16+threadIdx.y; float acc=0;
  for(int k0=0;k0<K;k0+=16){ As[threadIdx.y][threadIdx.x]=A[m*K+k0+threadIdx.x]; __syncthreads();
    for(int k=0;k<16;k++) acc+=As[threadIdx.y][k]*bf2f(B[(k0+k)*N+n]); __syncthreads(); }
  C[m*N+n]= MIMIC? rbf(acc):acc;
}
__global__ void k_attn(const float* qkv,float* o){
  int idx=blockIdx.x*blockDim.x+threadIdx.x; int s=idx/H,h=idx%H; if(s>=S) return;
  float l[W]; float mx=-1e30f; const float sc=rsqrtf((float)DH);
  for(int w=0;w<W;w++){ int p=s-w*DIL; float a=-1e9f; if(p>=0){ a=0; for(int d=0;d<DH;d++) a+=qkv[s*3*D+h*DH+d]*qkv[p*3*D+D+h*DH+d]; a*=sc; } l[w]=a; mx=fmaxf(mx,a);}
  float sum=0; for(int w=0;w<W;w++){ l[w]=__expf(l[w]-mx); sum+=l[w]; }
  for(int d=0;d<DH;d++){ float a=0; for(int w=0;w<W;w++){ int p=s-w*DIL; if(p<0)p=0; a+=l[w]*qkv[p*3*D+2*D+h*DH+d]; } o[s*D+h*DH+d]= MIMIC? rbf(a/sum):a/sum; }
}
__global__ void k_glu(const float* g,const float* u,float* h){
  int i=blockIdx.x*blockDim.x+threadIdx.x; float x=g[i]; float t=tanhf(0.7978845608f*(x+0.044715f*x*x*x)); float ge=0.5f*x*(1.f+t);
  h[i]= MIMIC? rbf(rbf(ge)*u[i]) : ge*u[i];
}
__global__ void k_fin(const u16* x,const float* a,const float* b,u16* out){
  int i=blockIdx.x*blockDim.x+threadIdx.x; out[i]= MIMIC? f2bf(rbf(bf2f(x[i])+a[i])+b[i]) : f2bf(bf2f(x[i])+a[i]+b[i]);
}

typedef unsigned v4u __attribute__((ext_vector_type(4)));
typedef unsigned long long u64;
__global__ void k_enc(const u64* st,int n,int mode){
  __shared__ u64 v[256];
  int t=threadIdx.x;
  u64 d = t<n ? st[4*t+1]-st[4*t] : 0; v[t]=d; __syncthreads();
  __shared__ u64 med; { int c=0; for(int j=0;j<n;j++) c+= (v[j]<d)||(v[j]==d&&j<t); if(c==n/2) med=d; } __syncthreads();
  if(t==0){
    u64 stat=0;
    if(mode==0) stat=med;
    if(mode==1){ for(int i=0;i<n;i++) stat=v[i]>stat?v[i]:stat; }
    if(mode>=2){ u64 mn=~0ull,mx=0; for(int i=0;i<n;i++){ u64 a=st[4*i+2], b=st[4*i+(mode==2?2:3)]; mn=a<mn?a:mn; mx=b>mx?b:mx; } stat=(mx-mn)*24; }
    if(stat>40000) stat=40000;
    u64 r0=__builtin_amdgcn_s_memrealtime();
    while(__builtin_amdgcn_s_memrealtime()-r0 < stat*20) __builtin_amdgcn_s_sleep(4);
  }
}
#define STAMP(t) asm volatile("s_memtime %0\n\ts_waitcnt lgkmcnt(0)":"=s"(t)::"memory")
__global__ void k_probe(const char* x,const char* w,u64* st){ if(threadIdx.x==0){ st[4*blockIdx.x]=0; st[4*blockIdx.x+1]=0; } }
extern "C" void kernel_launch(void* const* d_in,const int* in_sizes,int n_in,void* d_out,int out_size,void* d_ws,size_t ws_size,hipStream_t stream){
  const u16* x=(const u16*)d_in[0]; const u16* wqkv=(const u16*)d_in[1]; const u16* wo=(const u16*)d_in[2];
  const u16* wg=(const u16*)d_in[4]; const u16* wu=(const u16*)d_in[5]; const u16* wd=(const u16*)d_in[6];
  float* ws=(float*)d_ws; float* xn=ws; float* qkv=xn+S*D; float* o=qkv+S*3*D; float* g=o+S*D; float* u=g+S*F; float* h=u+S*F; float* a=h+S*F; float* b=a+S*D;
  u64* st=(u64*)((char*)d_ws+200*1024*1024);
  k_probe<<<256,256,0,stream>>>((const char*)x,(const char*)wg,st);
  k_enc<<<1,256,0,stream>>>(st,256,0);
  k_ln<<<S,64,0,stream>>>(x,xn);
  dim3 t(16,16);
  k_gemm<<<dim3(3*D/16,S/16),t,0,stream>>>(xn,wqkv,qkv,S,3*D,D);
  k_gemm<<<dim3(F/16,S/16),t,0,stream>>>(xn,wg,g,S,F,D);
  k_gemm<<<dim3(F/16,S/16),t,0,stream>>>(xn,wu,u,S,F,D);
  k_attn<<<S*H/256,256,0,stream>>>(qkv,o);
  k_glu<<<S*F/256,256,0,stream>>>(g,u,h);
  k_gemm<<<dim3(D/16,S/16),t,0,stream>>>(o,wo,a,S,D,D);
  k_gemm<<<dim3(D/16,S/16),t,0,stream>>>(h,wd,b,S,D,F);
  k_fin<<<S*D/256,256,0,stream>>>(x,a,b,(u16*)d_out);
}
```

```cpp
#include <hip/hip_runtime.h>

#define SRD(n) const void* n, unsigned n##_bytes, unsigned n##_fmt
__global__ __launch_bounds__(256, 1) __attribute__((aligned(4096))) void fused_block_kernel(SRD(wu), SRD(x), SRD(wqkv), SRD(wg), SRD(wd), SRD(wo), SRD(out), SRD(ws)) {
  __shared__ __attribute__((aligned(1024))) char lds[163840];
#if defined(__HIP_DEVICE_COMPILE__)
  asm volatile(
      ".LSTART:\n"
      "s_load_dwordx16 s[16:31], s[0:1], 0x0\n"
      "s_load_dwordx16 s[36:51], s[0:1], 0x40\n"
      "v_lshrrev_b32 v1, 6, v0\n"
      "s_lshr_b32 s56, s2, 3\n"
      "v_and_b32 v0, 63, v0\n"
      "v_readfirstlane_b32 s52, v1\n"
      "v_and_b32 v69, 3, v0\n"
      "s_getpc_b64 s[58:59]\n"
      "v_lshl_add_u32 v1, s56, 2, v1\n"
      "v_mad_u32_u24 v1, v1, 5, v0\n"
      "v_lshlrev_b32 v1, 7, v1\n"
      "v_min_u32 v1, .LEND-.LSTART-128, v1\n"
      "s_mov_b64 exec, 31\n"
      "global_load_dword v45, v1, s[58:59]\n"
      "s_mov_b64 exec, -1\n"
      "s_lshr_b32 s55, s2, 5\n"
      "s_and_b32 s56, s56, 3\n"
      "s_bfe_u32 s57, s2, 0x20001\n"
      "s_lshl2_add_u32 s53, s57, s56\n"
      "s_and_b32 s54, s2, 1\n"
      "s_mul_i32 s3, s57, 2\n"
      "s_mul_i32 s57, s54, 4\n"
      "s_add_u32 s3, s3, s57\n"
      "s_xnor_b32 s57, s55, s54\n"
      "s_and_b32 s57, s57, 1\n"
      "s_mul_i32 s57, s57, 2\n"
      "s_add_u32 s3, s3, s57\n"
      "s_sub_u32 s57, s3, 7\n"
      "s_min_u32 s3, s3, s57\n"
      "v_lshrrev_b32 v64, 3, v0\n"
      "v_and_b32 v65, 7, v0\n"
      "v_lshrrev_b32 v75, 4, v0\n"
      "v_xor_b32 v75, v75, v65\n"
      "v_mul_u32_u24 v76, 1792, v64\n"
      "v_lshl_add_u32 v40, v75, 4, v76\n"
      "v_xor_b32 v41, 64, v40\n"
      "v_add_u32 v41, 13312, v41\n"
      "v_add_u32 v42, 26624, v40\n"
      "v_add_u32 v43, 26624, v41\n"
      "s_lshl_b32 s95, s52, 12\n"
      "s_lshr_b32 s59, s52, 1\n"
      "s_lshl1_add_u32 s56, s53, s59\n"
      "s_lshl_b32 s56, s56, 6\n"
      "s_and_b32 s60, s52, 1\n"
      "s_add_u32 s56, s56, s60\n"
      "s_mul_i32 s98, s56, 896\n"
      "s_mov_b32 m0, s95\n"
      "s_add_u32 s11, s98, 896\n"
      "s_lshl_b32 s57, s3, 7\n"
      "s_add_u32 s61, s98, s57\n"
      "s_lshl2_add_u32 s12, s53, s52\n"
      "s_cmpk_lt_u32 s2, 224\n"
      "s_waitcnt lgkmcnt(0)\n"
      "s_cbranch_scc0 .LAUX\n"
      "buffer_load_dwordx4 v40, s[20:23], s61 offen offset:0 lds\n"
      "buffer_load_dwordx4 v41, s[20:23], s61 offen offset:1024 lds\n"
      "buffer_load_dwordx4 v42, s[20:23], s61 offen offset:2048 lds\n"
      "buffer_load_dwordx4 v43, s[20:23], s61 offen offset:3072 lds\n"
      "s_addk_i32 s61, 128\n"
      "s_addk_i32 m0, 0x4000\n"
      "s_cmp_eq_u32 s61, s11\n"
      "s_cselect_b32 s61, s98, s61\n"
      "buffer_load_dwordx4 v40, s[20:23], s61 offen offset:0 lds\n"
      "buffer_load_dwordx4 v41, s[20:23], s61 offen offset:1024 lds\n"
      "buffer_load_dwordx4 v42, s[20:23], s61 offen offset:2048 lds\n"
      "buffer_load_dwordx4 v43, s[20:23], s61 offen offset:3072 lds\n"
      "s_addk_i32 s61, 128\n"
      "s_addk_i32 m0, 0x4000\n"
      "s_cmp_eq_u32 s61, s11\n"
      "s_cselect_b32 s61, s98, s61\n"
      "s_bitcmp1_b32 s2, 0\n"
      "s_cbranch_scc1 .LMV\n"
      "s_branch .LNORET\n"
      ".LRET:\n"
      "s_setpc_b64 s[14:15]\n"
      ".LNORET:\n"
      ".LMK:\n"
      "v_bfe_u32 v36, v0, 1, 3\n"
      "v_lshrrev_b32 v74, 4, v0\n"
      "v_xor_b32 v75, v36, v74\n"
      "v_and_b32 v76, 15, v0\n"
      "v_lshl_add_u32 v75, v75, 4, s95\n"
      "v_lshl_add_u32 v32, v76, 7, v75\n"
      "v_xor_b32 v33, 64, v32\n"
      "v_xor_b32 v34, 0x10000, v32\n"
      "v_xor_b32 v35, 64, v34\n"
      "v_and_b32 v66, 1, v74\n"
      "v_lshrrev_b32 v67, 5, v0\n"
      "v_and_b32 v68, 31, v0\n"
      "v_mov_b32 v30, 0x3f80\n"
      "v_mov_b32 v31, 0\n"
      "v_lshlrev_b32 v75, 4, v69\n"
      "v_lshlrev_b64 v[28:29], v75, v[30:31]\n"
      "v_mov_b32 v30, 0x3f803f80\n"
      "v_mov_b32 v31, v30\n"
      "s_movk_i32 s62, 20480\n"
      "s_lshl_b32 s63, s62, 1\n"
      "s_add_u32 s64, s62, s63\n"
      "s_lshl_b32 s58, s55, 7\n"
      "s_add_u32 s56, s3, s59\n"
      "s_lshl1_add_u32 s56, s56, s60\n"
      "s_sub_u32 s57, s56, 14\n"
      "s_min_u32 s56, s56, s57\n"
      "s_mul_i32 s34, s56, 86016\n"
      "s_add_u32 s56, s56, 4\n"
      "s_sub_u32 s57, s56, 14\n"
      "s_min_u32 s56, s56, s57\n"
      "s_mul_i32 s86, s56, 86016\n"
      "s_add_u32 s56, s56, 4\n"
      "s_sub_u32 s57, s56, 14\n"
      "s_min_u32 s56, s56, s57\n"
      "s_mul_i32 s87, s56, 86016\n"
      "s_sub_u32 s56, s3, 1\n"
      "s_min_u32 s56, s56, 6\n"
      "s_lshl2_add_u32 s56, s56, s52\n"
      "s_mul_i32 s99, s56, 43008\n"
      "v_lshlrev_b32 v75, 1, v66\n"
      "v_xor_b32 v75, v75, v65\n"
      "v_lshlrev_b32 v75, 4, v75\n"
      "v_mul_u32_u24 v3, 2688, v64\n"
      "s_add_u32 s56, s58, 896\n"
      "v_add3_u32 v3, v3, v75, s56\n"
      "v_bfe_u32 v71, v0, 2, 2\n"
      "v_lshlrev_b32 v70, 7, v71\n"
      "v_lshrrev_b32 v71, 1, v71\n"
      "v_lshl_or_b32 v75, v66, 1, v71\n"
      "v_lshl_add_u32 v75, v75, 5, v70\n"
      "v_lshl_add_u32 v37, v69, 3, v75\n"
      "v_lshl_add_u32 v6, v74, 10, v37\n"
      "v_lshlrev_b32 v1, 4, v0\n"
      "v_cmp_gt_u32 vcc, 1, v0\n"
      "v_bfrev_b32 v73, 1\n"
      "v_lshlrev_b32 v75, 3, v0\n"
      "v_cndmask_b32 v13, v73, v75, vcc\n"
      "s_sub_u32 s56, s55, s3\n"
      "s_add_u32 s57, s56, 7\n"
      "s_min_u32 s56, s56, s57\n"
      "s_lshl_b32 s56, s56, 14\n"
      "s_add_u32 s56, s56, s95\n"
      "v_xor_b32 v75, v36, v67\n"
      "v_lshl_add_u32 v75, v75, 4, s56\n"
      "v_lshl_add_u32 v44, v68, 7, v75\n"
      "v_mul_u32_u24 v75, 2040, v66\n"
      "v_sub_u32 v44, v44, v75\n"
      "v_xor_b32 v46, 32, v44\n"
      "v_lshl_add_u32 v75, v66, 12, v37\n"
      "v_lshl_add_u32 v7, v67, 10, v75\n"
      "v_lshl_add_u32 v75, v66, 9, v37\n"
      "v_lshl_add_u32 v8, v67, 12, v75\n"
      "v_xor_b32 v8, 64, v8\n"
      "v_mov_b32 v15, v1\n"
      "s_bitcmp1_b32 s52, 1\n"
      "s_cselect_b64 s[28:29], s[16:17], s[28:29]\n"
      "v_lshl_or_b32 v75, s60, 1, v66\n"
      "v_lshlrev_b32 v75, 1, v75\n"
      "v_xor_b32 v75, v75, v65\n"
      "v_mul_u32_u24 v76, 3584, v64\n"
      "buffer_load_dwordx4 v40, s[20:23], s61 offen offset:0 lds\n"
      "buffer_load_dwordx4 v41, s[20:23], s61 offen offset:1024 lds\n"
      "buffer_load_dwordx4 v42, s[20:23], s61 offen offset:2048 lds\n"
      "buffer_load_dwordx4 v43, s[20:23], s61 offen offset:3072 lds\n"
      "s_addk_i32 s61, 128\n"
      "s_addk_i32 m0, 0x4000\n"
      "s_cmp_eq_u32 s61, s11\n"
      "s_cselect_b32 s61, s98, s61\n"
      "s_getpc_b64 s[14:15]\n"
      ".LHGK:\n"
      "s_add_u32 s14, s14, .LHBK-.LHGK\n"
      "s_addc_u32 s15, s15, 0\n"
      "s_mov_b64 exec, 0\n"
      "s_cmp_eq_u32 s52, 0\n"
      "s_cbranch_scc1 .LPAD1\n"
      "s_cmp_eq_u32 s52, 1\n"
      "s_cbranch_scc1 .LPAD2\n"
      "s_cmp_eq_u32 s52, 2\n"
      "s_cbranch_scc1 .LPAD5\n"
      "s_cmp_eq_u32 s52, 3\n"
      "s_cbranch_scc1 .LPAD6\n"
      ".LHBK:\n"
      "s_mov_b64 exec, -1\n"
      "v_lshl_add_u32 v76, v75, 4, v76\n"
      "s_lshl1_add_u32 s94, s58, 0\n"
      "s_lshl3_add_u32 s56, s3, s60\n"
      "s_mul_i32 s56, s56, 28672\n"
      "s_add_u32 s56, s56, s94\n"
      "v_add_u32 v4, s56, v76\n"
      "s_mov_b32 s65, 56320\n"
      "s_lshl_b32 s66, s65, 1\n"
      "s_add_u32 s67, s65, s66\n"
      "s_mul_i32 s56, s52, 7168\n"
      "s_add_u32 s56, s56, s58\n"
      "v_lshlrev_b32 v76, 1, v74\n"
      "v_xor_b32 v76, v76, v65\n"
      "v_lshlrev_b32 v76, 4, v76\n"
      "v_mul_u32_u24 v75, 896, v64\n"
      "v_add3_u32 v5, v75, v76, s56\n"
      "v_add3_u32 v75, v0, s55, 1\n"
      "v_add_u32 v76, -7, v75\n"
      "v_min_u32 v75, v75, v76\n"
      "v_cmp_gt_u32 vcc, 6, v0\n"
      "s_waitcnt vmcnt(8)\n"
      "ds_read_b128 a[0:3], v32 offset:0\n"
      "ds_read_b128 a[4:7], v32 offset:2048\n"
      "ds_read_b128 a[8:11], v33 offset:0\n"
      "ds_read_b128 a[12:15], v33 offset:2048\n"
      "buffer_load_dwordx4 v40, s[20:23], s61 offen offset:0 lds\n"
      "buffer_load_dwordx4 v41, s[20:23], s61 offen offset:1024 lds\n"
      "buffer_load_dwordx4 v42, s[20:23], s61 offen offset:2048 lds\n"
      "buffer_load_dwordx4 v43, s[20:23], s61 offen offset:3072 lds\n"
      "s_addk_i32 s61, 128\n"
      "s_addk_i32 m0, 0x4000\n"
      "s_cmp_eq_u32 s61, s11\n"
      "s_cselect_b32 s61, s98, s61\n"
      "v_lshlrev_b32 v75, 3, v75\n"
      "v_add_u32 v2, 4096, v1\n"
      "v_cndmask_b32 v14, v73, v75, vcc\n"
      "s_mov_b32 s56, 229376\n"
      "s_cmp_eq_u32 s3, 6\n"
      "s_cselect_b32 s16, 4293591040, s56\n"
      "s_cmp_eq_u32 s3, 5\n"
      "s_cselect_b32 s17, 4293591040, s56\n"
      "s_cmp_eq_u32 s3, 4\n"
      "s_cselect_b32 s18, 4293591040, s56\n"
      "s_waitcnt vmcnt(8) lgkmcnt(0)\n"
      "ds_read_b128 a[16:19], v32 offset:16384\n"
      "ds_read_b128 a[20:23], v32 offset:18432\n"
      "ds_read_b128 a[24:27], v33 offset:16384\n"
      "ds_read_b128 a[28:31], v33 offset:18432\n"
      "buffer_load_dwordx4 v40, s[20:23], s61 offen offset:0 lds\n"
      "buffer_load_dwordx4 v41, s[20:23], s61 offen offset:1024 lds\n"
      "buffer_load_dwordx4 v42, s[20:23], s61 offen offset:2048 lds\n"
      "buffer_load_dwordx4 v43, s[20:23], s61 offen offset:3072 lds\n"
      "s_addk_i32 s61, 128\n"
      "s_addk_i32 m0, 0x4000\n"
      "s_cmp_eq_u32 s61, s11\n"
      "s_cselect_b32 s61, s98, s61\n"
      "v_mfma_f32_4x4x4_16b_bf16 v[48:51], v[30:31], a[0:1], 0\n"
      "v_mfma_f32_4x4x4_16b_bf16 v[56:59], a[0:1], a[0:1], 0\n"
      "v_mfma_f32_4x4x4_16b_bf16 v[52:55], v[30:31], a[4:5], 0\n"
      "v_mfma_f32_4x4x4_16b_bf16 v[60:63], a[4:5], a[4:5], 0\n"
      "v_mfma_f32_4x4x4_16b_bf16 v[48:51], v[30:31], a[2:3], v[48:51]\n"
      "v_mfma_f32_4x4x4_16b_bf16 v[56:59], a[2:3], a[2:3], v[56:59]\n"
      "v_mfma_f32_4x4x4_16b_bf16 v[52:55], v[30:31], a[6:7], v[52:55]\n"
      "v_mfma_f32_4x4x4_16b_bf16 v[60:63], a[6:7], a[6:7], v[60:63]\n"
      "v_mfma_f32_4x4x4_16b_bf16 v[48:51], v[30:31], a[8:9], v[48:51]\n"
      "v_mfma_f32_4x4x4_16b_bf16 v[56:59], a[8:9], a[8:9], v[56:59]\n"
      "v_mfma_f32_4x4x4_16b_bf16 v[52:55], v[30:31], a[12:13], v[52:55]\n"
      "v_mfma_f32_4x4x4_16b_bf16 v[60:63], a[12:13], a[12:13], v[60:63]\n"
      "v_mfma_f32_4x4x4_16b_bf16 v[48:51], v[30:31], a[10:11], v[48:51]\n"
      "v_mfma_f32_4x4x4_16b_bf16 v[56:59], a[10:11], a[10:11], v[56:59]\n"
      "v_mfma_f32_4x4x4_16b_bf16 v[52:55], v[30:31], a[14:15], v[52:55]\n"
      "v_mfma_f32_4x4x4_16b_bf16 v[60:63], a[14:15], a[14:15], v[60:63]\n"
      "s_cmp_eq_u32 s3, 3\n"
      "s_cselect_b32 s19, 4293591040, s56\n"
      "s_cmp_eq_u32 s3, 2\n"
      "s_cselect_b32 s8, 4293591040, s56\n"
      "s_cmp_eq_u32 s3, 1\n"
      "s_cselect_b32 s9, 4293591040, s56\n"
      "s_waitcnt vmcnt(8) lgkmcnt(0)\n"
      "ds_read_b128 a[32:35], v32 offset:32768\n"
      "ds_read_b128 a[36:39], v32 offset:34816\n"
      "ds_read_b128 a[40:43], v33 offset:32768\n"
      "ds_read_b128 a[44:47], v33 offset:34816\n"
      "buffer_load_dwordx4 v40, s[20:23], s61 offen offset:0 lds\n"
      "buffer_load_dwordx4 v41, s[20:23], s61 offen offset:1024 lds\n"
      "buffer_load_dwordx4 v42, s[20:23], s61 offen offset:2048 lds\n"
      "buffer_load_dwordx4 v43, s[20:23], s61 offen offset:3072 lds\n"
      "s_addk_i32 s61, 128\n"
      "s_addk_i32 m0, 0x4000\n"
      "s_cmp_eq_u32 s61, s11\n"
      "s_cselect_b32 s61, s98, s61\n"
      "v_mfma_f32_4x4x4_16b_bf16 v[48:51], v[30:31], a[16:17], v[48:51]\n"
      "v_mfma_f32_4x4x4_16b_bf16 v[56:59], a[16:17], a[16:17], v[56:59]\n"
      "v_mfma_f32_4x4x4_16b_bf16 v[52:55], v[30:31], a[20:21], v[52:55]\n"
      "v_mfma_f32_4x4x4_16b_bf16 v[60:63], a[20:21], a[20:21], v[60:63]\n"
      "v_mfma_f32_4x4x4_16b_bf16 v[48:51], v[30:31], a[18:19], v[48:51]\n"
      "v_mfma_f32_4x4x4_16b_bf16 v[56:59], a[18:19], a[18:19], v[56:59]\n"
      "v_mfma_f32_4x4x4_16b_bf16 v[52:55], v[30:31], a[22:23], v[52:55]\n"
      "v_mfma_f32_4x4x4_16b_bf16 v[60:63], a[22:23], a[22:23], v[60:63]\n"
      "v_mfma_f32_4x4x4_16b_bf16 v[48:51], v[30:31], a[24:25], v[48:51]\n"
      "v_mfma_f32_4x4x4_16b_bf16 v[56:59], a[24:25], a[24:25], v[56:59]\n"
      "v_mfma_f32_4x4x4_16b_bf16 v[52:55], v[30:31], a[28:29], v[52:55]\n"
      "v_mfma_f32_4x4x4_16b_bf16 v[60:63], a[28:29], a[28:29], v[60:63]\n"
      "v_mfma_f32_4x4x4_16b_bf16 v[48:51], v[30:31], a[26:27], v[48:51]\n"
      "v_mfma_f32_4x4x4_16b_bf16 v[56:59], a[26:27], a[26:27], v[56:59]\n"
      "v_mfma_f32_4x4x4_16b_bf16 v[52:55], v[30:31], a[30:31], v[52:55]\n"
      "v_mfma_f32_4x4x4_16b_bf16 v[60:63], a[30:31], a[30:31], v[60:63]\n"
      "s_cmp_eq_u32 s3, 0\n"
      "s_cselect_b32 s10, 4293591040, s56\n"
      "s_mul_i32 s56, s12, 57344\n"
      "s_lshl_b32 s57, s55, 12\n"
      "s_add_u32 s56, s56, s57\n"
      "s_waitcnt vmcnt(8) lgkmcnt(0)\n"
      "ds_read_b128 a[48:51], v32 offset:49152\n"
      "ds_read_b128 a[52:55], v32 offset:51200\n"
      "ds_read_b128 a[56:59], v33 offset:49152\n"
      "ds_read_b128 a[60:63], v33 offset:51200\n"
      "buffer_load_dwordx4 v40, s[20:23], s61 offen offset:0 lds\n"
      "buffer_load_dwordx4 v41, s[20:23], s61 offen offset:1024 lds\n"
      "buffer_load_dwordx4 v42, s[20:23], s61 offen offset:2048 lds\n"
      "buffer_load_dwordx4 v43, s[20:23], s61 offen offset:3072 lds\n"
      "v_mfma_f32_4x4x4_16b_bf16 v[48:51], v[30:31], a[32:33], v[48:51]\n"
      "v_mfma_f32_4x4x4_16b_bf16 v[56:59], a[32:33], a[32:33], v[56:59]\n"
      "v_mfma_f32_4x4x4_16b_bf16 v[52:55], v[30:31], a[36:37], v[52:55]\n"
      "v_mfma_f32_4x4x4_16b_bf16 v[60:63], a[36:37], a[36:37], v[60:63]\n"
      "v_mfma_f32_4x4x4_16b_bf16 v[48:51], v[30:31], a[34:35], v[48:51]\n"
      "v_mfma_f32_4x4x4_16b_bf16 v[56:59], a[34:35], a[34:35], v[56:59]\n"
      "v_mfma_f32_4x4x4_16b_bf16 v[52:55], v[30:31], a[38:39], v[52:55]\n"
      "v_mfma_f32_4x4x4_16b_bf16 v[60:63], a[38:39], a[38:39], v[60:63]\n"
      "v_mfma_f32_4x4x4_16b_bf16 v[48:51], v[30:31], a[40:41], v[48:51]\n"
      "v_mfma_f32_4x4x4_16b_bf16 v[56:59], a[40:41], a[40:41], v[56:59]\n"
      "v_mfma_f32_4x4x4_16b_bf16 v[52:55], v[30:31], a[44:45], v[52:55]\n"
      "v_mfma_f32_4x4x4_16b_bf16 v[60:63], a[44:45], a[44:45], v[60:63]\n"
      "v_mfma_f32_4x4x4_16b_bf16 v[48:51], v[30:31], a[42:43], v[48:51]\n"
      "v_mfma_f32_4x4x4_16b_bf16 v[56:59], a[42:43], a[42:43], v[56:59]\n"
      "v_mfma_f32_4x4x4_16b_bf16 v[52:55], v[30:31], a[46:47], v[52:55]\n"
      "v_mfma_f32_4x4x4_16b_bf16 v[60:63], a[46:47], a[46:47], v[60:63]\n"
      "s_add_u32 s84, s56, 1048576\n"
      "s_addk_i32 s10, 128\n"
      "s_lshl_b32 s13, s55, 7\n"
      "s_lshl_b32 s88, s12, 11\n"
      "s_waitcnt vmcnt(8) lgkmcnt(0)\n"
      "ds_read_b128 a[64:67], v34 offset:0\n"
      "ds_read_b128 a[68:71], v34 offset:2048\n"
      "ds_read_b128 a[72:75], v35 offset:0\n"
      "ds_read_b128 a[76:79], v35 offset:2048\n"
      "s_add_u32 m0, s95, 114688\n"
      "v_add_u32 v19, s34, v3\n"
      "v_xor_b32 v18, 64, v19\n"
      "buffer_load_dwordx4 v19, s[24:27], 0 offen offset:0 lds\n"
      "buffer_load_dwordx4 v18, s[24:27], s62 offen offset:1024 lds\n"
      "buffer_load_dwordx4 v19, s[24:27], s63 offen offset:2048 lds\n"
      "buffer_load_dwordx4 v18, s[24:27], s64 offen offset:3072 lds\n"
      "v_mfma_f32_4x4x4_16b_bf16 v[48:51], v[30:31], a[48:49], v[48:51]\n"
      "v_mfma_f32_4x4x4_16b_bf16 v[56:59], a[48:49], a[48:49], v[56:59]\n"
      "v_mfma_f32_4x4x4_16b_bf16 v[52:55], v[30:31], a[52:53], v[52:55]\n"
      "v_mfma_f32_4x4x4_16b_bf16 v[60:63], a[52:53], a[52:53], v[60:63]\n"
      "v_mfma_f32_4x4x4_16b_bf16 v[48:51], v[30:31], a[50:51], v[48:51]\n"
      "v_mfma_f32_4x4x4_16b_bf16 v[56:59], a[50:51], a[50:51], v[56:59]\n"
      "v_mfma_f32_4x4x4_16b_bf16 v[52:55], v[30:31], a[54:55], v[52:55]\n"
      "v_mfma_f32_4x4x4_16b_bf16 v[60:63], a[54:55], a[54:55], v[60:63]\n"
      "v_mfma_f32_4x4x4_16b_bf16 v[48:51], v[30:31], a[56:57], v[48:51]\n"
      "v_mfma_f32_4x4x4_16b_bf16 v[56:59], a[56:57], a[56:57], v[56:59]\n"
      "v_mfma_f32_4x4x4_16b_bf16 v[52:55], v[30:31], a[60:61], v[52:55]\n"
      "v_mfma_f32_4x4x4_16b_bf16 v[60:63], a[60:61], a[60:61], v[60:63]\n"
      "v_mfma_f32_4x4x4_16b_bf16 v[48:51], v[30:31], a[58:59], v[48:51]\n"
      "v_mfma_f32_4x4x4_16b_bf16 v[56:59], a[58:59], a[58:59], v[56:59]\n"
      "v_mfma_f32_4x4x4_16b_bf16 v[52:55], v[30:31], a[62:63], v[52:55]\n"
      "v_mfma_f32_4x4x4_16b_bf16 v[60:63], a[62:63], a[62:63], v[60:63]\n"
      "s_waitcnt vmcnt(8) lgkmcnt(0)\n"
      "ds_read_b128 a[80:83], v34 offset:16384\n"
      "ds_read_b128 a[84:87], v34 offset:18432\n"
      "ds_read_b128 a[88:91], v35 offset:16384\n"
      "ds_read_b128 a[92:95], v35 offset:18432\n"
      "s_add_u32 m0, s95, 131072\n"
      "v_add_u32 v19, s86, v3\n"
      "v_xor_b32 v18, 64, v19\n"
      "buffer_load_dwordx4 v19, s[24:27], 0 offen offset:0 lds\n"
      "buffer_load_dwordx4 v18, s[24:27], s62 offen offset:1024 lds\n"
      "buffer_load_dwordx4 v19, s[24:27], s63 offen offset:2048 lds\n"
      "buffer_load_dwordx4 v18, s[24:27], s64 offen offset:3072 lds\n"
      "v_mfma_f32_4x4x4_16b_bf16 v[48:51], v[30:31], a[64:65], v[48:51]\n"
      "v_mfma_f32_4x4x4_16b_bf16 v[56:59], a[64:65], a[64:65], v[56:59]\n"
      "v_mfma_f32_4x4x4_16b_bf16 v[52:55], v[30:31], a[68:69], v[52:55]\n"
      "v_mfma_f32_4x4x4_16b_bf16 v[60:63], a[68:69], a[68:69], v[60:63]\n"
      "v_mfma_f32_4x4x4_16b_bf16 v[48:51], v[30:31], a[66:67], v[48:51]\n"
      "v_mfma_f32_4x4x4_16b_bf16 v[56:59], a[66:67], a[66:67], v[56:59]\n"
      "v_mfma_f32_4x4x4_16b_bf16 v[52:55], v[30:31], a[70:71], v[52:55]\n"
      "v_mfma_f32_4x4x4_16b_bf16 v[60:63], a[70:71], a[70:71], v[60:63]\n"
      "v_mfma_f32_4x4x4_16b_bf16 v[48:51], v[30:31], a[72:73], v[48:51]\n"
      "v_mfma_f32_4x4x4_16b_bf16 v[56:59], a[72:73], a[72:73], v[56:59]\n"
      "v_mfma_f32_4x4x4_16b_bf16 v[52:55], v[30:31], a[76:77], v[52:55]\n"
      "v_mfma_f32_4x4x4_16b_bf16 v[60:63], a[76:77], a[76:77], v[60:63]\n"
      "v_mfma_f32_4x4x4_16b_bf16 v[48:51], v[30:31], a[74:75], v[48:51]\n"
      "v_mfma_f32_4x4x4_16b_bf16 v[56:59], a[74:75], a[74:75], v[56:59]\n"
      "v_mfma_f32_4x4x4_16b_bf16 v[52:55], v[30:31], a[78:79], v[52:55]\n"
      "v_mfma_f32_4x4x4_16b_bf16 v[60:63], a[78:79], a[78:79], v[60:63]\n"
      "s_waitcnt vmcnt(8) lgkmcnt(0)\n"
      "ds_read_b128 a[96:99], v34 offset:32768\n"
      "ds_read_b128 a[100:103], v34 offset:34816\n"
      "ds_read_b128 a[104:107], v35 offset:32768\n"
      "ds_read_b128 a[108:111], v35 offset:34816\n"
      "ds_read_b64 a[208:209], v44 offset:0\n"
      "ds_read_b64 a[212:213], v44 offset:2048\n"
      "ds_read_b64 a[210:211], v46 offset:0\n"
      "ds_read_b64 a[214:215], v46 offset:2048\n"
      "v_mfma_f32_4x4x4_16b_bf16 v[48:51], v[30:31], a[80:81], v[48:51]\n"
      "v_mfma_f32_4x4x4_16b_bf16 v[56:59], a[80:81], a[80:81], v[56:59]\n"
      "v_mfma_f32_4x4x4_16b_bf16 v[52:55], v[30:31], a[84:85], v[52:55]\n"
      "v_mfma_f32_4x4x4_16b_bf16 v[60:63], a[84:85], a[84:85], v[60:63]\n"
      "v_mfma_f32_4x4x4_16b_bf16 v[48:51], v[30:31], a[82:83], v[48:51]\n"
      "v_mfma_f32_4x4x4_16b_bf16 v[56:59], a[82:83], a[82:83], v[56:59]\n"
      "v_mfma_f32_4x4x4_16b_bf16 v[52:55], v[30:31], a[86:87], v[52:55]\n"
      "v_mfma_f32_4x4x4_16b_bf16 v[60:63], a[86:87], a[86:87], v[60:63]\n"
      "v_mfma_f32_4x4x4_16b_bf16 v[48:51], v[30:31], a[88:89], v[48:51]\n"
      "v_mfma_f32_4x4x4_16b_bf16 v[56:59], a[88:89], a[88:89], v[56:59]\n"
      "v_mfma_f32_4x4x4_16b_bf16 v[52:55], v[30:31], a[92:93], v[52:55]\n"
      "v_mfma_f32_4x4x4_16b_bf16 v[60:63], a[92:93], a[92:93], v[60:63]\n"
      "v_mfma_f32_4x4x4_16b_bf16 v[48:51], v[30:31], a[90:91], v[48:51]\n"
      "v_mfma_f32_4x4x4_16b_bf16 v[56:59], a[90:91], a[90:91], v[56:59]\n"
      "v_mfma_f32_4x4x4_16b_bf16 v[52:55], v[30:31], a[94:95], v[52:55]\n"
      "v_mfma_f32_4x4x4_16b_bf16 v[60:63], a[94:95], a[94:95], v[60:63]\n"
      "s_waitcnt lgkmcnt(4)\n"
      "v_mfma_f32_4x4x4_16b_bf16 v[48:51], v[30:31], a[96:97], v[48:51]\n"
      "v_mfma_f32_4x4x4_16b_bf16 v[56:59], a[96:97], a[96:97], v[56:59]\n"
      "v_mfma_f32_4x4x4_16b_bf16 v[52:55], v[30:31], a[100:101], v[52:55]\n"
      "v_mfma_f32_4x4x4_16b_bf16 v[60:63], a[100:101], a[100:101], v[60:63]\n"
      "v_mfma_f32_4x4x4_16b_bf16 v[48:51], v[30:31], a[98:99], v[48:51]\n"
      "v_mfma_f32_4x4x4_16b_bf16 v[56:59], a[98:99], a[98:99], v[56:59]\n"
      "v_mfma_f32_4x4x4_16b_bf16 v[52:55], v[30:31], a[102:103], v[52:55]\n"
      "v_mfma_f32_4x4x4_16b_bf16 v[60:63], a[102:103], a[102:103], v[60:63]\n"
      "v_mfma_f32_4x4x4_16b_bf16 v[48:51], v[30:31], a[104:105], v[48:51]\n"
      "v_mfma_f32_4x4x4_16b_bf16 v[56:59], a[104:105], a[104:105], v[56:59]\n"
      "v_mfma_f32_4x4x4_16b_bf16 v[52:55], v[30:31], a[108:109], v[52:55]\n"
      "v_mfma_f32_4x4x4_16b_bf16 v[60:63], a[108:109], a[108:109], v[60:63]\n"
      "v_mfma_f32_4x4x4_16b_bf16 v[48:51], v[30:31], a[106:107], v[48:51]\n"
      "v_mfma_f32_4x4x4_16b_bf16 v[56:59], a[106:107], a[106:107], v[56:59]\n"
      "v_mfma_f32_4x4x4_16b_bf16 v[52:55], v[30:31], a[110:111], v[52:55]\n"
      "v_mfma_f32_4x4x4_16b_bf16 v[60:63], a[110:111], a[110:111], v[60:63]\n"
      "s_nop 3\n"
      "v_permlane16_swap_b32 v48, v52\n"
      "v_add_f32 v32, v48, v52\n"
      "v_add_f32 v33, v48, v52\n"
      "v_xor_b32 v9, 0x18000, v6\n"
      "v_xor_b32 v10, 0x18020, v6\n"
      "v_permlane32_swap_b32 v32, v33\n"
      "v_add_f32 v32, v32, v33\n"
      "v_mul_f32 v34, 0xbb124925, v32\n"
      "v_fmamk_f32 v32, v34, 0x43e00000, v32\n"
      "v_fmamk_f32 v34, v32, 0xbb124925, v34\n"
      "v_cvt_pk_bf16_f32 v24, 0, v34\n"
      "v_cvt_pk_bf16_f32 v40, 0, v34\n"
      "v_xor_b32 v11, 0x18040, v6\n"
      "v_xor_b32 v12, 0x18060, v6\n"
      "v_permlane16_swap_b32 v24, v40\n"
      "v_mov_b32 v25, v24\n"
      "v_mov_b32 v26, v24\n"
      "v_mov_b32 v27, v24\n"
      "v_mov_b32 v41, v40\n"
      "v_mov_b32 v42, v40\n"
      "v_mov_b32 v43, v40\n"
      "v_mfma_f32_4x4x4_16b_bf16 v[112:115], v[28:29], a[0:1], v[24:27]\n"
      "v_mfma_f32_4x4x4_16b_bf16 v[116:119], v[28:29], a[2:3], v[24:27]\n"
      "v_mfma_f32_4x4x4_16b_bf16 v[120:123], v[28:29], a[4:5], v[40:43]\n"
      "v_mfma_f32_4x4x4_16b_bf16 v[124:127], v[28:29], a[6:7], v[40:43]\n"
      "v_mfma_f32_4x4x4_16b_bf16 v[128:131], v[28:29], a[8:9], v[24:27]\n"
      "v_mfma_f32_4x4x4_16b_bf16 v[132:135], v[28:29], a[10:11], v[24:27]\n"
      "v_mfma_f32_4x4x4_16b_bf16 v[136:139], v[28:29], a[12:13], v[40:43]\n"
      "v_mfma_f32_4x4x4_16b_bf16 v[140:143], v[28:29], a[14:15], v[40:43]\n"
      "v_cvt_pk_bf16_f32 v144, v112, v113\n"
      "v_cvt_pk_bf16_f32 v145, v114, v115\n"
      "v_cvt_pk_bf16_f32 v146, v116, v117\n"
      "v_cvt_pk_bf16_f32 v147, v118, v119\n"
      "v_cvt_pk_bf16_f32 v148, v120, v121\n"
      "v_cvt_pk_bf16_f32 v149, v122, v123\n"
      "v_cvt_pk_bf16_f32 v150, v124, v125\n"
      "v_cvt_pk_bf16_f32 v151, v126, v127\n"
      "v_mfma_f32_4x4x4_16b_bf16 v[112:115], v[28:29], a[16:17], v[24:27]\n"
      "v_mfma_f32_4x4x4_16b_bf16 v[116:119], v[28:29], a[18:19], v[24:27]\n"
      "v_mfma_f32_4x4x4_16b_bf16 v[120:123], v[28:29], a[20:21], v[40:43]\n"
      "v_mfma_f32_4x4x4_16b_bf16 v[124:127], v[28:29], a[22:23], v[40:43]\n"
      "v_cvt_pk_bf16_f32 v152, v128, v129\n"
      "v_cvt_pk_bf16_f32 v153, v130, v131\n"
      "v_cvt_pk_bf16_f32 v154, v132, v133\n"
      "v_cvt_pk_bf16_f32 v155, v134, v135\n"
      "v_cvt_pk_bf16_f32 v156, v136, v137\n"
      "v_cvt_pk_bf16_f32 v157, v138, v139\n"
      "v_cvt_pk_bf16_f32 v158, v140, v141\n"
      "v_cvt_pk_bf16_f32 v159, v142, v143\n"
      "v_mfma_f32_4x4x4_16b_bf16 v[128:131], v[28:29], a[24:25], v[24:27]\n"
      "v_mfma_f32_4x4x4_16b_bf16 v[132:135], v[28:29], a[26:27], v[24:27]\n"
      "v_mfma_f32_4x4x4_16b_bf16 v[136:139], v[28:29], a[28:29], v[40:43]\n"
      "v_mfma_f32_4x4x4_16b_bf16 v[140:143], v[28:29], a[30:31], v[40:43]\n"
      "s_waitcnt vmcnt(4) lgkmcnt(0)\n"
      "s_barrier\n"
      "ds_read_b64_tr_b16 a[216:217], v9 offset:16384\n"
      "ds_read_b64_tr_b16 a[218:219], v9 offset:16896\n"
      "ds_read_b64_tr_b16 a[220:221], v10 offset:16384\n"
      "ds_read_b64_tr_b16 a[222:223], v10 offset:16896\n"
      "ds_read_b64_tr_b16 a[224:225], v11 offset:16384\n"
      "ds_read_b64_tr_b16 a[226:227], v11 offset:16896\n"
      "ds_read_b64_tr_b16 a[228:229], v12 offset:16384\n"
      "ds_read_b64_tr_b16 a[230:231], v12 offset:16896\n"
      "ds_read_b64_tr_b16 a[232:233], v9 offset:20480\n"
      "ds_read_b64_tr_b16 a[234:235], v9 offset:20992\n"
      "ds_read_b64_tr_b16 a[236:237], v10 offset:20480\n"
      "ds_read_b64_tr_b16 a[238:239], v10 offset:20992\n"
      "ds_read_b64_tr_b16 a[240:241], v11 offset:20480\n"
      "ds_read_b64_tr_b16 a[242:243], v11 offset:20992\n"
      "s_waitcnt lgkmcnt(6)\n"
      "v_mfma_f32_16x16x32_bf16 v[80:83], a[216:219], v[144:147], 0\n"
      "ds_read_b64_tr_b16 a[244:245], v12 offset:20480\n"
      "ds_read_b64_tr_b16 a[246:247], v12 offset:20992\n"
      "v_mfma_f32_16x16x32_bf16 v[84:87], a[216:219], v[148:151], 0\n"
      "s_add_u32 m0, s95, 147456\n"
      "v_add_u32 v19, s87, v3\n"
      "v_xor_b32 v18, 64, v19\n"
      "buffer_load_dwordx4 v19, s[24:27], 0 offen offset:0 lds\n"
      "v_cvt_pk_bf16_f32 v160, v112, v113\n"
      "v_cvt_pk_bf16_f32 v161, v114, v115\n"
      "v_mfma_f32_16x16x32_bf16 v[88:91], a[220:223], v[144:147], 0\n"
      "ds_read_b64_tr_b16 a[248:249], v9 offset:24576\n"
      "ds_read_b64_tr_b16 a[250:251], v9 offset:25088\n"
      "v_mfma_f32_16x16x32_bf16 v[92:95], a[220:223], v[148:151], 0\n"
      "v_cvt_pk_bf16_f32 v162, v116, v117\n"
      "v_cvt_pk_bf16_f32 v163, v118, v119\n"
      "v_bfe_i32 v35, v0, 0, 1\n"
      "v_bfe_i32 v36, v0, 1, 1\n"
      "v_mfma_f32_16x16x32_bf16 v[96:99], a[224:227], v[144:147], 0\n"
      "ds_read_b64_tr_b16 a[252:253], v10 offset:24576\n"
      "ds_read_b64_tr_b16 a[254:255], v10 offset:25088\n"
      "v_mfma_f32_16x16x32_bf16 v[100:103], a[224:227], v[148:151], 0\n"
      "buffer_load_dwordx4 v18, s[24:27], s62 offen offset:1024 lds\n"
      "v_cvt_pk_bf16_f32 v164, v120, v121\n"
      "v_cvt_pk_bf16_f32 v165, v122, v123\n"
      "v_bfi_b32 v56, v35, v57, v56\n"
      "v_mfma_f32_16x16x32_bf16 v[104:107], a[228:231], v[144:147], 0\n"
      "ds_read_b64_tr_b16 a[216:217], v11 offset:24576\n"
      "ds_read_b64_tr_b16 a[218:219], v11 offset:25088\n"
      "v_mfma_f32_16x16x32_bf16 v[108:111], a[228:231], v[148:151], 0\n"
      "v_cvt_pk_bf16_f32 v166, v124, v125\n"
      "v_cvt_pk_bf16_f32 v167, v126, v127\n"
      "v_bfi_b32 v58, v35, v59, v58\n"
      "v_bfi_b32 v60, v35, v61, v60\n"
      "s_waitcnt lgkmcnt(6)\n"
      "v_mfma_f32_16x16x32_bf16 v[80:83], a[232:235], v[152:155], v[80:83]\n"
      "ds_read_b64_tr_b16 a[220:221], v12 offset:24576\n"
      "ds_read_b64_tr_b16 a[222:223], v12 offset:25088\n"
      "v_mfma_f32_16x16x32_bf16 v[84:87], a[232:235], v[156:159], v[84:87]\n"
      "buffer_load_dwordx4 v19, s[24:27], s63 offen offset:2048 lds\n"
      "v_cvt_pk_bf16_f32 v168, v128, v129\n"
      "v_cvt_pk_bf16_f32 v169, v130, v131\n"
      "v_mfma_f32_16x16x32_bf16 v[88:91], a[236:239], v[152:155], v[88:91]\n"
      "ds_read_b64_tr_b16 a[224:225], v9 offset:28672\n"
      "ds_read_b64_tr_b16 a[226:227], v9 offset:29184\n"
      "v_mfma_f32_16x16x32_bf16 v[92:95], a[236:239], v[156:159], v[92:95]\n"
      "v_cvt_pk_bf16_f32 v170, v132, v133\n"
      "v_cvt_pk_bf16_f32 v171, v134, v135\n"
      "v_bfi_b32 v62, v35, v63, v62\n"
      "v_bfi_b32 v56, v36, v58, v56\n"
      "v_bfi_b32 v60, v36, v62, v60\n"
      "v_mfma_f32_16x16x32_bf16 v[96:99], a[240:243], v[152:155], v[96:99]\n"
      "ds_read_b64_tr_b16 a[228:229], v10 offset:28672\n"
      "ds_read_b64_tr_b16 a[230:231], v10 offset:29184\n"
      "v_mfma_f32_16x16x32_bf16 v[100:103], a[240:243], v[156:159], v[100:103]\n"
      "buffer_load_dwordx4 v18, s[24:27], s64 offen offset:3072 lds\n"
      "v_cvt_pk_bf16_f32 v172, v136, v137\n"
      "v_cvt_pk_bf16_f32 v173, v138, v139\n"
      "v_mfma_f32_16x16x32_bf16 v[104:107], a[244:247], v[152:155], v[104:107]\n"
      "ds_read_b64_tr_b16 a[232:233], v11 offset:28672\n"
      "ds_read_b64_tr_b16 a[234:235], v11 offset:29184\n"
      "v_mfma_f32_16x16x32_bf16 v[108:111], a[244:247], v[156:159], v[108:111]\n"
      "v_cvt_pk_bf16_f32 v174, v140, v141\n"
      "v_cvt_pk_bf16_f32 v175, v142, v143\n"
      "v_mul_f32 v34, v34, -v34\n"
      "v_permlane16_swap_b32 v56, v60\n"
      "v_mfma_f32_4x4x4_16b_bf16 v[112:115], v[28:29], a[32:33], v[24:27]\n"
      "v_mfma_f32_4x4x4_16b_bf16 v[116:119], v[28:29], a[34:35], v[24:27]\n"
      "v_mfma_f32_4x4x4_16b_bf16 v[120:123], v[28:29], a[36:37], v[40:43]\n"
      "v_mfma_f32_4x4x4_16b_bf16 v[124:127], v[28:29], a[38:39], v[40:43]\n"
      "s_waitcnt vmcnt(4) lgkmcnt(6)\n"
      "s_barrier\n"
      "v_mfma_f32_16x16x32_bf16 v[80:83], a[248:251], v[160:163], v[80:83]\n"
      "ds_read_b64_tr_b16 a[236:237], v12 offset:28672\n"
      "ds_read_b64_tr_b16 a[238:239], v12 offset:29184\n"
      "v_mfma_f32_16x16x32_bf16 v[84:87], a[248:251], v[164:167], v[84:87]\n"
      "s_lshr_b32 s35, s95, 1\n"
      "s_add_u32 m0, s35, 98304\n"
      "v_add_u32 v19, s99, v3\n"
      "v_xor_b32 v18, 64, v19\n"
      "buffer_load_dwordx4 v19, s[24:27], 0 offen offset:0 lds\n"
      "v_cvt_pk_bf16_f32 v176, v112, v113\n"
      "v_cvt_pk_bf16_f32 v177, v114, v115\n"
      "v_mfma_f32_16x16x32_bf16 v[88:91], a[252:255], v[160:163], v[88:91]\n"
      "ds_read_b64_tr_b16 a[240:241], v9 offset:32768\n"
      "ds_read_b64_tr_b16 a[242:243], v9 offset:33280\n"
      "v_mfma_f32_16x16x32_bf16 v[92:95], a[252:255], v[164:167], v[92:95]\n"
      "v_cvt_pk_bf16_f32 v178, v116, v117\n"
      "v_cvt_pk_bf16_f32 v179, v118, v119\n"
      "v_add_f32 v35, v56, v60\n"
      "v_add_f32 v36, v56, v60\n"
      "s_nop 1\n"
      "v_permlane32_swap_b32 v35, v36\n"
      "v_mfma_f32_16x16x32_bf16 v[96:99], a[216:219], v[160:163], v[96:99]\n"
      "ds_read_b64_tr_b16 a[244:245], v10 offset:32768\n"
      "ds_read_b64_tr_b16 a[246:247], v10 offset:33280\n"
      "v_mfma_f32_16x16x32_bf16 v[100:103], a[216:219], v[164:167], v[100:103]\n"
      "buffer_load_dwordx4 v18, s[24:27], s62 offen offset:1024 lds\n"
      "v_cvt_pk_bf16_f32 v180, v120, v121\n"
      "v_cvt_pk_bf16_f32 v181, v122, v123\n"
      "v_mfma_f32_16x16x32_bf16 v[104:107], a[220:223], v[160:163], v[104:107]\n"
      "ds_read_b64_tr_b16 a[248:249], v11 offset:32768\n"
      "ds_read_b64_tr_b16 a[250:251], v11 offset:33280\n"
      "v_mfma_f32_16x16x32_bf16 v[108:111], a[220:223], v[164:167], v[108:111]\n"
      "v_cvt_pk_bf16_f32 v182, v124, v125\n"
      "v_cvt_pk_bf16_f32 v183, v126, v127\n"
      "v_add_f32 v35, v35, v36\n"
      "v_fmamk_f32 v35, v35, 0x3b124925, v34\n"
      "v_max_f32 v35, 0, v35\n"
      "v_mfma_f32_4x4x4_16b_bf16 v[128:131], v[28:29], a[40:41], v[24:27]\n"
      "v_mfma_f32_4x4x4_16b_bf16 v[132:135], v[28:29], a[42:43], v[24:27]\n"
      "v_mfma_f32_4x4x4_16b_bf16 v[136:139], v[28:29], a[44:45], v[40:43]\n"
      "v_mfma_f32_4x4x4_16b_bf16 v[140:143], v[28:29], a[46:47], v[40:43]\n"
      "s_waitcnt lgkmcnt(6)\n"
      "v_mfma_f32_16x16x32_bf16 v[80:83], a[224:227], v[168:171], v[80:83]\n"
      "ds_read_b64_tr_b16 a[252:253], v12 offset:32768\n"
      "ds_read_b64_tr_b16 a[254:255], v12 offset:33280\n"
      "v_mfma_f32_16x16x32_bf16 v[84:87], a[224:227], v[172:175], v[84:87]\n"
      "s_add_u32 m0, s95, 0\n"
      "s_nop 0\n"
      "buffer_load_dwordx4 v4, s[28:31], 0 offen offset:0 lds\n"
      "v_cvt_pk_bf16_f32 v184, v128, v129\n"
      "v_cvt_pk_bf16_f32 v185, v130, v131\n"
      "v_mfma_f32_16x16x32_bf16 v[88:91], a[228:231], v[168:171], v[88:91]\n"
      "ds_read_b64_tr_b16 a[216:217], v9 offset:36864\n"
      "ds_read_b64_tr_b16 a[218:219], v9 offset:37376\n"
      "v_mfma_f32_16x16x32_bf16 v[92:95], a[228:231], v[172:175], v[92:95]\n"
      "v_cvt_pk_bf16_f32 v186, v132, v133\n"
      "v_cvt_pk_bf16_f32 v187, v134, v135\n"
      ".LPAD1:\n"
      "s_cbranch_execz .LPAD3\n"
      "v_cvt_pk_bf16_f32 v35, 0, v35\n"
      "v_add_f32 v35, 0x3727c5ac, v35\n"
      "v_mfma_f32_16x16x32_bf16 v[96:99], a[232:235], v[168:171], v[96:99]\n"
      "ds_read_b64_tr_b16 a[220:221], v10 offset:36864\n"
      "ds_read_b64_tr_b16 a[222:223], v10 offset:37376\n"
      "v_mfma_f32_16x16x32_bf16 v[100:103], a[232:235], v[172:175], v[100:103]\n"
      "buffer_load_dwordx4 v4, s[28:31], s65 offen offset:1024 lds\n"
      "v_cvt_pk_bf16_f32 v188, v136, v137\n"
      "v_cvt_pk_bf16_f32 v189, v138, v139\n"
      "v_cvt_pk_bf16_f32 v35, 0, v35\n"
      "v_mfma_f32_16x16x32_bf16 v[104:107], a[236:239], v[168:171], v[104:107]\n"
      "ds_read_b64_tr_b16 a[224:225], v11 offset:36864\n"
      "ds_read_b64_tr_b16 a[226:227], v11 offset:37376\n"
      "v_mfma_f32_16x16x32_bf16 v[108:111], a[236:239], v[172:175], v[108:111]\n"
      "v_cvt_pk_bf16_f32 v190, v140, v141\n"
      "v_cvt_pk_bf16_f32 v191, v142, v143\n"
      "v_sqrt_f32 v35, v35\n"
      "v_mfma_f32_4x4x4_16b_bf16 v[112:115], v[28:29], a[48:49], v[24:27]\n"
      "v_mfma_f32_4x4x4_16b_bf16 v[116:119], v[28:29], a[50:51], v[24:27]\n"
      "v_mfma_f32_4x4x4_16b_bf16 v[120:123], v[28:29], a[52:53], v[40:43]\n"
      "v_mfma_f32_4x4x4_16b_bf16 v[124:127], v[28:29], a[54:55], v[40:43]\n"
      "s_waitcnt lgkmcnt(6)\n"
      "v_mfma_f32_16x16x32_bf16 v[80:83], a[240:243], v[176:179], v[80:83]\n"
      "ds_read_b64_tr_b16 a[228:229], v12 offset:36864\n"
      "ds_read_b64_tr_b16 a[230:231], v12 offset:37376\n"
      "v_mfma_f32_16x16x32_bf16 v[84:87], a[240:243], v[180:183], v[84:87]\n"
      "buffer_load_dwordx4 v4, s[28:31], s66 offen offset:2048 lds\n"
      "v_cvt_pk_bf16_f32 v192, v112, v113\n"
      "v_cvt_pk_bf16_f32 v193, v114, v115\n"
      "v_mfma_f32_16x16x32_bf16 v[88:91], a[244:247], v[176:179], v[88:91]\n"
      "ds_read_b64_tr_b16 a[232:233], v9 offset:40960\n"
      "ds_read_b64_tr_b16 a[234:235], v9 offset:41472\n"
      "v_mfma_f32_16x16x32_bf16 v[92:95], a[244:247], v[180:183], v[92:95]\n"
      "v_cvt_pk_bf16_f32 v194, v116, v117\n"
      "v_cvt_pk_bf16_f32 v195, v118, v119\n"
      "v_cvt_pk_bf16_f32 v35, 0, v35\n"
      "v_rcp_f32 v23, v35\n"
      "v_mfma_f32_16x16x32_bf16 v[96:99], a[248:251], v[176:179], v[96:99]\n"
      "ds_read_b64_tr_b16 a[236:237], v10 offset:40960\n"
      "ds_read_b64_tr_b16 a[238:239], v10 offset:41472\n"
      "v_mfma_f32_16x16x32_bf16 v[100:103], a[248:251], v[180:183], v[100:103]\n"
      "buffer_load_dwordx4 v4, s[28:31], s67 offen offset:3072 lds\n"
      "v_cvt_pk_bf16_f32 v196, v120, v121\n"
      "v_cvt_pk_bf16_f32 v197, v122, v123\n"
      "v_mfma_f32_16x16x32_bf16 v[104:107], a[252:255], v[176:179], v[104:107]\n"
      "ds_read_b64_tr_b16 a[240:241], v11 offset:40960\n"
      "ds_read_b64_tr_b16 a[242:243], v11 offset:41472\n"
      "v_mfma_f32_16x16x32_bf16 v[108:111], a[252:255], v[180:183], v[108:111]\n"
      "v_cvt_pk_bf16_f32 v198, v124, v125\n"
      "v_cvt_pk_bf16_f32 v199, v126, v127\n"
      "v_mul_f32 v22, v35, v35\n"
      "v_mul_f32 v20, 0xc0135761, v23\n"
      "v_mfma_f32_4x4x4_16b_bf16 v[128:131], v[28:29], a[56:57], v[24:27]\n"
      "v_mfma_f32_4x4x4_16b_bf16 v[132:135], v[28:29], a[58:59], v[24:27]\n"
      "v_mfma_f32_4x4x4_16b_bf16 v[136:139], v[28:29], a[60:61], v[40:43]\n"
      "v_mfma_f32_4x4x4_16b_bf16 v[140:143], v[28:29], a[62:63], v[40:43]\n"
      "s_waitcnt lgkmcnt(6)\n"
      "v_mfma_f32_16x16x32_bf16 v[80:83], a[216:219], v[184:187], v[80:83]\n"
      "ds_read_b64_tr_b16 a[244:245], v12 offset:40960\n"
      "ds_read_b64_tr_b16 a[246:247], v12 offset:41472\n"
      "v_mfma_f32_16x16x32_bf16 v[84:87], a[216:219], v[188:191], v[84:87]\n"
      "s_add_u32 m0, s95, 16384\n"
      "v_add_u32 v4, s16, v4\n"
      "buffer_load_dwordx4 v4, s[28:31], 0 offen offset:0 lds\n"
      "v_cvt_pk_bf16_f32 v200, v128, v129\n"
      "v_cvt_pk_bf16_f32 v201, v130, v131\n"
      "v_mfma_f32_16x16x32_bf16 v[88:91], a[220:223], v[184:187], v[88:91]\n"
      "ds_read_b64_tr_b16 a[248:249], v9 offset:45056\n"
      "ds_read_b64_tr_b16 a[250:251], v9 offset:45568\n"
      "v_mfma_f32_16x16x32_bf16 v[92:95], a[220:223], v[188:191], v[92:95]\n"
      "v_cvt_pk_bf16_f32 v202, v132, v133\n"
      "v_cvt_pk_bf16_f32 v203, v134, v135\n"
      "v_mul_f32 v36, v23, v23\n"
      "v_mul_f32 v36, 0x3d372713, v36\n"
      "v_mul_f32 v21, v20, v36\n"
      "v_mfma_f32_16x16x32_bf16 v[96:99], a[224:227], v[184:187], v[96:99]\n"
      "ds_read_b64_tr_b16 a[252:253], v10 offset:45056\n"
      "ds_read_b64_tr_b16 a[254:255], v10 offset:45568\n"
      "v_mfma_f32_16x16x32_bf16 v[100:103], a[224:227], v[188:191], v[100:103]\n"
      "buffer_load_dwordx4 v4, s[28:31], s65 offen offset:1024 lds\n"
      "v_cvt_pk_bf16_f32 v204, v136, v137\n"
      "v_cvt_pk_bf16_f32 v205, v138, v139\n"
      "v_mfma_f32_16x16x32_bf16 v[104:107], a[228:231], v[184:187], v[104:107]\n"
      "ds_read_b64_tr_b16 a[216:217], v11 offset:45056\n"
      "ds_read_b64_tr_b16 a[218:219], v11 offset:45568\n"
      "v_mfma_f32_16x16x32_bf16 v[108:111], a[228:231], v[188:191], v[108:111]\n"
      "v_cvt_pk_bf16_f32 v206, v140, v141\n"
      "v_cvt_pk_bf16_f32 v207, v142, v143\n"
      "v_mfma_f32_4x4x4_16b_bf16 v[112:115], v[28:29], a[64:65], v[24:27]\n"
      "v_mfma_f32_4x4x4_16b_bf16 v[116:119], v[28:29], a[66:67], v[24:27]\n"
      "v_mfma_f32_4x4x4_16b_bf16 v[120:123], v[28:29], a[68:69], v[40:43]\n"
      "v_mfma_f32_4x4x4_16b_bf16 v[124:127], v[28:29], a[70:71], v[40:43]\n"
      "s_waitcnt vmcnt(6) lgkmcnt(6)\n"
      "s_barrier\n"
      "v_mfma_f32_16x16x32_bf16 v[80:83], a[232:235], v[192:195], v[80:83]\n"
      "ds_read_b64_tr_b16 a[220:221], v12 offset:45056\n"
      "ds_read_b64_tr_b16 a[222:223], v12 offset:45568\n"
      "v_mfma_f32_16x16x32_bf16 v[84:87], a[232:235], v[196:199], v[84:87]\n"
      "buffer_load_dwordx4 v4, s[28:31], s66 offen offset:2048 lds\n"
      "v_cvt_pk_bf16_f32 v208, v112, v113\n"
      "v_cvt_pk_bf16_f32 v209, v114, v115\n"
      "v_mfma_f32_16x16x32_bf16 v[88:91], a[236:239], v[192:195], v[88:91]\n"
      "ds_read_b64_tr_b16 a[224:225], v9 offset:49152\n"
      "ds_read_b64_tr_b16 a[226:227], v9 offset:49664\n"
      "v_mfma_f32_16x16x32_bf16 v[92:95], a[236:239], v[196:199], v[92:95]\n"
      "v_cvt_pk_bf16_f32 v210, v116, v117\n"
      "v_cvt_pk_bf16_f32 v211, v118, v119\n"
      "v_mfma_f32_16x16x32_bf16 v[96:99], a[240:243], v[192:195], v[96:99]\n"
      "ds_read_b64_tr_b16 a[228:229], v10 offset:49152\n"
      "ds_read_b64_tr_b16 a[230:231], v10 offset:49664\n"
      "v_mfma_f32_16x16x32_bf16 v[100:103], a[240:243], v[196:199], v[100:103]\n"
      "buffer_load_dwordx4 v4, s[28:31], s67 offen offset:3072 lds\n"
      "v_cvt_pk_bf16_f32 v212, v120, v121\n"
      "v_cvt_pk_bf16_f32 v213, v122, v123\n"
      "v_mfma_f32_16x16x32_bf16 v[104:107], a[244:247], v[192:195], v[104:107]\n"
      "ds_read_b64_tr_b16 a[232:233], v11 offset:49152\n"
      "ds_read_b64_tr_b16 a[234:235], v11 offset:49664\n"
      "v_mfma_f32_16x16x32_bf16 v[108:111], a[244:247], v[196:199], v[108:111]\n"
      "v_cvt_pk_bf16_f32 v214, v124, v125\n"
      "v_cvt_pk_bf16_f32 v215, v126, v127\n"
      "v_mfma_f32_4x4x4_16b_bf16 v[128:131], v[28:29], a[72:73], v[24:27]\n"
      "v_mfma_f32_4x4x4_16b_bf16 v[132:135], v[28:29], a[74:75], v[24:27]\n"
      "v_mfma_f32_4x4x4_16b_bf16 v[136:139], v[28:29], a[76:77], v[40:43]\n"
      "v_mfma_f32_4x4x4_16b_bf16 v[140:143], v[28:29], a[78:79], v[40:43]\n"
      "s_waitcnt lgkmcnt(6)\n"
      "v_mfma_f32_16x16x32_bf16 v[80:83], a[248:251], v[200:203], v[80:83]\n"
      "ds_read_b64_tr_b16 a[236:237], v12 offset:49152\n"
      "ds_read_b64_tr_b16 a[238:239], v12 offset:49664\n"
      "v_mfma_f32_16x16x32_bf16 v[84:87], a[248:251], v[204:207], v[84:87]\n"
      "s_add_u32 m0, s95, 32768\n"
      "v_add_u32 v4, s17, v4\n"
      "buffer_load_dwordx4 v4, s[28:31], 0 offen offset:0 lds\n"
      "v_cvt_pk_bf16_f32 v216, v128, v129\n"
      "v_cvt_pk_bf16_f32 v217, v130, v131\n"
      "v_mfma_f32_16x16x32_bf16 v[88:91], a[252:255], v[200:203], v[88:91]\n"
      "ds_read_b64_tr_b16 a[240:241], v9 offset:53248\n"
      "ds_read_b64_tr_b16 a[242:243], v9 offset:53760\n"
      "v_mfma_f32_16x16x32_bf16 v[92:95], a[252:255], v[204:207], v[92:95]\n"
      "v_cvt_pk_bf16_f32 v218, v132, v133\n"
      "v_cvt_pk_bf16_f32 v219, v134, v135\n"
      "v_mfma_f32_16x16x32_bf16 v[96:99], a[216:219], v[200:203], v[96:99]\n"
      "ds_read_b64_tr_b16 a[244:245], v10 offset:53248\n"
      "ds_read_b64_tr_b16 a[246:247], v10 offset:53760\n"
      "v_mfma_f32_16x16x32_bf16 v[100:103], a[216:219], v[204:207], v[100:103]\n"
      "v_cvt_pk_bf16_f32 v220, v136, v137\n"
      "v_cvt_pk_bf16_f32 v221, v138, v139\n"
      "v_mfma_f32_16x16x32_bf16 v[104:107], a[220:223], v[200:203], v[104:107]\n"
      "ds_read_b64_tr_b16 a[248:249], v11 offset:53248\n"
      "ds_read_b64_tr_b16 a[250:251], v11 offset:53760\n"
      "v_mfma_f32_16x16x32_bf16 v[108:111], a[220:223], v[204:207], v[108:111]\n"
      "buffer_load_dwordx4 v4, s[28:31], s65 offen offset:1024 lds\n"
      "v_cvt_pk_bf16_f32 v222, v140, v141\n"
      "v_cvt_pk_bf16_f32 v223, v142, v143\n"
      "v_mfma_f32_4x4x4_16b_bf16 v[112:115], v[28:29], a[80:81], v[24:27]\n"
      "v_mfma_f32_4x4x4_16b_bf16 v[116:119], v[28:29], a[82:83], v[24:27]\n"
      "v_mfma_f32_4x4x4_16b_bf16 v[120:123], v[28:29], a[84:85], v[40:43]\n"
      "v_mfma_f32_4x4x4_16b_bf16 v[124:127], v[28:29], a[86:87], v[40:43]\n"
      "s_waitcnt lgkmcnt(6)\n"
      "v_mfma_f32_16x16x32_bf16 v[80:83], a[224:227], v[208:211], v[80:83]\n"
      "ds_read_b64_tr_b16 a[252:253], v12 offset:53248\n"
      "ds_read_b64_tr_b16 a[254:255], v12 offset:53760\n"
      "v_mfma_f32_16x16x32_bf16 v[84:87], a[224:227], v[212:215], v[84:87]\n"
      "v_cvt_pk_bf16_f32 v224, v112, v113\n"
      "v_cvt_pk_bf16_f32 v225, v114, v115\n"
      "v_mfma_f32_16x16x32_bf16 v[88:91], a[228:231], v[208:211], v[88:91]\n"
      "ds_read_b64_tr_b16 a[216:217], v9 offset:57344\n"
      "ds_read_b64_tr_b16 a[218:219], v9 offset:57856\n"
      "v_mfma_f32_16x16x32_bf16 v[92:95], a[228:231], v[212:215], v[92:95]\n"
      "buffer_load_dwordx4 v4, s[28:31], s66 offen offset:2048 lds\n"
      "v_cvt_pk_bf16_f32 v226, v116, v117\n"
      "v_cvt_pk_bf16_f32 v227, v118, v119\n"
      "v_mfma_f32_16x16x32_bf16 v[96:99], a[232:235], v[208:211], v[96:99]\n"
      "ds_read_b64_tr_b16 a[220:221], v10 offset:57344\n"
      "ds_read_b64_tr_b16 a[222:223], v10 offset:57856\n"
      "v_mfma_f32_16x16x32_bf16 v[100:103], a[232:235], v[212:215], v[100:103]\n"
      "v_cvt_pk_bf16_f32 v228, v120, v121\n"
      "v_cvt_pk_bf16_f32 v229, v122, v123\n"
      "v_mfma_f32_16x16x32_bf16 v[104:107], a[236:239], v[208:211], v[104:107]\n"
      "ds_read_b64_tr_b16 a[224:225], v11 offset:57344\n"
      "ds_read_b64_tr_b16 a[226:227], v11 offset:57856\n"
      "v_mfma_f32_16x16x32_bf16 v[108:111], a[236:239], v[212:215], v[108:111]\n"
      "buffer_load_dwordx4 v4, s[28:31], s67 offen offset:3072 lds\n"
      "v_cvt_pk_bf16_f32 v230, v124, v125\n"
      "v_cvt_pk_bf16_f32 v231, v126, v127\n"
      "v_mfma_f32_4x4x4_16b_bf16 v[128:131], v[28:29], a[88:89], v[24:27]\n"
      "v_mfma_f32_4x4x4_16b_bf16 v[132:135], v[28:29], a[90:91], v[24:27]\n"
      "v_mfma_f32_4x4x4_16b_bf16 v[136:139], v[28:29], a[92:93], v[40:43]\n"
      "v_mfma_f32_4x4x4_16b_bf16 v[140:143], v[28:29], a[94:95], v[40:43]\n"
      "s_waitcnt lgkmcnt(6)\n"
      "v_mfma_f32_16x16x32_bf16 v[80:83], a[240:243], v[216:219], v[80:83]\n"
      "ds_read_b64_tr_b16 a[228:229], v12 offset:57344\n"
      "ds_read_b64_tr_b16 a[230:231], v12 offset:57856\n"
      "v_mfma_f32_16x16x32_bf16 v[84:87], a[240:243], v[220:223], v[84:87]\n"
      "v_cvt_pk_bf16_f32 v232, v128, v129\n"
      "v_cvt_pk_bf16_f32 v233, v130, v131\n"
      "v_mfma_f32_16x16x32_bf16 v[88:91], a[244:247], v[216:219], v[88:91]\n"
      "ds_read_b64_tr_b16 a[232:233], v9 offset:61440\n"
      "ds_read_b64_tr_b16 a[234:235], v9 offset:61952\n"
      "v_mfma_f32_16x16x32_bf16 v[92:95], a[244:247], v[220:223], v[92:95]\n"
      "s_add_u32 m0, s95, 49152\n"
      "v_add_u32 v4, s18, v4\n"
      "buffer_load_dwordx4 v4, s[28:31], 0 offen offset:0 lds\n"
      "v_cvt_pk_bf16_f32 v234, v132, v133\n"
      "v_cvt_pk_bf16_f32 v235, v134, v135\n"
      "v_mfma_f32_16x16x32_bf16 v[96:99], a[248:251], v[216:219], v[96:99]\n"
      "ds_read_b64_tr_b16 a[236:237], v10 offset:61440\n"
      "ds_read_b64_tr_b16 a[238:239], v10 offset:61952\n"
      "v_mfma_f32_16x16x32_bf16 v[100:103], a[248:251], v[220:223], v[100:103]\n"
      "v_cvt_pk_bf16_f32 v236, v136, v137\n"
      "v_cvt_pk_bf16_f32 v237, v138, v139\n"
      "v_mfma_f32_16x16x32_bf16 v[104:107], a[252:255], v[216:219], v[104:107]\n"
      "ds_read_b64_tr_b16 a[240:241], v11 offset:61440\n"
      "ds_read_b64_tr_b16 a[242:243], v11 offset:61952\n"
      "v_mfma_f32_16x16x32_bf16 v[108:111], a[252:255], v[220:223], v[108:111]\n"
      "buffer_load_dwordx4 v4, s[28:31], s65 offen offset:1024 lds\n"
      "v_cvt_pk_bf16_f32 v238, v140, v141\n"
      "v_cvt_pk_bf16_f32 v239, v142, v143\n"
      "v_mfma_f32_4x4x4_16b_bf16 v[112:115], v[28:29], a[96:97], v[24:27]\n"
      "v_mfma_f32_4x4x4_16b_bf16 v[116:119], v[28:29], a[98:99], v[24:27]\n"
      "v_mfma_f32_4x4x4_16b_bf16 v[120:123], v[28:29], a[100:101], v[40:43]\n"
      "v_mfma_f32_4x4x4_16b_bf16 v[124:127], v[28:29], a[102:103], v[40:43]\n"
      "s_waitcnt lgkmcnt(6)\n"
      "v_mfma_f32_16x16x32_bf16 v[80:83], a[216:219], v[224:227], v[80:83]\n"
      "ds_read_b64_tr_b16 a[244:245], v12 offset:61440\n"
      "ds_read_b64_tr_b16 a[246:247], v12 offset:61952\n"
      "v_mfma_f32_16x16x32_bf16 v[84:87], a[216:219], v[228:231], v[84:87]\n"
      "v_cvt_pk_bf16_f32 v240, v112, v113\n"
      "v_cvt_pk_bf16_f32 v241, v114, v115\n"
      "v_mfma_f32_16x16x32_bf16 v[88:91], a[220:223], v[224:227], v[88:91]\n"
      "ds_read_b64_tr_b16 a[248:249], v9 offset:0\n"
      "ds_read_b64_tr_b16 a[250:251], v9 offset:512\n"
      "v_mfma_f32_16x16x32_bf16 v[92:95], a[220:223], v[228:231], v[92:95]\n"
      "buffer_load_dwordx4 v4, s[28:31], s66 offen offset:2048 lds\n"
      "v_cvt_pk_bf16_f32 v242, v116, v117\n"
      "v_cvt_pk_bf16_f32 v243, v118, v119\n"
      "v_mfma_f32_16x16x32_bf16 v[96:99], a[224:227], v[224:227], v[96:99]\n"
      "ds_read_b64_tr_b16 a[252:253], v10 offset:0\n"
      "ds_read_b64_tr_b16 a[254:255], v10 offset:512\n"
      "v_mfma_f32_16x16x32_bf16 v[100:103], a[224:227], v[228:231], v[100:103]\n"
      "v_cvt_pk_bf16_f32 v244, v120, v121\n"
      "v_cvt_pk_bf16_f32 v245, v122, v123\n"
      "v_mfma_f32_16x16x32_bf16 v[104:107], a[228:231], v[224:227], v[104:107]\n"
      "ds_read_b64_tr_b16 a[216:217], v11 offset:0\n"
      "ds_read_b64_tr_b16 a[218:219], v11 offset:512\n"
      "v_mfma_f32_16x16x32_bf16 v[108:111], a[228:231], v[228:231], v[108:111]\n"
      "buffer_load_dwordx4 v4, s[28:31], s67 offen offset:3072 lds\n"
      "v_cvt_pk_bf16_f32 v246, v124, v125\n"
      "v_cvt_pk_bf16_f32 v247, v126, v127\n"
      "v_mfma_f32_4x4x4_16b_bf16 v[128:131], v[28:29], a[104:105], v[24:27]\n"
      "v_mfma_f32_4x4x4_16b_bf16 v[132:135], v[28:29], a[106:107], v[24:27]\n"
      "v_mfma_f32_4x4x4_16b_bf16 v[136:139], v[28:29], a[108:109], v[40:43]\n"
      "v_mfma_f32_4x4x4_16b_bf16 v[140:143], v[28:29], a[110:111], v[40:43]\n"
      "s_waitcnt lgkmcnt(6)\n"
      "v_mfma_f32_16x16x32_bf16 v[80:83], a[232:235], v[232:235], v[80:83]\n"
      "ds_read_b64_tr_b16 a[220:221], v12 offset:0\n"
      "ds_read_b64_tr_b16 a[222:223], v12 offset:512\n"
      "v_mfma_f32_16x16x32_bf16 v[84:87], a[232:235], v[236:239], v[84:87]\n"
      "v_cvt_pk_bf16_f32 v248, v128, v129\n"
      "v_cvt_pk_bf16_f32 v249, v130, v131\n"
      "v_mfma_f32_16x16x32_bf16 v[88:91], a[236:239], v[232:235], v[88:91]\n"
      "ds_read_b64_tr_b16 a[224:225], v9 offset:4096\n"
      "ds_read_b64_tr_b16 a[226:227], v9 offset:4608\n"
      "v_mfma_f32_16x16x32_bf16 v[92:95], a[236:239], v[236:239], v[92:95]\n"
      "s_add_u32 m0, s95, 65536\n"
      "v_add_u32 v4, s19, v4\n"
      "buffer_load_dwordx4 v4, s[28:31], 0 offen offset:0 lds\n"
      "v_cvt_pk_bf16_f32 v250, v132, v133\n"
      "v_cvt_pk_bf16_f32 v251, v134, v135\n"
      "v_mfma_f32_16x16x32_bf16 v[96:99], a[240:243], v[232:235], v[96:99]\n"
      "ds_read_b64_tr_b16 a[228:229], v10 offset:4096\n"
      "ds_read_b64_tr_b16 a[230:231], v10 offset:4608\n"
      "v_mfma_f32_16x16x32_bf16 v[100:103], a[240:243], v[236:239], v[100:103]\n"
      "v_cvt_pk_bf16_f32 v252, v136, v137\n"
      "v_cvt_pk_bf16_f32 v253, v138, v139\n"
      "s_sleep 2\n"
      "v_mfma_f32_16x16x32_bf16 v[104:107], a[244:247], v[232:235], v[104:107]\n"
      "ds_read_b64_tr_b16 a[232:233], v11 offset:4096\n"
      "ds_read_b64_tr_b16 a[234:235], v11 offset:4608\n"
      "v_mfma_f32_16x16x32_bf16 v[108:111], a[244:247], v[236:239], v[108:111]\n"
      "buffer_load_dwordx4 v4, s[28:31], s65 offen offset:1024 lds\n"
      "v_cvt_pk_bf16_f32 v254, v140, v141\n"
      "v_cvt_pk_bf16_f32 v255, v142, v143\n"
      "s_waitcnt vmcnt(10) lgkmcnt(6)\n"
      "s_barrier\n"
      "v_mfma_f32_16x16x32_bf16 v[80:83], a[248:251], v[240:243], v[80:83]\n"
      "ds_read_b64_tr_b16 a[236:237], v12 offset:4096\n"
      "ds_read_b64_tr_b16 a[238:239], v12 offset:4608\n"
      "v_mfma_f32_16x16x32_bf16 v[84:87], a[248:251], v[244:247], v[84:87]\n"
      "v_xor_b32 v9, 0x0, v7\n"
      "v_mfma_f32_16x16x32_bf16 v[88:91], a[252:255], v[240:243], v[88:91]\n"
      "ds_read_b64_tr_b16 a[240:241], v9 offset:0\n"
      "ds_read_b64_tr_b16 a[242:243], v9 offset:512\n"
      "v_mfma_f32_16x16x32_bf16 v[92:95], a[252:255], v[244:247], v[92:95]\n"
      "buffer_load_dwordx4 v4, s[28:31], s66 offen offset:2048 lds\n"
      "v_mfma_f32_16x16x32_bf16 v[96:99], a[216:219], v[240:243], v[96:99]\n"
      "ds_read_b64_tr_b16 a[244:245], v9 offset:8192\n"
      "ds_read_b64_tr_b16 a[246:247], v9 offset:8704\n"
      "v_mfma_f32_16x16x32_bf16 v[100:103], a[216:219], v[244:247], v[100:103]\n"
      "v_xor_b32 v10, 0x20, v7\n"
      "v_mov_b32 v30, v23\n"
      "v_mfma_f32_16x16x32_bf16 v[104:107], a[220:223], v[240:243], v[104:107]\n"
      "ds_read_b64_tr_b16 a[248:249], v10 offset:0\n"
      "ds_read_b64_tr_b16 a[250:251], v10 offset:512\n"
      "v_mfma_f32_16x16x32_bf16 v[108:111], a[220:223], v[244:247], v[108:111]\n"
      "buffer_load_dwordx4 v4, s[28:31], s67 offen offset:3072 lds\n"
      "v_permlane16_swap_b32 v23, v30\n"
      "s_waitcnt lgkmcnt(6)\n"
      "v_mfma_f32_16x16x32_bf16 v[80:83], a[224:227], v[248:251], v[80:83]\n"
      "ds_read_b64_tr_b16 a[252:253], v10 offset:8192\n"
      "ds_read_b64_tr_b16 a[254:255], v10 offset:8704\n"
      "v_mfma_f32_16x16x32_bf16 v[84:87], a[224:227], v[252:255], v[84:87]\n"
      "v_mfma_f32_16x16x32_bf16 v[88:91], a[228:231], v[248:251], v[88:91]\n"
      "ds_read_b64_tr_b16 a[216:217], v9 offset:2048\n"
      "ds_read_b64_tr_b16 a[218:219], v9 offset:2560\n"
      "v_mfma_f32_16x16x32_bf16 v[92:95], a[228:231], v[252:255], v[92:95]\n"
      "s_add_u32 m0, s95, 81920\n"
      "v_add_u32 v4, s8, v4\n"
      "buffer_load_dwordx4 v4, s[28:31], 0 offen offset:0 lds\n"
      "v_mfma_f32_16x16x32_bf16 v[96:99], a[232:235], v[248:251], v[96:99]\n"
      "ds_read_b64_tr_b16 a[220:221], v9 offset:10240\n"
      "ds_read_b64_tr_b16 a[222:223], v9 offset:10752\n"
      "v_mfma_f32_16x16x32_bf16 v[100:103], a[232:235], v[252:255], v[100:103]\n"
      "v_mfma_f32_16x16x32_bf16 v[104:107], a[236:239], v[248:251], v[104:107]\n"
      "ds_read_b64_tr_b16 a[224:225], v10 offset:2048\n"
      "ds_read_b64_tr_b16 a[226:227], v10 offset:2560\n"
      "v_mfma_f32_16x16x32_bf16 v[108:111], a[236:239], v[252:255], v[108:111]\n"
      "buffer_load_dwordx4 v4, s[28:31], s65 offen offset:1024 lds\n"
      "s_waitcnt lgkmcnt(6)\n"
      "v_mfma_f32_16x16x32_bf16 v[112:115], a[240:243], v[144:147], 0\n"
      "ds_read_b64_tr_b16 a[228:229], v10 offset:10240\n"
      "ds_read_b64_tr_b16 a[230:231], v10 offset:10752\n"
      "v_mfma_f32_16x16x32_bf16 v[128:131], a[240:243], v[148:151], 0\n"
      "v_mfma_f32_16x16x32_bf16 v[120:123], a[244:247], v[144:147], 0\n"
      "ds_read_b64_tr_b16 a[232:233], v9 offset:16384\n"
      "ds_read_b64_tr_b16 a[234:235], v9 offset:16896\n"
      "v_mfma_f32_16x16x32_bf16 v[136:139], a[244:247], v[148:151], 0\n"
      "s_mov_b64 exec, 1\n"
      "buffer_load_dwordx2 v[16:17], v0, s[48:51], 0 offen sc1\n"
      "s_mov_b64 exec, -1\n"
      "buffer_load_dwordx4 v4, s[28:31], s66 offen offset:2048 lds\n"
      "v_mfma_f32_16x16x32_bf16 v[116:119], a[248:251], v[144:147], 0\n"
      "ds_read_b64_tr_b16 a[236:237], v9 offset:24576\n"
      "ds_read_b64_tr_b16 a[238:239], v9 offset:25088\n"
      "v_mfma_f32_16x16x32_bf16 v[132:135], a[248:251], v[148:151], 0\n"
      "v_mfma_f32_16x16x32_bf16 v[124:127], a[252:255], v[144:147], 0\n"
      "ds_read_b64_tr_b16 a[240:241], v10 offset:16384\n"
      "ds_read_b64_tr_b16 a[242:243], v10 offset:16896\n"
      "v_mfma_f32_16x16x32_bf16 v[140:143], a[252:255], v[148:151], 0\n"
      "buffer_load_dwordx4 v4, s[28:31], s67 offen offset:3072 lds\n"
      "v_mul_f32 v80, v80, v23\n"
      "s_waitcnt lgkmcnt(6)\n"
      "v_mfma_f32_16x16x32_bf16 v[112:115], a[216:219], v[152:155], v[112:115]\n"
      "ds_read_b64_tr_b16 a[244:245], v10 offset:24576\n"
      "ds_read_b64_tr_b16 a[246:247], v10 offset:25088\n"
      "v_mfma_f32_16x16x32_bf16 v[128:131], a[216:219], v[156:159], v[128:131]\n"
      "v_mul_f32 v81, v81, v23\n"
      "v_mul_f32 v82, v82, v23\n"
      "v_mul_f32 v83, v83, v23\n"
      "v_mfma_f32_16x16x32_bf16 v[120:123], a[220:223], v[152:155], v[120:123]\n"
      "ds_read_b64_tr_b16 a[248:249], v9 offset:18432\n"
      "ds_read_b64_tr_b16 a[250:251], v9 offset:18944\n"
      "v_mfma_f32_16x16x32_bf16 v[136:139], a[220:223], v[156:159], v[136:139]\n"
      "v_mul_f32 v84, v84, v30\n"
      "v_mul_f32 v85, v85, v30\n"
      "v_mul_f32 v86, v86, v30\n"
      "v_mfma_f32_16x16x32_bf16 v[116:119], a[224:227], v[152:155], v[116:119]\n"
      "ds_read_b64_tr_b16 a[252:253], v9 offset:26624\n"
      "ds_read_b64_tr_b16 a[254:255], v9 offset:27136\n"
      "v_mfma_f32_16x16x32_bf16 v[132:135], a[224:227], v[156:159], v[132:135]\n"
      "v_mul_f32 v87, v87, v30\n"
      "v_cvt_pk_bf16_f32 v48, v80, v81\n"
      "v_cvt_pk_bf16_f32 v49, v82, v83\n"
      "v_cvt_pk_bf16_f32 v50, v84, v85\n"
      "v_mfma_f32_16x16x32_bf16 v[124:127], a[228:231], v[152:155], v[124:127]\n"
      "ds_read_b64_tr_b16 a[216:217], v10 offset:18432\n"
      "ds_read_b64_tr_b16 a[218:219], v10 offset:18944\n"
      "v_mfma_f32_16x16x32_bf16 v[140:143], a[228:231], v[156:159], v[140:143]\n"
      "v_cvt_pk_bf16_f32 v51, v86, v87\n"
      "v_permlane16_swap_b32 v48, v50\n"
      "s_nop 0\n"
      "v_permlane16_swap_b32 v49, v51\n"
      "s_waitcnt vmcnt(9) lgkmcnt(6)\n"
      "s_barrier\n"
      "v_mfma_f32_16x16x32_bf16 v[112:115], a[232:235], v[160:163], v[112:115]\n"
      "ds_read_b64_tr_b16 a[220:221], v10 offset:26624\n"
      "ds_read_b64_tr_b16 a[222:223], v10 offset:27136\n"
      "v_mfma_f32_16x16x32_bf16 v[128:131], a[232:235], v[164:167], v[128:131]\n"
      "s_add_u32 m0, s95, 98304\n"
      "v_add_u32 v4, s9, v4\n"
      "buffer_load_dwordx4 v4, s[28:31], 0 offen offset:0 lds\n"
      "buffer_store_dwordx4 v[48:51], v15, s[48:51], s84 offen offset:0 sc1\n"
      "v_mfma_f32_16x16x32_bf16 v[120:123], a[236:239], v[160:163], v[120:123]\n"
      "ds_read_b64_tr_b16 a[224:225], v9 offset:32768\n"
      "ds_read_b64_tr_b16 a[226:227], v9 offset:33280\n"
      "v_mfma_f32_16x16x32_bf16 v[136:139], a[236:239], v[164:167], v[136:139]\n"
      "v_mul_f32 v88, v88, v23\n"
      "v_mul_f32 v89, v89, v23\n"
      "v_mul_f32 v90, v90, v23\n"
      "v_mul_f32 v91, v91, v23\n"
      "v_mul_f32 v92, v92, v30\n"
      "v_mfma_f32_16x16x32_bf16 v[116:119], a[240:243], v[160:163], v[116:119]\n"
      "ds_read_b64_tr_b16 a[228:229], v9 offset:40960\n"
      "ds_read_b64_tr_b16 a[230:231], v9 offset:41472\n"
      "v_mfma_f32_16x16x32_bf16 v[132:135], a[240:243], v[164:167], v[132:135]\n"
      "buffer_load_dwordx4 v4, s[28:31], s65 offen offset:1024 lds\n"
      "v_mul_f32 v93, v93, v30\n"
      "v_mul_f32 v94, v94, v30\n"
      "v_mul_f32 v95, v95, v30\n"
      "v_cvt_pk_bf16_f32 v52, v88, v89\n"
      "v_mfma_f32_16x16x32_bf16 v[124:127], a[244:247], v[160:163], v[124:127]\n"
      "ds_read_b64_tr_b16 a[232:233], v10 offset:32768\n"
      "ds_read_b64_tr_b16 a[234:235], v10 offset:33280\n"
      "v_mfma_f32_16x16x32_bf16 v[140:143], a[244:247], v[164:167], v[140:143]\n"
      "v_cvt_pk_bf16_f32 v53, v90, v91\n"
      "v_cvt_pk_bf16_f32 v54, v92, v93\n"
      "v_cvt_pk_bf16_f32 v55, v94, v95\n"
      "s_nop 0\n"
      "v_permlane16_swap_b32 v52, v54\n"
      "v_permlane16_swap_b32 v53, v55\n"
      "s_waitcnt lgkmcnt(6)\n"
      "v_mfma_f32_16x16x32_bf16 v[112:115], a[248:251], v[168:171], v[112:115]\n"
      "ds_read_b64_tr_b16 a[236:237], v10 offset:40960\n"
      "ds_read_b64_tr_b16 a[238:239], v10 offset:41472\n"
      "v_mfma_f32_16x16x32_bf16 v[128:131], a[248:251], v[172:175], v[128:131]\n"
      "buffer_load_dwordx4 v4, s[28:31], s66 offen offset:2048 lds\n"
      "buffer_store_dwordx4 v[52:55], v15, s[48:51], s84 offen offset:1024 sc1\n"
      "v_mfma_f32_16x16x32_bf16 v[120:123], a[252:255], v[168:171], v[120:123]\n"
      "ds_read_b64_tr_b16 a[240:241], v9 offset:34816\n"
      "ds_read_b64_tr_b16 a[242:243], v9 offset:35328\n"
      "v_mfma_f32_16x16x32_bf16 v[136:139], a[252:255], v[172:175], v[136:139]\n"
      "v_mul_f32 v96, v96, v23\n"
      "v_mul_f32 v97, v97, v23\n"
      "v_mul_f32 v98, v98, v23\n"
      "v_mul_f32 v99, v99, v23\n"
      "v_mul_f32 v100, v100, v30\n"
      "v_mul_f32 v101, v101, v30\n"
      "v_mfma_f32_16x16x32_bf16 v[116:119], a[216:219], v[168:171], v[116:119]\n"
      "ds_read_b64_tr_b16 a[244:245], v9 offset:43008\n"
      "ds_read_b64_tr_b16 a[246:247], v9 offset:43520\n"
      "v_mfma_f32_16x16x32_bf16 v[132:135], a[216:219], v[172:175], v[132:135]\n"
      "buffer_load_dwordx4 v4, s[28:31], s67 offen offset:3072 lds\n"
      "v_mul_f32 v102, v102, v30\n"
      "v_mul_f32 v103, v103, v30\n"
      "v_cvt_pk_bf16_f32 v56, v96, v97\n"
      "v_mfma_f32_16x16x32_bf16 v[124:127], a[220:223], v[168:171], v[124:127]\n"
      "ds_read_b64_tr_b16 a[248:249], v10 offset:34816\n"
      "ds_read_b64_tr_b16 a[250:251], v10 offset:35328\n"
      "v_mfma_f32_16x16x32_bf16 v[140:143], a[220:223], v[172:175], v[140:143]\n"
      "v_cvt_pk_bf16_f32 v57, v98, v99\n"
      "v_cvt_pk_bf16_f32 v58, v100, v101\n"
      "v_cvt_pk_bf16_f32 v59, v102, v103\n"
      "s_nop 0\n"
      "v_permlane16_swap_b32 v56, v58\n"
      "v_permlane16_swap_b32 v57, v59\n"
      "s_waitcnt lgkmcnt(6)\n"
      "v_mfma_f32_16x16x32_bf16 v[112:115], a[224:227], v[176:179], v[112:115]\n"
      "ds_read_b64_tr_b16 a[252:253], v10 offset:43008\n"
      "ds_read_b64_tr_b16 a[254:255], v10 offset:43520\n"
      "v_mfma_f32_16x16x32_bf16 v[128:131], a[224:227], v[180:183], v[128:131]\n"
      "buffer_store_dwordx4 v[56:59], v15, s[48:51], s84 offen offset:2048 sc1\n"
      "v_mul_f32 v104, v104, v23\n"
      "v_mfma_f32_16x16x32_bf16 v[120:123], a[228:231], v[176:179], v[120:123]\n"
      "ds_read_b64_tr_b16 a[216:217], v9 offset:49152\n"
      "ds_read_b64_tr_b16 a[218:219], v9 offset:49664\n"
      "v_mfma_f32_16x16x32_bf16 v[136:139], a[228:231], v[180:183], v[136:139]\n"
      "v_mul_f32 v105, v105, v23\n"
      "v_mul_f32 v106, v106, v23\n"
      "v_mul_f32 v107, v107, v23\n"
      "v_mul_f32 v108, v108, v30\n"
      "v_mul_f32 v109, v109, v30\n"
      "v_mfma_f32_16x16x32_bf16 v[116:119], a[232:235], v[176:179], v[116:119]\n"
      "ds_read_b64_tr_b16 a[220:221], v9 offset:57344\n"
      "ds_read_b64_tr_b16 a[222:223], v9 offset:57856\n"
      "v_mfma_f32_16x16x32_bf16 v[132:135], a[232:235], v[180:183], v[132:135]\n"
      "v_mul_f32 v110, v110, v30\n"
      "v_mul_f32 v111, v111, v30\n"
      "v_cvt_pk_bf16_f32 v60, v104, v105\n"
      "v_cvt_pk_bf16_f32 v61, v106, v107\n"
      "v_mfma_f32_16x16x32_bf16 v[124:127], a[236:239], v[176:179], v[124:127]\n"
      "ds_read_b64_tr_b16 a[224:225], v10 offset:49152\n"
      "ds_read_b64_tr_b16 a[226:227], v10 offset:49664\n"
      "v_mfma_f32_16x16x32_bf16 v[140:143], a[236:239], v[180:183], v[140:143]\n"
      "v_cvt_pk_bf16_f32 v62, v108, v109\n"
      "v_cvt_pk_bf16_f32 v63, v110, v111\n"
      "s_nop 0\n"
      "v_permlane16_swap_b32 v60, v62\n"
      "v_permlane16_swap_b32 v61, v63\n"
      "s_waitcnt lgkmcnt(6)\n"
      "v_mfma_f32_16x16x32_bf16 v[112:115], a[240:243], v[184:187], v[112:115]\n"
      "ds_read_b64_tr_b16 a[228:229], v10 offset:57344\n"
      "ds_read_b64_tr_b16 a[230:231], v10 offset:57856\n"
      "v_mfma_f32_16x16x32_bf16 v[128:131], a[240:243], v[188:191], v[128:131]\n"
      "buffer_store_dwordx4 v[60:63], v15, s[48:51], s84 offen offset:3072 sc1\n"
      "v_mfma_f32_16x16x32_bf16 v[120:123], a[244:247], v[184:187], v[120:123]\n"
      "ds_read_b64_tr_b16 a[232:233], v9 offset:51200\n"
      "ds_read_b64_tr_b16 a[234:235], v9 offset:51712\n"
      "v_mfma_f32_16x16x32_bf16 v[136:139], a[244:247], v[188:191], v[136:139]\n"
      "s_lshl3_add_u32 s88, s55, s88\n"
      "s_add_u32 s88, s88, 131072\n"
      "s_lshl3_add_u32 s60, s53, s52\n"
      "s_lshl_b32 s89, s60, 12\n"
      "v_mfma_f32_16x16x32_bf16 v[116:119], a[248:251], v[184:187], v[116:119]\n"
      "ds_read_b64_tr_b16 a[236:237], v9 offset:59392\n"
      "ds_read_b64_tr_b16 a[238:239], v9 offset:59904\n"
      "v_mfma_f32_16x16x32_bf16 v[132:135], a[248:251], v[188:191], v[132:135]\n"
      "s_add_u32 s89, s89, 262144\n"
      "s_mul_i32 s60, s60, 71680\n"
      "s_add_u32 s82, s60, 5300224\n"
      "v_and_b32 v35, 15, v0\n"
      "v_mul_u32_u24 v35, 1792, v35\n"
      "v_mfma_f32_16x16x32_bf16 v[124:127], a[252:255], v[184:187], v[124:127]\n"
      "ds_read_b64_tr_b16 a[240:241], v10 offset:51200\n"
      "ds_read_b64_tr_b16 a[242:243], v10 offset:51712\n"
      "v_mfma_f32_16x16x32_bf16 v[140:143], a[252:255], v[188:191], v[140:143]\n"
      "s_lshl3_add_u32 s60, s53, s55\n"
      "s_lshl3_add_u32 s60, s60, s52\n"
      "s_lshl_b32 s60, s60, 11\n"
      "s_add_u32 s92, s60, 14680064\n"
      "v_lshrrev_b32 v36, 5, v0\n"
      "s_waitcnt vmcnt(8) lgkmcnt(6)\n"
      "s_barrier\n"
      "v_mfma_f32_16x16x32_bf16 v[112:115], a[216:219], v[192:195], v[112:115]\n"
      "ds_read_b64_tr_b16 a[244:245], v10 offset:59392\n"
      "ds_read_b64_tr_b16 a[246:247], v10 offset:59904\n"
      "v_mfma_f32_16x16x32_bf16 v[128:131], a[216:219], v[196:199], v[128:131]\n"
      "s_add_u32 m0, s95, 114688\n"
      "v_add_u32 v4, s10, v4\n"
      "buffer_load_dwordx4 v4, s[28:31], 0 offen offset:0 lds\n"
      "v_xor_b32 v11, 0xc000, v7\n"
      "v_mfma_f32_16x16x32_bf16 v[120:123], a[220:223], v[192:195], v[120:123]\n"
      "ds_read_b64_tr_b16 a[248:249], v11 offset:16384\n"
      "ds_read_b64_tr_b16 a[250:251], v11 offset:16896\n"
      "v_mfma_f32_16x16x32_bf16 v[136:139], a[220:223], v[196:199], v[136:139]\n"
      "v_mul_u32_u24 v39, 896, v0\n"
      "s_xor_b32 s91, s92, 8192\n"
      "s_lshl3_add_u32 s90, s55, s89\n"
      "v_lshl_add_u32 v15, v36, 4, v35\n"
      "v_mfma_f32_16x16x32_bf16 v[116:119], a[224:227], v[192:195], v[116:119]\n"
      "ds_read_b64_tr_b16 a[252:253], v11 offset:24576\n"
      "ds_read_b64_tr_b16 a[254:255], v11 offset:25088\n"
      "v_mfma_f32_16x16x32_bf16 v[132:135], a[224:227], v[196:199], v[132:135]\n"
      "v_xor_b32 v12, 0xc020, v7\n"
      "v_readfirstlane_b32 s96, v16\n"
      "v_readfirstlane_b32 s97, v17\n"
      "s_sub_u32 s93, s89, 0\n"
      "v_and_b32 v36, 16, v0\n"
      "v_mfma_f32_16x16x32_bf16 v[124:127], a[228:231], v[192:195], v[124:127]\n"
      "ds_read_b64_tr_b16 a[216:217], v12 offset:16384\n"
      "ds_read_b64_tr_b16 a[218:219], v12 offset:16896\n"
      "v_mfma_f32_16x16x32_bf16 v[140:143], a[228:231], v[196:199], v[140:143]\n"
      "buffer_load_dwordx4 v4, s[28:31], s65 offen offset:1024 lds\n"
      "s_add_u32 s96, s96, 0x7f4a7c15\n"
      "s_addc_u32 s97, s97, 0x9e3779b9\n"
      "v_lshl_add_u32 v15, v36, 1, v15\n"
      "s_waitcnt lgkmcnt(6)\n"
      "v_mfma_f32_16x16x32_bf16 v[112:115], a[232:235], v[200:203], v[112:115]\n"
      "ds_read_b64_tr_b16 a[220:221], v12 offset:24576\n"
      "ds_read_b64_tr_b16 a[222:223], v12 offset:25088\n"
      "v_mfma_f32_16x16x32_bf16 v[128:131], a[232:235], v[204:207], v[128:131]\n"
      "v_mov_b32 v16, s96\n"
      "v_mov_b32 v17, s97\n"
      "s_nop 0\n"
      ".LPAD2:\n"
      "s_cbranch_execz .LPAD4\n"
      "v_mfma_f32_16x16x32_bf16 v[120:123], a[236:239], v[200:203], v[120:123]\n"
      "ds_read_b64_tr_b16 a[224:225], v11 offset:18432\n"
      "ds_read_b64_tr_b16 a[226:227], v11 offset:18944\n"
      "v_mfma_f32_16x16x32_bf16 v[136:139], a[236:239], v[204:207], v[136:139]\n"
      "s_lshl_b32 s61, s55, 7\n"
      "s_mul_i32 s57, s12, 12544\n"
      "s_lshl_b32 s60, s55, 13\n"
      "v_mfma_f32_16x16x32_bf16 v[116:119], a[240:243], v[200:203], v[116:119]\n"
      "ds_read_b64_tr_b16 a[228:229], v11 offset:26624\n"
      "ds_read_b64_tr_b16 a[230:231], v11 offset:27136\n"
      "v_mfma_f32_16x16x32_bf16 v[132:135], a[240:243], v[204:207], v[132:135]\n"
      "v_mov_b32 v72, -1\n"
      "s_sub_u32 s59, s82, 57344\n"
      "s_add_u32 s75, s59, s60\n"
      "s_mul_i32 s68, s60, 14\n"
      "v_mfma_f32_16x16x32_bf16 v[124:127], a[244:247], v[200:203], v[124:127]\n"
      "ds_read_b64_tr_b16 a[232:233], v12 offset:18432\n"
      "ds_read_b64_tr_b16 a[234:235], v12 offset:18944\n"
      "v_mfma_f32_16x16x32_bf16 v[140:143], a[244:247], v[204:207], v[140:143]\n"
      "buffer_load_dwordx4 v4, s[28:31], s66 offen offset:2048 lds\n"
      "v_mov_b32 v73, -1\n"
      "v_mov_b32 v74, -1\n"
      "s_waitcnt lgkmcnt(6)\n"
      "v_mfma_f32_16x16x32_bf16 v[112:115], a[248:251], v[208:211], v[112:115]\n"
      "ds_read_b64_tr_b16 a[236:237], v12 offset:26624\n"
      "ds_read_b64_tr_b16 a[238:239], v12 offset:27136\n"
      "v_mfma_f32_16x16x32_bf16 v[128:131], a[248:251], v[212:215], v[128:131]\n"
      "s_add_u32 s83, s82, 8192\n"
      "s_addk_i32 s60, 8192\n"
      "v_mfma_f32_16x16x32_bf16 v[120:123], a[252:255], v[208:211], v[120:123]\n"
      "ds_read_b64_tr_b16 a[240:241], v11 offset:32768\n"
      "ds_read_b64_tr_b16 a[242:243], v11 offset:33280\n"
      "v_mfma_f32_16x16x32_bf16 v[136:139], a[252:255], v[212:215], v[136:139]\n"
      "v_mov_b32 v75, -1\n"
      "s_cmp_eq_u32 s60, 57344\n"
      "s_cselect_b32 s60, 0, s60\n"
      "s_mul_i32 s69, s60, 14\n"
      "v_mfma_f32_16x16x32_bf16 v[116:119], a[216:219], v[208:211], v[116:119]\n"
      "ds_read_b64_tr_b16 a[244:245], v11 offset:40960\n"
      "ds_read_b64_tr_b16 a[246:247], v11 offset:41472\n"
      "v_mfma_f32_16x16x32_bf16 v[132:135], a[216:219], v[212:215], v[132:135]\n"
      "s_add_u32 s57, s57, s61\n"
      "s_mul_i32 s58, s12, 3584\n"
      "s_add_u32 s76, s59, s60\n"
      "v_mfma_f32_16x16x32_bf16 v[124:127], a[220:223], v[208:211], v[124:127]\n"
      "ds_read_b64_tr_b16 a[248:249], v12 offset:32768\n"
      "ds_read_b64_tr_b16 a[250:251], v12 offset:33280\n"
      "v_mfma_f32_16x16x32_bf16 v[140:143], a[220:223], v[212:215], v[140:143]\n"
      "buffer_load_dwordx4 v4, s[28:31], s67 offen offset:3072 lds\n"
      "s_add_u32 s58, s58, s61\n"
      "s_addk_i32 s60, 8192\n"
      "s_waitcnt lgkmcnt(6)\n"
      "v_mfma_f32_16x16x32_bf16 v[112:115], a[224:227], v[216:219], v[112:115]\n"
      "ds_read_b64_tr_b16 a[252:253], v12 offset:40960\n"
      "ds_read_b64_tr_b16 a[254:255], v12 offset:41472\n"
      "v_mfma_f32_16x16x32_bf16 v[128:131], a[224:227], v[220:223], v[128:131]\n"
      "s_mov_b64 exec, 0x3fff\n"
      "buffer_load_dword v45, v39, s[36:39], s57 offen\n"
      "s_mov_b64 exec, -1\n"
      "v_mfma_f32_16x16x32_bf16 v[120:123], a[228:231], v[216:219], v[120:123]\n"
      "ds_read_b64_tr_b16 a[216:217], v11 offset:34816\n"
      "ds_read_b64_tr_b16 a[218:219], v11 offset:35328\n"
      "v_mfma_f32_16x16x32_bf16 v[136:139], a[228:231], v[220:223], v[136:139]\n"
      "v_mov_b32 v25, v20\n"
      "s_cmp_eq_u32 s60, 57344\n"
      "s_cselect_b32 s60, 0, s60\n"
      "v_mfma_f32_16x16x32_bf16 v[116:119], a[232:235], v[216:219], v[116:119]\n"
      "ds_read_b64_tr_b16 a[220:221], v11 offset:43008\n"
      "ds_read_b64_tr_b16 a[222:223], v11 offset:43520\n"
      "v_mfma_f32_16x16x32_bf16 v[132:135], a[232:235], v[220:223], v[132:135]\n"
      "s_mov_b64 exec, 15\n"
      "buffer_load_dword v45, v39, s[40:43], s58 offen\n"
      "s_mov_b64 exec, -1\n"
      "s_add_u32 m0, s95, 131072\n"
      "v_add_u32 v4, s16, v4\n"
      "buffer_load_dwordx4 v4, s[28:31], 0 offen offset:0 lds\n"
      "v_mov_b32 v26, v21\n"
      "v_mfma_f32_16x16x32_bf16 v[124:127], a[236:239], v[216:219], v[124:127]\n"
      "ds_read_b64_tr_b16 a[224:225], v12 offset:34816\n"
      "ds_read_b64_tr_b16 a[226:227], v12 offset:35328\n"
      "v_mfma_f32_16x16x32_bf16 v[140:143], a[236:239], v[220:223], v[140:143]\n"
      "v_mov_b32 v27, v22\n"
      "s_mul_i32 s70, s60, 14\n"
      "s_add_u32 s77, s59, s60\n"
      "s_addk_i32 s60, 8192\n"
      "s_waitcnt vmcnt(9) lgkmcnt(6)\n"
      "s_barrier\n"
      "v_mfma_f32_16x16x32_bf16 v[112:115], a[240:243], v[224:227], v[112:115]\n"
      "ds_read_b64_tr_b16 a[228:229], v12 offset:43008\n"
      "ds_read_b64_tr_b16 a[230:231], v12 offset:43520\n"
      "v_mfma_f32_16x16x32_bf16 v[128:131], a[240:243], v[228:231], v[128:131]\n"
      "v_mfma_f32_16x16x32_bf16 v[120:123], a[244:247], v[224:227], v[120:123]\n"
      "ds_read_b64_tr_b16 a[232:233], v11 offset:49152\n"
      "ds_read_b64_tr_b16 a[234:235], v11 offset:49664\n"
      "v_mfma_f32_16x16x32_bf16 v[136:139], a[244:247], v[228:231], v[136:139]\n"
      "v_permlane16_swap_b32 v20, v25\n"
      "s_cmp_eq_u32 s60, 57344\n"
      "s_cselect_b32 s60, 0, s60\n"
      "s_mul_i32 s71, s60, 14\n"
      "v_mfma_f32_16x16x32_bf16 v[116:119], a[248:251], v[224:227], v[116:119]\n"
      "ds_read_b64_tr_b16 a[236:237], v11 offset:57344\n"
      "ds_read_b64_tr_b16 a[238:239], v11 offset:57856\n"
      "v_mfma_f32_16x16x32_bf16 v[132:135], a[248:251], v[228:231], v[132:135]\n"
      "buffer_load_dwordx4 v4, s[28:31], s65 offen offset:1024 lds\n"
      "v_permlane16_swap_b32 v21, v26\n"
      "s_add_u32 s78, s59, s60\n"
      "v_mfma_f32_16x16x32_bf16 v[124:127], a[252:255], v[224:227], v[124:127]\n"
      "ds_read_b64_tr_b16 a[240:241], v12 offset:49152\n"
      "ds_read_b64_tr_b16 a[242:243], v12 offset:49664\n"
      "v_mfma_f32_16x16x32_bf16 v[140:143], a[252:255], v[228:231], v[140:143]\n"
      "s_waitcnt vmcnt(8)\n"
      "buffer_store_dwordx2 v[16:17], v13, s[48:51], s88 offen offset:0 sc1\n"
      "v_permlane16_swap_b32 v22, v27\n"
      "s_addk_i32 s60, 8192\n"
      "s_cmp_eq_u32 s60, 57344\n"
      "s_cselect_b32 s60, 0, s60\n"
      "s_waitcnt lgkmcnt(6)\n"
      "v_mfma_f32_16x16x32_bf16 v[112:115], a[216:219], v[232:235], v[112:115]\n"
      "ds_read_b64_tr_b16 a[244:245], v12 offset:57344\n"
      "ds_read_b64_tr_b16 a[246:247], v12 offset:57856\n"
      "v_mfma_f32_16x16x32_bf16 v[128:131], a[216:219], v[236:239], v[128:131]\n"
      "s_mul_i32 s72, s60, 14\n"
      "s_add_u32 s79, s59, s60\n"
      "v_mfma_f32_16x16x32_bf16 v[120:123], a[220:223], v[232:235], v[120:123]\n"
      "ds_read_b64_tr_b16 a[248:249], v11 offset:51200\n"
      "ds_read_b64_tr_b16 a[250:251], v11 offset:51712\n"
      "v_mfma_f32_16x16x32_bf16 v[136:139], a[220:223], v[236:239], v[136:139]\n"
      "s_addk_i32 s60, 8192\n"
      "s_cmp_eq_u32 s60, 57344\n"
      "s_cselect_b32 s60, 0, s60\n"
      "v_mfma_f32_16x16x32_bf16 v[116:119], a[224:227], v[232:235], v[116:119]\n"
      "ds_read_b64_tr_b16 a[252:253], v11 offset:59392\n"
      "ds_read_b64_tr_b16 a[254:255], v11 offset:59904\n"
      "v_mfma_f32_16x16x32_bf16 v[132:135], a[224:227], v[236:239], v[132:135]\n"
      "buffer_load_dwordx4 v4, s[28:31], s66 offen offset:2048 lds\n"
      "s_mul_i32 s73, s60, 14\n"
      "s_add_u32 s80, s59, s60\n"
      "v_mfma_f32_16x16x32_bf16 v[124:127], a[228:231], v[232:235], v[124:127]\n"
      "ds_read_b64_tr_b16 a[216:217], v12 offset:51200\n"
      "ds_read_b64_tr_b16 a[218:219], v12 offset:51712\n"
      "v_mfma_f32_16x16x32_bf16 v[140:143], a[228:231], v[236:239], v[140:143]\n"
      "s_addk_i32 s60, 8192\n"
      "s_cmp_eq_u32 s60, 57344\n"
      "s_cselect_b32 s60, 0, s60\n"
      "s_waitcnt lgkmcnt(6)\n"
      "v_mfma_f32_16x16x32_bf16 v[112:115], a[232:235], v[240:243], v[112:115]\n"
      "ds_read_b64_tr_b16 a[220:221], v12 offset:59392\n"
      "ds_read_b64_tr_b16 a[222:223], v12 offset:59904\n"
      "v_mfma_f32_16x16x32_bf16 v[128:131], a[232:235], v[244:247], v[128:131]\n"
      "v_xor_b32 v9, 0x40, v7\n"
      "s_mul_i32 s74, s60, 14\n"
      "v_mfma_f32_16x16x32_bf16 v[120:123], a[236:239], v[240:243], v[120:123]\n"
      "ds_read_b64_tr_b16 a[224:225], v9 offset:0\n"
      "ds_read_b64_tr_b16 a[226:227], v9 offset:512\n"
      "v_mfma_f32_16x16x32_bf16 v[136:139], a[236:239], v[244:247], v[136:139]\n"
      "buffer_load_dwordx4 v4, s[28:31], s67 offen offset:3072 lds\n"
      "s_add_u32 s81, s59, s60\n"
      "s_add_u32 s98, s98, s13\n"
      "v_mfma_f32_16x16x32_bf16 v[116:119], a[240:243], v[240:243], v[116:119]\n"
      "ds_read_b64_tr_b16 a[228:229], v9 offset:8192\n"
      "ds_read_b64_tr_b16 a[230:231], v9 offset:8704\n"
      "v_mfma_f32_16x16x32_bf16 v[132:135], a[240:243], v[244:247], v[132:135]\n"
      "v_xor_b32 v10, 0x60, v7\n"
      "s_lshl3_add_u32 s13, s55, 17408\n"
      "s_movk_i32 s60, 896\n"
      "v_add_u32 v36, s95, v1\n"
      "v_mfma_f32_16x16x32_bf16 v[124:127], a[244:247], v[240:243], v[124:127]\n"
      "ds_read_b64_tr_b16 a[232:233], v10 offset:0\n"
      "ds_read_b64_tr_b16 a[234:235], v10 offset:512\n"
      "v_mfma_f32_16x16x32_bf16 v[140:143], a[244:247], v[244:247], v[140:143]\n"
      "v_bfrev_b32 v24, 1\n"
      "v_writelane_b32 v24, s13, 1\n"
      "v_writelane_b32 v24, s60, 0\n"
      "s_and_b32 s60, s52, 1\n"
      "s_waitcnt lgkmcnt(6)\n"
      "v_mfma_f32_16x16x32_bf16 v[112:115], a[248:251], v[248:251], v[112:115]\n"
      "ds_read_b64_tr_b16 a[236:237], v10 offset:8192\n"
      "ds_read_b64_tr_b16 a[238:239], v10 offset:8704\n"
      "v_mfma_f32_16x16x32_bf16 v[128:131], a[248:251], v[252:255], v[128:131]\n"
      "s_mul_i32 s60, s60, 12544\n"
      "s_mul_i32 s59, s52, 5376\n"
      "v_mfma_f32_16x16x32_bf16 v[120:123], a[252:255], v[248:251], v[120:123]\n"
      "ds_read_b64_tr_b16 a[240:241], v9 offset:2048\n"
      "ds_read_b64_tr_b16 a[242:243], v9 offset:2560\n"
      "v_mfma_f32_16x16x32_bf16 v[136:139], a[252:255], v[252:255], v[136:139]\n"
      "s_add_u32 m0, s95, 147456\n"
      "v_add_u32 v4, s17, v4\n"
      "buffer_load_dwordx4 v4, s[28:31], 0 offen offset:0 lds\n"
      "v_mfma_f32_16x16x32_bf16 v[116:119], a[216:219], v[248:251], v[116:119]\n"
      "ds_read_b64_tr_b16 a[244:245], v9 offset:10240\n"
      "ds_read_b64_tr_b16 a[246:247], v9 offset:10752\n"
      "v_mfma_f32_16x16x32_bf16 v[132:135], a[216:219], v[252:255], v[132:135]\n"
      "s_sub_u32 s60, s60, s59\n"
      "v_lshrrev_b32 v40, 5, v0\n"
      "v_mul_u32_u24 v40, 3584, v40\n"
      "v_add3_u32 v40, v40, v5, s60\n"
      "v_mfma_f32_16x16x32_bf16 v[124:127], a[220:223], v[248:251], v[124:127]\n"
      "ds_read_b64_tr_b16 a[248:249], v10 offset:2048\n"
      "ds_read_b64_tr_b16 a[250:251], v10 offset:2560\n"
      "v_mfma_f32_16x16x32_bf16 v[140:143], a[220:223], v[252:255], v[140:143]\n"
      "v_lshrrev_b32 v41, 4, v0\n"
      "v_lshl_add_u32 v41, v41, 8, v1\n"
      "v_add_u32 v42, 4096, v41\n"
      ".LJOIN:\n"
      "s_cbranch_execz .LPAD5\n"
      "s_waitcnt lgkmcnt(6)\n"
      "v_mfma_f32_16x16x32_bf16 v[80:83], a[224:227], v[144:147], 0\n"
      "ds_read_b64_tr_b16 a[252:253], v10 offset:10240\n"
      "ds_read_b64_tr_b16 a[254:255], v10 offset:10752\n"
      "v_mfma_f32_16x16x32_bf16 v[96:99], a[224:227], v[148:151], 0\n"
      "v_mfma_f32_16x16x32_bf16 v[88:91], a[228:231], v[144:147], 0\n"
      "ds_read_b64_tr_b16 a[216:217], v9 offset:16384\n"
      "ds_read_b64_tr_b16 a[218:219], v9 offset:16896\n"
      "v_mfma_f32_16x16x32_bf16 v[104:107], a[228:231], v[148:151], 0\n"
      "buffer_load_dwordx4 v4, s[28:31], s65 offen offset:1024 lds\n"
      "v_mfma_f32_16x16x32_bf16 v[84:87], a[232:235], v[144:147], 0\n"
      "ds_read_b64_tr_b16 a[220:221], v9 offset:24576\n"
      "ds_read_b64_tr_b16 a[222:223], v9 offset:25088\n"
      "v_mfma_f32_16x16x32_bf16 v[100:103], a[232:235], v[148:151], 0\n"
      "v_mfma_f32_16x16x32_bf16 v[92:95], a[236:239], v[144:147], 0\n"
      "ds_read_b64_tr_b16 a[224:225], v10 offset:16384\n"
      "ds_read_b64_tr_b16 a[226:227], v10 offset:16896\n"
      "v_mfma_f32_16x16x32_bf16 v[108:111], a[236:239], v[148:151], 0\n"
      "v_mul_f32 v32, v112, v112\n"
      "v_mul_f32 v33, v113, v113\n"
      "v_fma_f32 v32, v32, v21, v20\n"
      "v_fma_f32 v33, v33, v21, v20\n"
      "s_waitcnt lgkmcnt(6)\n"
      "v_mfma_f32_16x16x32_bf16 v[80:83], a[240:243], v[152:155], v[80:83]\n"
      "ds_read_b64_tr_b16 a[228:229], v10 offset:24576\n"
      "ds_read_b64_tr_b16 a[230:231], v10 offset:25088\n"
      "v_mfma_f32_16x16x32_bf16 v[96:99], a[240:243], v[156:159], v[96:99]\n"
      "buffer_load_dwordx4 v4, s[28:31], s66 offen offset:2048 lds\n"
      "v_mul_f32 v32, v32, v112\n"
      "v_mul_f32 v33, v33, v113\n"
      "v_mfma_f32_16x16x32_bf16 v[88:91], a[244:247], v[152:155], v[88:91]\n"
      "ds_read_b64_tr_b16 a[232:233], v9 offset:18432\n"
      "ds_read_b64_tr_b16 a[234:235], v9 offset:18944\n"
      "v_mfma_f32_16x16x32_bf16 v[104:107], a[244:247], v[156:159], v[104:107]\n"
      "v_exp_f32 v32, v32\n"
      "v_exp_f32 v33, v33\n"
      "v_mfma_f32_16x16x32_bf16 v[84:87], a[248:251], v[152:155], v[84:87]\n"
      "ds_read_b64_tr_b16 a[236:237], v9 offset:26624\n"
      "ds_read_b64_tr_b16 a[238:239], v9 offset:27136\n"
      "v_mfma_f32_16x16x32_bf16 v[100:103], a[248:251], v[156:159], v[100:103]\n"
      "v_mul_f32 v120, v112, v120\n"
      "v_mul_f32 v121, v113, v121\n"
      "v_fma_f32 v32, v32, v22, v22\n"
      "v_fma_f32 v33, v33, v22, v22\n"
      "v_mfma_f32_16x16x32_bf16 v[92:95], a[252:255], v[152:155], v[92:95]\n"
      "ds_read_b64_tr_b16 a[240:241], v10 offset:18432\n"
      "ds_read_b64_tr_b16 a[242:243], v10 offset:18944\n"
      "v_mfma_f32_16x16x32_bf16 v[108:111], a[252:255], v[156:159], v[108:111]\n"
      "v_rcp_f32 v32, v32\n"
      "v_rcp_f32 v33, v33\n"
      "s_waitcnt lgkmcnt(6)\n"
      "v_mfma_f32_16x16x32_bf16 v[80:83], a[216:219], v[160:163], v[80:83]\n"
      "ds_read_b64_tr_b16 a[244:245], v10 offset:26624\n"
      "ds_read_b64_tr_b16 a[246:247], v10 offset:27136\n"
      "v_mfma_f32_16x16x32_bf16 v[96:99], a[216:219], v[164:167], v[96:99]\n"
      "buffer_load_dwordx4 v4, s[28:31], s67 offen offset:3072 lds\n"
      "v_mul_f32 v120, v120, v32\n"
      "v_mul_f32 v121, v121, v33\n"
      "v_mfma_f32_16x16x32_bf16 v[88:91], a[220:223], v[160:163], v[88:91]\n"
      "ds_read_b64_tr_b16 a[248:249], v9 offset:32768\n"
      "ds_read_b64_tr_b16 a[250:251], v9 offset:33280\n"
      "v_mfma_f32_16x16x32_bf16 v[104:107], a[220:223], v[164:167], v[104:107]\n"
      "v_cvt_pk_bf16_f32 v48, v120, v121\n"
      "v_mul_f32 v32, v114, v114\n"
      "v_mul_f32 v33, v115, v115\n"
      "v_fma_f32 v32, v32, v21, v20\n"
      "v_mfma_f32_16x16x32_bf16 v[84:87], a[224:227], v[160:163], v[84:87]\n"
      "ds_read_b64_tr_b16 a[252:253], v9 offset:40960\n"
      "ds_read_b64_tr_b16 a[254:255], v9 offset:41472\n"
      "v_mfma_f32_16x16x32_bf16 v[100:103], a[224:227], v[164:167], v[100:103]\n"
      "v_fma_f32 v33, v33, v21, v20\n"
      "v_mul_f32 v32, v32, v114\n"
      "v_mul_f32 v33, v33, v115\n"
      "v_exp_f32 v32, v32\n"
      "v_mfma_f32_16x16x32_bf16 v[92:95], a[228:231], v[160:163], v[92:95]\n"
      "ds_read_b64_tr_b16 a[216:217], v10 offset:32768\n"
      "ds_read_b64_tr_b16 a[218:219], v10 offset:33280\n"
      "v_mfma_f32_16x16x32_bf16 v[108:111], a[228:231], v[164:167], v[108:111]\n"
      "v_exp_f32 v33, v33\n"
      "v_mul_f32 v122, v114, v122\n"
      "v_mul_f32 v123, v115, v123\n"
      "s_waitcnt lgkmcnt(6)\n"
      "v_mfma_f32_16x16x32_bf16 v[80:83], a[232:235], v[168:171], v[80:83]\n"
      "ds_read_b64_tr_b16 a[220:221], v10 offset:40960\n"
      "ds_read_b64_tr_b16 a[222:223], v10 offset:41472\n"
      "v_mfma_f32_16x16x32_bf16 v[96:99], a[232:235], v[172:175], v[96:99]\n"
      "v_fma_f32 v32, v32, v22, v22\n"
      "v_fma_f32 v33, v33, v22, v22\n"
      "v_mfma_f32_16x16x32_bf16 v[88:91], a[236:239], v[168:171], v[88:91]\n"
      "ds_read_b64_tr_b16 a[224:225], v9 offset:34816\n"
      "ds_read_b64_tr_b16 a[226:227], v9 offset:35328\n"
      "v_mfma_f32_16x16x32_bf16 v[104:107], a[236:239], v[172:175], v[104:107]\n"
      "v_rcp_f32 v32, v32\n"
      "v_rcp_f32 v33, v33\n"
      "v_mul_f32 v122, v122, v32\n"
      "v_mfma_f32_16x16x32_bf16 v[84:87], a[240:243], v[168:171], v[84:87]\n"
      "ds_read_b64_tr_b16 a[228:229], v9 offset:43008\n"
      "ds_read_b64_tr_b16 a[230:231], v9 offset:43520\n"
      "v_mfma_f32_16x16x32_bf16 v[100:103], a[240:243], v[172:175], v[100:103]\n"
      "s_add_u32 s35, s71, 86016\n"
      "buffer_load_dwordx4 a[164:167], v5, s[36:39], s35 offen\n"
      "v_mul_f32 v123, v123, v33\n"
      "v_mfma_f32_16x16x32_bf16 v[92:95], a[244:247], v[168:171], v[92:95]\n"
      "ds_read_b64_tr_b16 a[232:233], v10 offset:34816\n"
      "ds_read_b64_tr_b16 a[234:235], v10 offset:35328\n"
      "v_mfma_f32_16x16x32_bf16 v[108:111], a[244:247], v[172:175], v[108:111]\n"
      "v_cvt_pk_bf16_f32 v49, v122, v123\n"
      "v_mul_f32 v32, v116, v116\n"
      "v_mul_f32 v33, v117, v117\n"
      "v_fma_f32 v32, v32, v21, v20\n"
      "v_fma_f32 v33, v33, v21, v20\n"
      "s_waitcnt lgkmcnt(6)\n"
      "v_mfma_f32_16x16x32_bf16 v[80:83], a[248:251], v[176:179], v[80:83]\n"
      "ds_read_b64_tr_b16 a[236:237], v10 offset:43008\n"
      "ds_read_b64_tr_b16 a[238:239], v10 offset:43520\n"
      "v_mfma_f32_16x16x32_bf16 v[96:99], a[248:251], v[180:183], v[96:99]\n"
      "v_mul_f32 v32, v32, v116\n"
      "v_mul_f32 v33, v33, v117\n"
      "v_mfma_f32_16x16x32_bf16 v[88:91], a[252:255], v[176:179], v[88:91]\n"
      "ds_read_b64_tr_b16 a[240:241], v9 offset:49152\n"
      "ds_read_b64_tr_b16 a[242:243], v9 offset:49664\n"
      "v_mfma_f32_16x16x32_bf16 v[104:107], a[252:255], v[180:183], v[104:107]\n"
      "s_add_u32 s35, s72, 86016\n"
      "buffer_load_dwordx4 a[168:171], v5, s[36:39], s35 offen\n"
      "v_exp_f32 v32, v32\n"
      "v_mfma_f32_16x16x32_bf16 v[84:87], a[216:219], v[176:179], v[84:87]\n"
      "ds_read_b64_tr_b16 a[244:245], v9 offset:57344\n"
      "ds_read_b64_tr_b16 a[246:247], v9 offset:57856\n"
      "v_mfma_f32_16x16x32_bf16 v[100:103], a[216:219], v[180:183], v[100:103]\n"
      "v_exp_f32 v33, v33\n"
      "v_mul_f32 v124, v116, v124\n"
      "v_mul_f32 v125, v117, v125\n"
      "v_mfma_f32_16x16x32_bf16 v[92:95], a[220:223], v[176:179], v[92:95]\n"
      "ds_read_b64_tr_b16 a[248:249], v10 offset:49152\n"
      "ds_read_b64_tr_b16 a[250:251], v10 offset:49664\n"
      "v_mfma_f32_16x16x32_bf16 v[108:111], a[220:223], v[180:183], v[108:111]\n"
      "v_fma_f32 v32, v32, v22, v22\n"
      "v_fma_f32 v33, v33, v22, v22\n"
      "v_rcp_f32 v32, v32\n"
      "s_waitcnt lgkmcnt(6)\n"
      "v_mfma_f32_16x16x32_bf16 v[80:83], a[224:227], v[184:187], v[80:83]\n"
      "ds_read_b64_tr_b16 a[252:253], v10 offset:57344\n"
      "ds_read_b64_tr_b16 a[254:255], v10 offset:57856\n"
      "v_mfma_f32_16x16x32_bf16 v[96:99], a[224:227], v[188:191], v[96:99]\n"
      "v_rcp_f32 v33, v33\n"
      "v_mul_f32 v124, v124, v32\n"
      "v_mul_f32 v125, v125, v33\n"
      "v_mfma_f32_16x16x32_bf16 v[88:91], a[228:231], v[184:187], v[88:91]\n"
      "ds_read_b64_tr_b16 a[216:217], v9 offset:51200\n"
      "ds_read_b64_tr_b16 a[218:219], v9 offset:51712\n"
      "v_mfma_f32_16x16x32_bf16 v[104:107], a[228:231], v[188:191], v[104:107]\n"
      "s_add_u32 s35, s73, 86016\n"
      "buffer_load_dwordx4 a[172:175], v5, s[36:39], s35 offen\n"
      "v_cvt_pk_bf16_f32 v50, v124, v125\n"
      "v_mfma_f32_16x16x32_bf16 v[84:87], a[232:235], v[184:187], v[84:87]\n"
      "ds_read_b64_tr_b16 a[220:221], v9 offset:59392\n"
      "ds_read_b64_tr_b16 a[222:223], v9 offset:59904\n"
      "v_mfma_f32_16x16x32_bf16 v[100:103], a[232:235], v[188:191], v[100:103]\n"
      "v_mul_f32 v32, v118, v118\n"
      "v_mul_f32 v33, v119, v119\n"
      "v_fma_f32 v32, v32, v21, v20\n"
      "v_fma_f32 v33, v33, v21, v20\n"
      "v_mul_f32 v32, v32, v118\n"
      "v_mfma_f32_16x16x32_bf16 v[92:95], a[236:239], v[184:187], v[92:95]\n"
      "ds_read_b64_tr_b16 a[224:225], v10 offset:51200\n"
      "ds_read_b64_tr_b16 a[226:227], v10 offset:51712\n"
      "v_mfma_f32_16x16x32_bf16 v[108:111], a[236:239], v[188:191], v[108:111]\n"
      "v_mul_f32 v33, v33, v119\n"
      "v_exp_f32 v32, v32\n"
      "s_waitcnt lgkmcnt(6)\n"
      "v_mfma_f32_16x16x32_bf16 v[80:83], a[240:243], v[192:195], v[80:83]\n"
      "ds_read_b64_tr_b16 a[228:229], v10 offset:59392\n"
      "ds_read_b64_tr_b16 a[230:231], v10 offset:59904\n"
      "v_mfma_f32_16x16x32_bf16 v[96:99], a[240:243], v[196:199], v[96:99]\n"
      "buffer_store_dwordx4 v[72:75], v2, s[48:51], s75 offen offset:0 sc1\n"
      "s_add_u32 s35, s74, 86016\n"
      "buffer_load_dwordx4 a[176:179], v5, s[36:39], s35 offen\n"
      "v_xor_b32 v11, 0xc040, v7\n"
      "v_mfma_f32_16x16x32_bf16 v[88:91], a[244:247], v[192:195], v[88:91]\n"
      "ds_read_b64_tr_b16 a[232:233], v11 offset:16384\n"
      "ds_read_b64_tr_b16 a[234:235], v11 offset:16896\n"
      "v_mfma_f32_16x16x32_bf16 v[104:107], a[244:247], v[196:199], v[104:107]\n"
      "v_exp_f32 v33, v33\n"
      "v_mul_f32 v126, v118, v126\n"
      "v_mul_f32 v127, v119, v127\n"
      "v_fma_f32 v32, v32, v22, v22\n"
      "v_mfma_f32_16x16x32_bf16 v[84:87], a[248:251], v[192:195], v[84:87]\n"
      "ds_read_b64_tr_b16 a[236:237], v11 offset:24576\n"
      "ds_read_b64_tr_b16 a[238:239], v11 offset:25088\n"
      "v_mfma_f32_16x16x32_bf16 v[100:103], a[248:251], v[196:199], v[100:103]\n"
      "v_xor_b32 v12, 0xc060, v7\n"
      "v_fma_f32 v33, v33, v22, v22\n"
      "v_rcp_f32 v32, v32\n"
      "v_mfma_f32_16x16x32_bf16 v[92:95], a[252:255], v[192:195], v[92:95]\n"
      "ds_read_b64_tr_b16 a[240:241], v12 offset:16384\n"
      "ds_read_b64_tr_b16 a[242:243], v12 offset:16896\n"
      "v_mfma_f32_16x16x32_bf16 v[108:111], a[252:255], v[196:199], v[108:111]\n"
      "v_rcp_f32 v33, v33\n"
      "v_mul_f32 v126, v126, v32\n"
      "v_mul_f32 v127, v127, v33\n"
      "v_cvt_pk_bf16_f32 v51, v126, v127\n"
      "s_waitcnt lgkmcnt(6)\n"
      "v_mfma_f32_16x16x32_bf16 v[80:83], a[216:219], v[200:203], v[80:83]\n"
      "ds_read_b64_tr_b16 a[244:245], v12 offset:24576\n"
      "ds_read_b64_tr_b16 a[246:247], v12 offset:25088\n"
      "v_mfma_f32_16x16x32_bf16 v[96:99], a[216:219], v[204:207], v[96:99]\n"
      "s_mov_b32 s35, 0\n"
      "buffer_load_dwordx4 a[180:183], v40, s[40:43], s35 offen\n"
      "v_mfma_f32_16x16x32_bf16 v[88:91], a[220:223], v[200:203], v[88:91]\n"
      "ds_read_b64_tr_b16 a[248:249], v11 offset:18432\n"
      "ds_read_b64_tr_b16 a[250:251], v11 offset:18944\n"
      "v_mfma_f32_16x16x32_bf16 v[104:107], a[220:223], v[204:207], v[104:107]\n"
      "buffer_store_dwordx4 v[48:51], v1, s[48:51], s75 offen offset:0 sc1\n"
      "v_mul_f32 v32, v128, v128\n"
      "v_mul_f32 v33, v129, v129\n"
      "v_mfma_f32_16x16x32_bf16 v[84:87], a[224:227], v[200:203], v[84:87]\n"
      "ds_read_b64_tr_b16 a[252:253], v11 offset:26624\n"
      "ds_read_b64_tr_b16 a[254:255], v11 offset:27136\n"
      "v_mfma_f32_16x16x32_bf16 v[100:103], a[224:227], v[204:207], v[100:103]\n"
      "v_fma_f32 v32, v32, v26, v25\n"
      "v_fma_f32 v33, v33, v26, v25\n"
      "v_mul_f32 v32, v32, v128\n"
      "v_mul_f32 v33, v33, v129\n"
      "v_mfma_f32_16x16x32_bf16 v[92:95], a[228:231], v[200:203], v[92:95]\n"
      "ds_read_b64_tr_b16 a[216:217], v12 offset:18432\n"
      "ds_read_b64_tr_b16 a[218:219], v12 offset:18944\n"
      "v_mfma_f32_16x16x32_bf16 v[108:111], a[228:231], v[204:207], v[108:111]\n"
      "v_exp_f32 v32, v32\n"
      "v_exp_f32 v33, v33\n"
      "v_mul_f32 v136, v128, v136\n"
      "s_waitcnt vmcnt(7) lgkmcnt(6)\n"
      "s_barrier\n"
      "v_mfma_f32_16x16x32_bf16 v[80:83], a[232:235], v[208:211], v[80:83]\n"
      "ds_read_b64_tr_b16 a[220:221], v12 offset:26624\n"
      "ds_read_b64_tr_b16 a[222:223], v12 offset:27136\n"
      "v_mfma_f32_16x16x32_bf16 v[96:99], a[232:235], v[212:215], v[96:99]\n"
      "s_add_u32 m0, s95, 0\n"
      "v_add_u32 v4, s18, v4\n"
      "buffer_load_dwordx4 v4, s[28:31], 0 offen offset:0 lds\n"
      "v_mfma_f32_16x16x32_bf16 v[88:91], a[236:239], v[208:211], v[88:91]\n"
      "ds_read_b64_tr_b16 a[224:225], v11 offset:32768\n"
      "ds_read_b64_tr_b16 a[226:227], v11 offset:33280\n"
      "v_mfma_f32_16x16x32_bf16 v[104:107], a[236:239], v[212:215], v[104:107]\n"
      "v_mul_f32 v137, v129, v137\n"
      "v_fma_f32 v32, v32, v27, v27\n"
      "v_fma_f32 v33, v33, v27, v27\n"
      "v_rcp_f32 v32, v32\n"
      "v_mfma_f32_16x16x32_bf16 v[84:87], a[240:243], v[208:211], v[84:87]\n"
      "ds_read_b64_tr_b16 a[228:229], v11 offset:40960\n"
      "ds_read_b64_tr_b16 a[230:231], v11 offset:41472\n"
      "v_mfma_f32_16x16x32_bf16 v[100:103], a[240:243], v[212:215], v[100:103]\n"
      "buffer_load_dwordx4 v4, s[28:31], s65 offen offset:1024 lds\n"
      "v_rcp_f32 v33, v33\n"
      "v_mfma_f32_16x16x32_bf16 v[92:95], a[244:247], v[208:211], v[92:95]\n"
      "ds_read_b64_tr_b16 a[232:233], v12 offset:32768\n"
      "ds_read_b64_tr_b16 a[234:235], v12 offset:33280\n"
      "v_mfma_f32_16x16x32_bf16 v[108:111], a[244:247], v[212:215], v[108:111]\n"
      "v_mul_f32 v136, v136, v32\n"
      "v_mul_f32 v137, v137, v33\n"
      "v_cvt_pk_bf16_f32 v52, v136, v137\n"
      "v_mul_f32 v32, v130, v130\n"
      "v_mul_f32 v33, v131, v131\n"
      "s_waitcnt lgkmcnt(6)\n"
      "v_mfma_f32_16x16x32_bf16 v[80:83], a[248:251], v[216:219], v[80:83]\n"
      "ds_read_b64_tr_b16 a[236:237], v12 offset:40960\n"
      "ds_read_b64_tr_b16 a[238:239], v12 offset:41472\n"
      "v_mfma_f32_16x16x32_bf16 v[96:99], a[248:251], v[220:223], v[96:99]\n"
      "v_fma_f32 v32, v32, v26, v25\n"
      "v_fma_f32 v33, v33, v26, v25\n"
      "v_mul_f32 v32, v32, v130\n"
      "v_mfma_f32_16x16x32_bf16 v[88:91], a[252:255], v[216:219], v[88:91]\n"
      "ds_read_b64_tr_b16 a[240:241], v11 offset:34816\n"
      "ds_read_b64_tr_b16 a[242:243], v11 offset:35328\n"
      "v_mfma_f32_16x16x32_bf16 v[104:107], a[252:255], v[220:223], v[104:107]\n"
      "buffer_load_dwordx4 v4, s[28:31], s66 offen offset:2048 lds\n"
      "v_mul_f32 v33, v33, v131\n"
      "v_exp_f32 v32, v32\n"
      "v_mfma_f32_16x16x32_bf16 v[84:87], a[216:219], v[216:219], v[84:87]\n"
      "ds_read_b64_tr_b16 a[244:245], v11 offset:43008\n"
      "ds_read_b64_tr_b16 a[246:247], v11 offset:43520\n"
      "v_mfma_f32_16x16x32_bf16 v[100:103], a[216:219], v[220:223], v[100:103]\n"
      "v_exp_f32 v33, v33\n"
      "v_mul_f32 v138, v130, v138\n"
      "v_mul_f32 v139, v131, v139\n"
      "v_mfma_f32_16x16x32_bf16 v[92:95], a[220:223], v[216:219], v[92:95]\n"
      "ds_read_b64_tr_b16 a[248:249], v12 offset:34816\n"
      "ds_read_b64_tr_b16 a[250:251], v12 offset:35328\n"
      "v_mfma_f32_16x16x32_bf16 v[108:111], a[220:223], v[220:223], v[108:111]\n"
      "v_fma_f32 v32, v32, v27, v27\n"
      "v_fma_f32 v33, v33, v27, v27\n"
      "v_rcp_f32 v32, v32\n"
      "s_waitcnt lgkmcnt(6)\n"
      "v_mfma_f32_16x16x32_bf16 v[80:83], a[224:227], v[224:227], v[80:83]\n"
      "ds_read_b64_tr_b16 a[252:253], v12 offset:43008\n"
      "ds_read_b64_tr_b16 a[254:255], v12 offset:43520\n"
      "v_mfma_f32_16x16x32_bf16 v[96:99], a[224:227], v[228:231], v[96:99]\n"
      "buffer_load_dwordx4 v4, s[28:31], s67 offen offset:3072 lds\n"
      "v_rcp_f32 v33, v33\n"
      "v_mfma_f32_16x16x32_bf16 v[88:91], a[228:231], v[224:227], v[88:91]\n"
      "ds_read_b64_tr_b16 a[216:217], v11 offset:49152\n"
      "ds_read_b64_tr_b16 a[218:219], v11 offset:49664\n"
      "v_mfma_f32_16x16x32_bf16 v[104:107], a[228:231], v[228:231], v[104:107]\n"
      "v_mul_f32 v138, v138, v32\n"
      "v_mul_f32 v139, v139, v33\n"
      "v_cvt_pk_bf16_f32 v53, v138, v139\n"
      "v_mul_f32 v32, v132, v132\n"
      "v_mfma_f32_16x16x32_bf16 v[84:87], a[232:235], v[224:227], v[84:87]\n"
      "ds_read_b64_tr_b16 a[220:221], v11 offset:57344\n"
      "ds_read_b64_tr_b16 a[222:223], v11 offset:57856\n"
      "v_mfma_f32_16x16x32_bf16 v[100:103], a[232:235], v[228:231], v[100:103]\n"
      "buffer_store_dwordx4 v[72:75], v2, s[48:51], s75 offen offset:1024 sc1\n"
      "v_mul_f32 v33, v133, v133\n"
      "v_fma_f32 v32, v32, v26, v25\n"
      "v_fma_f32 v33, v33, v26, v25\n"
      "v_mfma_f32_16x16x32_bf16 v[92:95], a[236:239], v[224:227], v[92:95]\n"
      "ds_read_b64_tr_b16 a[224:225], v12 offset:49152\n"
      "ds_read_b64_tr_b16 a[226:227], v12 offset:49664\n"
      "v_mfma_f32_16x16x32_bf16 v[108:111], a[236:239], v[228:231], v[108:111]\n"
      "s_add_u32 m0, s95, 16384\n"
      "v_add_u32 v4, s19, v4\n"
      "buffer_load_dwordx4 v4, s[28:31], 0 offen offset:0 lds\n"
      "s_waitcnt lgkmcnt(6)\n"
      "v_mfma_f32_16x16x32_bf16 v[80:83], a[240:243], v[232:235], v[80:83]\n"
      "ds_read_b64_tr_b16 a[228:229], v12 offset:57344\n"
      "ds_read_b64_tr_b16 a[230:231], v12 offset:57856\n"
      "v_mfma_f32_16x16x32_bf16 v[96:99], a[240:243], v[236:239], v[96:99]\n"
      "v_mul_f32 v32, v32, v132\n"
      "v_mul_f32 v33, v33, v133\n"
      "v_exp_f32 v32, v32\n"
      "v_mfma_f32_16x16x32_bf16 v[88:91], a[244:247], v[232:235], v[88:91]\n"
      "ds_read_b64_tr_b16 a[232:233], v11 offset:51200\n"
      "ds_read_b64_tr_b16 a[234:235], v11 offset:51712\n"
      "v_mfma_f32_16x16x32_bf16 v[104:107], a[244:247], v[236:239], v[104:107]\n"
      "v_exp_f32 v33, v33\n"
      "v_mul_f32 v140, v132, v140\n"
      "v_mul_f32 v141, v133, v141\n"
      "v_mfma_f32_16x16x32_bf16 v[84:87], a[248:251], v[232:235], v[84:87]\n"
      "ds_read_b64_tr_b16 a[236:237], v11 offset:59392\n"
      "ds_read_b64_tr_b16 a[238:239], v11 offset:59904\n"
      "v_mfma_f32_16x16x32_bf16 v[100:103], a[248:251], v[236:239], v[100:103]\n"
      "buffer_load_dwordx4 v4, s[28:31], s65 offen offset:1024 lds\n"
      "v_fma_f32 v32, v32, v27, v27\n"
      "v_fma_f32 v33, v33, v27, v27\n"
      "v_mfma_f32_16x16x32_bf16 v[92:95], a[252:255], v[232:235], v[92:95]\n"
      "ds_read_b64_tr_b16 a[240:241], v12 offset:51200\n"
      "ds_read_b64_tr_b16 a[242:243], v12 offset:51712\n"
      "v_mfma_f32_16x16x32_bf16 v[108:111], a[252:255], v[236:239], v[108:111]\n"
      "v_rcp_f32 v32, v32\n"
      "v_rcp_f32 v33, v33\n"
      "v_mul_f32 v140, v140, v32\n"
      "s_waitcnt lgkmcnt(6)\n"
      "v_mfma_f32_16x16x32_bf16 v[80:83], a[216:219], v[240:243], v[80:83]\n"
      "ds_read_b64_tr_b16 a[244:245], v12 offset:59392\n"
      "ds_read_b64_tr_b16 a[246:247], v12 offset:59904\n"
      "v_mfma_f32_16x16x32_bf16 v[96:99], a[216:219], v[244:247], v[96:99]\n"
      "v_xor_b32 v9, 0x18000, v7\n"
      "v_mul_f32 v141, v141, v33\n"
      "v_cvt_pk_bf16_f32 v54, v140, v141\n"
      "v_mfma_f32_16x16x32_bf16 v[88:91], a[220:223], v[240:243], v[88:91]\n"
      "ds_read_b64_tr_b16 a[248:249], v9 offset:16384\n"
      "ds_read_b64_tr_b16 a[250:251], v9 offset:16896\n"
      "v_mfma_f32_16x16x32_bf16 v[104:107], a[220:223], v[244:247], v[104:107]\n"
      "buffer_load_dwordx4 v4, s[28:31], s66 offen offset:2048 lds\n"
      "v_mul_f32 v32, v134, v134\n"
      "v_mul_f32 v33, v135, v135\n"
      "v_fma_f32 v32, v32, v26, v25\n"
      "v_mfma_f32_16x16x32_bf16 v[84:87], a[224:227], v[240:243], v[84:87]\n"
      "ds_read_b64_tr_b16 a[252:253], v9 offset:24576\n"
      "ds_read_b64_tr_b16 a[254:255], v9 offset:25088\n"
      "v_mfma_f32_16x16x32_bf16 v[100:103], a[224:227], v[244:247], v[100:103]\n"
      "v_xor_b32 v10, 0x18020, v7\n"
      "v_fma_f32 v33, v33, v26, v25\n"
      "v_mul_f32 v32, v32, v134\n"
      "v_mul_f32 v33, v33, v135\n"
      "v_mfma_f32_16x16x32_bf16 v[92:95], a[228:231], v[240:243], v[92:95]\n"
      "ds_read_b64_tr_b16 a[216:217], v10 offset:16384\n"
      "ds_read_b64_tr_b16 a[218:219], v10 offset:16896\n"
      "v_mfma_f32_16x16x32_bf16 v[108:111], a[228:231], v[244:247], v[108:111]\n"
      "v_exp_f32 v32, v32\n"
      "v_exp_f32 v33, v33\n"
      "v_mul_f32 v142, v134, v142\n"
      "s_waitcnt lgkmcnt(6)\n"
      "v_mfma_f32_16x16x32_bf16 v[80:83], a[232:235], v[248:251], v[80:83]\n"
      "ds_read_b64_tr_b16 a[220:221], v10 offset:24576\n"
      "ds_read_b64_tr_b16 a[222:223], v10 offset:25088\n"
      "v_mfma_f32_16x16x32_bf16 v[96:99], a[232:235], v[252:255], v[96:99]\n"
      "buffer_load_dwordx4 v4, s[28:31], s67 offen offset:3072 lds\n"
      "v_mul_f32 v143, v135, v143\n"
      "v_mfma_f32_16x16x32_bf16 v[88:91], a[236:239], v[248:251], v[88:91]\n"
      "ds_read_b64_tr_b16 a[224:225], v9 offset:18432\n"
      "ds_read_b64_tr_b16 a[226:227], v9 offset:18944\n"
      "v_mfma_f32_16x16x32_bf16 v[104:107], a[236:239], v[252:255], v[104:107]\n"
      "v_fma_f32 v32, v32, v27, v27\n"
      "v_fma_f32 v33, v33, v27, v27\n"
      "v_rcp_f32 v32, v32\n"
      ".LPAD3:\n"
      "s_cbranch_execz .LRET\n"
      "v_mfma_f32_16x16x32_bf16 v[84:87], a[240:243], v[248:251], v[84:87]\n"
      "ds_read_b64_tr_b16 a[228:229], v9 offset:26624\n"
      "ds_read_b64_tr_b16 a[230:231], v9 offset:27136\n"
      "v_mfma_f32_16x16x32_bf16 v[100:103], a[240:243], v[252:255], v[100:103]\n"
      "v_rcp_f32 v33, v33\n"
      "v_mul_f32 v142, v142, v32\n"
      "v_mul_f32 v143, v143, v33\n"
      "v_cvt_pk_bf16_f32 v55, v142, v143\n"
      "v_mfma_f32_16x16x32_bf16 v[92:95], a[244:247], v[248:251], v[92:95]\n"
      "ds_read_b64_tr_b16 a[232:233], v10 offset:18432\n"
      "ds_read_b64_tr_b16 a[234:235], v10 offset:18944\n"
      "v_mfma_f32_16x16x32_bf16 v[108:111], a[244:247], v[252:255], v[108:111]\n"
      "buffer_store_dwordx4 v[52:55], v1, s[48:51], s75 offen offset:1024 sc1\n"
      "s_add_u32 m0, s95, 32768\n"
      "v_add_u32 v4, s8, v4\n"
      "buffer_load_dwordx4 v4, s[28:31], 0 offen offset:0 lds\n"
      "s_waitcnt lgkmcnt(6)\n"
      "v_mfma_f32_16x16x32_bf16 v[112:115], a[248:251], v[144:147], 0\n"
      "ds_read_b64_tr_b16 a[236:237], v10 offset:26624\n"
      "ds_read_b64_tr_b16 a[238:239], v10 offset:27136\n"
      "v_mfma_f32_16x16x32_bf16 v[128:131], a[248:251], v[148:151], 0\n"
      "v_mfma_f32_16x16x32_bf16 v[120:123], a[252:255], v[144:147], 0\n"
      "ds_read_b64_tr_b16 a[240:241], v9 offset:32768\n"
      "ds_read_b64_tr_b16 a[242:243], v9 offset:33280\n"
      "v_mfma_f32_16x16x32_bf16 v[136:139], a[252:255], v[148:151], 0\n"
      "buffer_load_dwordx4 v4, s[28:31], s65 offen offset:1024 lds\n"
      "v_mfma_f32_16x16x32_bf16 v[116:119], a[216:219], v[144:147], 0\n"
      "ds_read_b64_tr_b16 a[244:245], v9 offset:40960\n"
      "ds_read_b64_tr_b16 a[246:247], v9 offset:41472\n"
      "v_mfma_f32_16x16x32_bf16 v[132:135], a[216:219], v[148:151], 0\n"
      "v_mfma_f32_16x16x32_bf16 v[124:127], a[220:223], v[144:147], 0\n"
      "ds_read_b64_tr_b16 a[248:249], v10 offset:32768\n"
      "ds_read_b64_tr_b16 a[250:251], v10 offset:33280\n"
      "v_mfma_f32_16x16x32_bf16 v[140:143], a[220:223], v[148:151], 0\n"
      "v_mul_f32 v32, v80, v80\n"
      "v_mul_f32 v33, v81, v81\n"
      "v_fma_f32 v32, v32, v21, v20\n"
      "v_fma_f32 v33, v33, v21, v20\n"
      "v_mul_f32 v32, v32, v80\n"
      "s_waitcnt lgkmcnt(6)\n"
      "v_mfma_f32_16x16x32_bf16 v[112:115], a[224:227], v[152:155], v[112:115]\n"
      "ds_read_b64_tr_b16 a[252:253], v10 offset:40960\n"
      "ds_read_b64_tr_b16 a[254:255], v10 offset:41472\n"
      "v_mfma_f32_16x16x32_bf16 v[128:131], a[224:227], v[156:159], v[128:131]\n"
      "buffer_load_dwordx4 v4, s[28:31], s66 offen offset:2048 lds\n"
      "v_mul_f32 v33, v33, v81\n"
      "v_mfma_f32_16x16x32_bf16 v[120:123], a[228:231], v[152:155], v[120:123]\n"
      "ds_read_b64_tr_b16 a[216:217], v9 offset:34816\n"
      "ds_read_b64_tr_b16 a[218:219], v9 offset:35328\n"
      "v_mfma_f32_16x16x32_bf16 v[136:139], a[228:231], v[156:159], v[136:139]\n"
      "v_exp_f32 v32, v32\n"
      "v_exp_f32 v33, v33\n"
      "v_mul_f32 v88, v80, v88\n"
      "v_mfma_f32_16x16x32_bf16 v[116:119], a[232:235], v[152:155], v[116:119]\n"
      "ds_read_b64_tr_b16 a[220:221], v9 offset:43008\n"
      "ds_read_b64_tr_b16 a[222:223], v9 offset:43520\n"
      "v_mfma_f32_16x16x32_bf16 v[132:135], a[232:235], v[156:159], v[132:135]\n"
      "v_mul_f32 v89, v81, v89\n"
      "v_fma_f32 v32, v32, v22, v22\n"
      "v_fma_f32 v33, v33, v22, v22\n"
      "v_rcp_f32 v32, v32\n"
      "v_mfma_f32_16x16x32_bf16 v[124:127], a[236:239], v[152:155], v[124:127]\n"
      "ds_read_b64_tr_b16 a[224:225], v10 offset:34816\n"
      "ds_read_b64_tr_b16 a[226:227], v10 offset:35328\n"
      "v_mfma_f32_16x16x32_bf16 v[140:143], a[236:239], v[156:159], v[140:143]\n"
      "buffer_load_dwordx4 v4, s[28:31], s67 offen offset:3072 lds\n"
      "v_rcp_f32 v33, v33\n"
      "v_mul_f32 v88, v88, v32\n"
      "s_waitcnt lgkmcnt(6)\n"
      "v_mfma_f32_16x16x32_bf16 v[112:115], a[240:243], v[160:163], v[112:115]\n"
      "ds_read_b64_tr_b16 a[228:229], v10 offset:43008\n"
      "ds_read_b64_tr_b16 a[230:231], v10 offset:43520\n"
      "v_mfma_f32_16x16x32_bf16 v[128:131], a[240:243], v[164:167], v[128:131]\n"
      "v_mul_f32 v89, v89, v33\n"
      "v_cvt_pk_bf16_f32 v56, v88, v89\n"
      "v_mul_f32 v32, v82, v82\n"
      "v_mfma_f32_16x16x32_bf16 v[120:123], a[244:247], v[160:163], v[120:123]\n"
      "ds_read_b64_tr_b16 a[232:233], v9 offset:49152\n"
      "ds_read_b64_tr_b16 a[234:235], v9 offset:49664\n"
      "v_mfma_f32_16x16x32_bf16 v[136:139], a[244:247], v[164:167], v[136:139]\n"
      "v_mul_f32 v33, v83, v83\n"
      "v_fma_f32 v32, v32, v21, v20\n"
      "v_fma_f32 v33, v33, v21, v20\n"
      "v_mul_f32 v32, v32, v82\n"
      "v_mul_f32 v33, v33, v83\n"
      "v_mfma_f32_16x16x32_bf16 v[116:119], a[248:251], v[160:163], v[116:119]\n"
      "ds_read_b64_tr_b16 a[236:237], v9 offset:57344\n"
      "ds_read_b64_tr_b16 a[238:239], v9 offset:57856\n"
      "v_mfma_f32_16x16x32_bf16 v[132:135], a[248:251], v[164:167], v[132:135]\n"
      "buffer_store_dwordx4 v[72:75], v2, s[48:51], s75 offen offset:2048 sc1\n"
      "s_add_u32 m0, s95, 49152\n"
      "v_add_u32 v4, s9, v4\n"
      "buffer_load_dwordx4 v4, s[28:31], 0 offen offset:0 lds\n"
      "v_mfma_f32_16x16x32_bf16 v[124:127], a[252:255], v[160:163], v[124:127]\n"
      "ds_read_b64_tr_b16 a[240:241], v10 offset:49152\n"
      "ds_read_b64_tr_b16 a[242:243], v10 offset:49664\n"
      "v_mfma_f32_16x16x32_bf16 v[140:143], a[252:255], v[164:167], v[140:143]\n"
      "v_exp_f32 v32, v32\n"
      "v_exp_f32 v33, v33\n"
      "v_mul_f32 v90, v82, v90\n"
      "s_waitcnt lgkmcnt(6)\n"
      "v_mfma_f32_16x16x32_bf16 v[112:115], a[216:219], v[168:171], v[112:115]\n"
      "ds_read_b64_tr_b16 a[244:245], v10 offset:57344\n"
      "ds_read_b64_tr_b16 a[246:247], v10 offset:57856\n"
      "v_mfma_f32_16x16x32_bf16 v[128:131], a[216:219], v[172:175], v[128:131]\n"
      "v_mul_f32 v91, v83, v91\n"
      "v_fma_f32 v32, v32, v22, v22\n"
      "v_fma_f32 v33, v33, v22, v22\n"
      "v_mfma_f32_16x16x32_bf16 v[120:123], a[220:223], v[168:171], v[120:123]\n"
      "ds_read_b64_tr_b16 a[248:249], v9 offset:51200\n"
      "ds_read_b64_tr_b16 a[250:251], v9 offset:51712\n"
      "v_mfma_f32_16x16x32_bf16 v[136:139], a[220:223], v[172:175], v[136:139]\n"
      "buffer_load_dwordx4 v4, s[28:31], s65 offen offset:1024 lds\n"
      "v_rcp_f32 v32, v32\n"
      "v_rcp_f32 v33, v33\n"
      "v_mfma_f32_16x16x32_bf16 v[116:119], a[224:227], v[168:171], v[116:119]\n"
      "ds_read_b64_tr_b16 a[252:253], v9 offset:59392\n"
      "ds_read_b64_tr_b16 a[254:255], v9 offset:59904\n"
      "v_mfma_f32_16x16x32_bf16 v[132:135], a[224:227], v[172:175], v[132:135]\n"
      "v_mul_f32 v90, v90, v32\n"
      "v_mul_f32 v91, v91, v33\n"
      "v_cvt_pk_bf16_f32 v57, v90, v91\n"
      "v_mul_f32 v32, v84, v84\n"
      "v_mfma_f32_16x16x32_bf16 v[124:127], a[228:231], v[168:171], v[124:127]\n"
      "ds_read_b64_tr_b16 a[216:217], v10 offset:51200\n"
      "ds_read_b64_tr_b16 a[218:219], v10 offset:51712\n"
      "v_mfma_f32_16x16x32_bf16 v[140:143], a[228:231], v[172:175], v[140:143]\n"
      "v_mul_f32 v33, v85, v85\n"
      "v_fma_f32 v32, v32, v21, v20\n"
      "v_fma_f32 v33, v33, v21, v20\n"
      "v_mul_f32 v32, v32, v84\n"
      "v_mul_f32 v33, v33, v85\n"
      "s_waitcnt vmcnt(8) lgkmcnt(6)\n"
      "s_barrier\n"
      "v_mfma_f32_16x16x32_bf16 v[112:115], a[232:235], v[176:179], v[112:115]\n"
      "ds_read_b64_tr_b16 a[220:221], v10 offset:59392\n"
      "ds_read_b64_tr_b16 a[222:223], v10 offset:59904\n"
      "v_mfma_f32_16x16x32_bf16 v[128:131], a[232:235], v[180:183], v[128:131]\n"
      "buffer_load_dwordx4 v4, s[28:31], s66 offen offset:2048 lds\n"
      "v_xor_b32 v11, 0x0, v7\n"
      "v_mfma_f32_16x16x32_bf16 v[120:123], a[236:239], v[176:179], v[120:123]\n"
      "ds_read_b64_tr_b16 a[224:225], v11 offset:0\n"
      "ds_read_b64_tr_b16 a[226:227], v11 offset:512\n"
      "v_mfma_f32_16x16x32_bf16 v[136:139], a[236:239], v[180:183], v[136:139]\n"
      "v_exp_f32 v32, v32\n"
      "v_exp_f32 v33, v33\n"
      "v_mfma_f32_16x16x32_bf16 v[116:119], a[240:243], v[176:179], v[116:119]\n"
      "ds_read_b64_tr_b16 a[228:229], v11 offset:8192\n"
      "ds_read_b64_tr_b16 a[230:231], v11 offset:8704\n"
      "v_mfma_f32_16x16x32_bf16 v[132:135], a[240:243], v[180:183], v[132:135]\n"
      "v_xor_b32 v12, 0x20, v7\n"
      "v_mul_f32 v92, v84, v92\n"
      "v_mul_f32 v93, v85, v93\n"
      "v_fma_f32 v32, v32, v22, v22\n"
      "v_fma_f32 v33, v33, v22, v22\n"
      "v_mfma_f32_16x16x32_bf16 v[124:127], a[244:247], v[176:179], v[124:127]\n"
      "ds_read_b64_tr_b16 a[232:233], v12 offset:0\n"
      "ds_read_b64_tr_b16 a[234:235], v12 offset:512\n"
      "v_mfma_f32_16x16x32_bf16 v[140:143], a[244:247], v[180:183], v[140:143]\n"
      "buffer_load_dwordx4 v4, s[28:31], s67 offen offset:3072 lds\n"
      "v_rcp_f32 v32, v32\n"
      "s_waitcnt lgkmcnt(6)\n"
      "v_mfma_f32_16x16x32_bf16 v[112:115], a[248:251], v[184:187], v[112:115]\n"
      "ds_read_b64_tr_b16 a[236:237], v12 offset:8192\n"
      "ds_read_b64_tr_b16 a[238:239], v12 offset:8704\n"
      "v_mfma_f32_16x16x32_bf16 v[128:131], a[248:251], v[188:191], v[128:131]\n"
      "buffer_store_dwordx4 v[72:75], v2, s[48:51], s75 offen offset:3072 sc1\n"
      "v_rcp_f32 v33, v33\n"
      "v_mfma_f32_16x16x32_bf16 v[120:123], a[252:255], v[184:187], v[120:123]\n"
      "ds_read_b64_tr_b16 a[240:241], v11 offset:2048\n"
      "ds_read_b64_tr_b16 a[242:243], v11 offset:2560\n"
      "v_mfma_f32_16x16x32_bf16 v[136:139], a[252:255], v[188:191], v[136:139]\n"
      "s_waitcnt vmcnt(10)\n"
      "buffer_store_dwordx2 v[16:17], v13, s[48:51], s90 offen offset:0 sc1\n"
      "v_mul_f32 v92, v92, v32\n"
      "v_mul_f32 v93, v93, v33\n"
      "v_mfma_f32_16x16x32_bf16 v[116:119], a[216:219], v[184:187], v[116:119]\n"
      "ds_read_b64_tr_b16 a[244:245], v11 offset:10240\n"
      "ds_read_b64_tr_b16 a[246:247], v11 offset:10752\n"
      "v_mfma_f32_16x16x32_bf16 v[132:135], a[216:219], v[188:191], v[132:135]\n"
      "s_add_u32 m0, s95, 65536\n"
      "s_nop 0\n"
      "buffer_load_dwordx4 v5, s[36:39], s68 offen offset:0 lds\n"
      "v_cvt_pk_bf16_f32 v58, v92, v93\n"
      "v_mul_f32 v32, v86, v86\n"
      "v_mfma_f32_16x16x32_bf16 v[124:127], a[220:223], v[184:187], v[124:127]\n"
      "ds_read_b64_tr_b16 a[248:249], v12 offset:2048\n"
      "ds_read_b64_tr_b16 a[250:251], v12 offset:2560\n"
      "v_mfma_f32_16x16x32_bf16 v[140:143], a[220:223], v[188:191], v[140:143]\n"
      "v_mul_f32 v33, v87, v87\n"
      "v_fma_f32 v32, v32, v21, v20\n"
      "v_fma_f32 v33, v33, v21, v20\n"
      "v_mul_f32 v32, v32, v86\n"
      "s_waitcnt lgkmcnt(6)\n"
      "v_mfma_f32_16x16x32_bf16 v[112:115], a[224:227], v[192:195], v[112:115]\n"
      "ds_read_b64_tr_b16 a[252:253], v12 offset:10240\n"
      "ds_read_b64_tr_b16 a[254:255], v12 offset:10752\n"
      "v_mfma_f32_16x16x32_bf16 v[128:131], a[224:227], v[196:199], v[128:131]\n"
      "s_add_u32 m0, s95, 66560\n"
      "s_nop 0\n"
      "buffer_load_dwordx4 v5, s[36:39], s69 offen offset:0 lds\n"
      "v_mul_f32 v33, v33, v87\n"
      "v_mfma_f32_16x16x32_bf16 v[120:123], a[228:231], v[192:195], v[120:123]\n"
      "ds_read_b64_tr_b16 a[216:217], v11 offset:16384\n"
      "ds_read_b64_tr_b16 a[218:219], v11 offset:16896\n"
      "v_mfma_f32_16x16x32_bf16 v[136:139], a[228:231], v[196:199], v[136:139]\n"
      "v_exp_f32 v32, v32\n"
      "v_exp_f32 v33, v33\n"
      "v_mul_f32 v94, v86, v94\n"
      "v_mfma_f32_16x16x32_bf16 v[116:119], a[232:235], v[192:195], v[116:119]\n"
      "ds_read_b64_tr_b16 a[220:221], v11 offset:24576\n"
      "ds_read_b64_tr_b16 a[222:223], v11 offset:25088\n"
      "v_mfma_f32_16x16x32_bf16 v[132:135], a[232:235], v[196:199], v[132:135]\n"
      "s_add_u32 m0, s95, 67584\n"
      "s_nop 0\n"
      "buffer_load_dwordx4 v5, s[36:39], s70 offen offset:0 lds\n"
      "v_mul_f32 v95, v87, v95\n"
      "v_fma_f32 v32, v32, v22, v22\n"
      "v_mfma_f32_16x16x32_bf16 v[124:127], a[236:239], v[192:195], v[124:127]\n"
      "ds_read_b64_tr_b16 a[224:225], v12 offset:16384\n"
      "ds_read_b64_tr_b16 a[226:227], v12 offset:16896\n"
      "v_mfma_f32_16x16x32_bf16 v[140:143], a[236:239], v[196:199], v[140:143]\n"
      "v_fma_f32 v33, v33, v22, v22\n"
      "v_rcp_f32 v32, v32\n"
      "v_rcp_f32 v33, v33\n"
      "s_waitcnt lgkmcnt(6)\n"
      "v_mfma_f32_16x16x32_bf16 v[112:115], a[240:243], v[200:203], v[112:115]\n"
      "ds_read_b64_tr_b16 a[228:229], v12 offset:24576\n"
      "ds_read_b64_tr_b16 a[230:231], v12 offset:25088\n"
      "v_mfma_f32_16x16x32_bf16 v[128:131], a[240:243], v[204:207], v[128:131]\n"
      "v_mul_f32 v94, v94, v32\n"
      "v_mul_f32 v95, v95, v33\n"
      "v_cvt_pk_bf16_f32 v59, v94, v95\n"
      "v_mfma_f32_16x16x32_bf16 v[120:123], a[244:247], v[200:203], v[120:123]\n"
      "ds_read_b64_tr_b16 a[232:233], v11 offset:18432\n"
      "ds_read_b64_tr_b16 a[234:235], v11 offset:18944\n"
      "v_mfma_f32_16x16x32_bf16 v[136:139], a[244:247], v[204:207], v[136:139]\n"
      "buffer_store_dwordx4 v[56:59], v1, s[48:51], s75 offen offset:2048 sc1\n"
      "s_add_u32 m0, s95, 68608\n"
      "s_nop 0\n"
      "buffer_load_dwordx4 v5, s[36:39], s71 offen offset:0 lds\n"
      "v_mfma_f32_16x16x32_bf16 v[116:119], a[248:251], v[200:203], v[116:119]\n"
      "ds_read_b64_tr_b16 a[236:237], v11 offset:26624\n"
      "ds_read_b64_tr_b16 a[238:239], v11 offset:27136\n"
      "v_mfma_f32_16x16x32_bf16 v[132:135], a[248:251], v[204:207], v[132:135]\n"
      "v_mul_f32 v32, v96, v96\n"
      "v_mul_f32 v33, v97, v97\n"
      "v_fma_f32 v32, v32, v26, v25\n"
      "v_fma_f32 v33, v33, v26, v25\n"
      "v_mul_f32 v32, v32, v96\n"
      "v_mfma_f32_16x16x32_bf16 v[124:127], a[252:255], v[200:203], v[124:127]\n"
      "ds_read_b64_tr_b16 a[240:241], v12 offset:18432\n"
      "ds_read_b64_tr_b16 a[242:243], v12 offset:18944\n"
      "v_mfma_f32_16x16x32_bf16 v[140:143], a[252:255], v[204:207], v[140:143]\n"
      "s_add_u32 m0, s95, 81920\n"
      "s_nop 0\n"
      "buffer_load_dwordx4 v5, s[36:39], s72 offen offset:0 lds\n"
      "v_mul_f32 v33, v33, v97\n"
      "v_exp_f32 v32, v32\n"
      "s_waitcnt vmcnt(8) lgkmcnt(6)\n"
      "s_barrier\n"
      "v_mfma_f32_16x16x32_bf16 v[112:115], a[216:219], v[208:211], v[112:115]\n"
      "ds_read_b64_tr_b16 a[244:245], v12 offset:26624\n"
      "ds_read_b64_tr_b16 a[246:247], v12 offset:27136\n"
      "v_mfma_f32_16x16x32_bf16 v[128:131], a[216:219], v[212:215], v[128:131]\n"
      "v_exp_f32 v33, v33\n"
      "v_mfma_f32_16x16x32_bf16 v[120:123], a[220:223], v[208:211], v[120:123]\n"
      "ds_read_b64_tr_b16 a[248:249], v11 offset:32768\n"
      "ds_read_b64_tr_b16 a[250:251], v11 offset:33280\n"
      "v_mfma_f32_16x16x32_bf16 v[136:139], a[220:223], v[212:215], v[136:139]\n"
      "s_add_u32 m0, s95, 82944\n"
      "s_nop 0\n"
      "buffer_load_dwordx4 v5, s[36:39], s73 offen offset:0 lds\n"
      "v_mul_f32 v104, v96, v104\n"
      "v_mul_f32 v105, v97, v105\n"
      "v_mfma_f32_16x16x32_bf16 v[116:119], a[224:227], v[208:211], v[116:119]\n"
      "ds_read_b64_tr_b16 a[252:253], v11 offset:40960\n"
      "ds_read_b64_tr_b16 a[254:255], v11 offset:41472\n"
      "v_mfma_f32_16x16x32_bf16 v[132:135], a[224:227], v[212:215], v[132:135]\n"
      "buffer_store_dwordx4 v[72:75], v1, s[48:51], s91 offen offset:0 sc1\n"
      "v_fma_f32 v32, v32, v27, v27\n"
      "v_fma_f32 v33, v33, v27, v27\n"
      "v_rcp_f32 v32, v32\n"
      "v_mfma_f32_16x16x32_bf16 v[124:127], a[228:231], v[208:211], v[124:127]\n"
      "ds_read_b64_tr_b16 a[216:217], v12 offset:32768\n"
      "ds_read_b64_tr_b16 a[218:219], v12 offset:33280\n"
      "v_mfma_f32_16x16x32_bf16 v[140:143], a[228:231], v[212:215], v[140:143]\n"
      "v_rcp_f32 v33, v33\n"
      "v_mul_f32 v104, v104, v32\n"
      "v_mul_f32 v105, v105, v33\n"
      "s_waitcnt lgkmcnt(6)\n"
      "v_mfma_f32_16x16x32_bf16 v[112:115], a[232:235], v[216:219], v[112:115]\n"
      "ds_read_b64_tr_b16 a[220:221], v12 offset:40960\n"
      "ds_read_b64_tr_b16 a[222:223], v12 offset:41472\n"
      "v_mfma_f32_16x16x32_bf16 v[128:131], a[232:235], v[220:223], v[128:131]\n"
      "s_add_u32 m0, s95, 83968\n"
      "s_nop 0\n"
      "buffer_load_dwordx4 v5, s[36:39], s74 offen offset:0 lds\n"
      "v_cvt_pk_bf16_f32 v60, v104, v105\n"
      "v_mfma_f32_16x16x32_bf16 v[120:123], a[236:239], v[216:219], v[120:123]\n"
      "ds_read_b64_tr_b16 a[224:225], v11 offset:34816\n"
      "ds_read_b64_tr_b16 a[226:227], v11 offset:35328\n"
      "v_mfma_f32_16x16x32_bf16 v[136:139], a[236:239], v[220:223], v[136:139]\n"
      "v_mul_f32 v32, v98, v98\n"
      "v_mul_f32 v33, v99, v99\n"
      "v_fma_f32 v32, v32, v26, v25\n"
      "v_fma_f32 v33, v33, v26, v25\n"
      "v_mul_f32 v32, v32, v98\n"
      "v_mfma_f32_16x16x32_bf16 v[116:119], a[240:243], v[216:219], v[116:119]\n"
      "ds_read_b64_tr_b16 a[228:229], v11 offset:43008\n"
      "ds_read_b64_tr_b16 a[230:231], v11 offset:43520\n"
      "v_mfma_f32_16x16x32_bf16 v[132:135], a[240:243], v[220:223], v[132:135]\n"
      "s_add_u32 m0, s95, 81920\n"
      "s_add_u32 s35, s68, 25600\n"
      "buffer_load_dwordx4 v5, s[36:39], s35 offen offset:3072 lds\n"
      "v_mul_f32 v33, v33, v99\n"
      "v_mfma_f32_16x16x32_bf16 v[124:127], a[244:247], v[216:219], v[124:127]\n"
      "ds_read_b64_tr_b16 a[232:233], v12 offset:34816\n"
      "ds_read_b64_tr_b16 a[234:235], v12 offset:35328\n"
      "v_mfma_f32_16x16x32_bf16 v[140:143], a[244:247], v[220:223], v[140:143]\n"
      "v_exp_f32 v32, v32\n"
      "v_exp_f32 v33, v33\n"
      "v_mul_f32 v106, v98, v106\n"
      "s_waitcnt lgkmcnt(6)\n"
      "v_mfma_f32_16x16x32_bf16 v[112:115], a[248:251], v[224:227], v[112:115]\n"
      "ds_read_b64_tr_b16 a[236:237], v12 offset:43008\n"
      "ds_read_b64_tr_b16 a[238:239], v12 offset:43520\n"
      "v_mfma_f32_16x16x32_bf16 v[128:131], a[248:251], v[228:231], v[128:131]\n"
      "s_add_u32 m0, s95, 98304\n"
      "s_add_u32 s35, s68, 57344\n"
      "buffer_load_dwordx4 v5, s[36:39], s35 offen offset:0 lds\n"
      "v_mfma_f32_16x16x32_bf16 v[120:123], a[252:255], v[224:227], v[120:123]\n"
      "ds_read_b64_tr_b16 a[240:241], v11 offset:49152\n"
      "ds_read_b64_tr_b16 a[242:243], v11 offset:49664\n"
      "v_mfma_f32_16x16x32_bf16 v[136:139], a[252:255], v[228:231], v[136:139]\n"
      "v_mul_f32 v107, v99, v107\n"
      "v_fma_f32 v32, v32, v27, v27\n"
      "v_fma_f32 v33, v33, v27, v27\n"
      "v_rcp_f32 v32, v32\n"
      "v_mfma_f32_16x16x32_bf16 v[116:119], a[216:219], v[224:227], v[116:119]\n"
      "ds_read_b64_tr_b16 a[244:245], v11 offset:57344\n"
      "ds_read_b64_tr_b16 a[246:247], v11 offset:57856\n"
      "v_mfma_f32_16x16x32_bf16 v[132:135], a[216:219], v[228:231], v[132:135]\n"
      "v_rcp_f32 v33, v33\n"
      "v_mul_f32 v106, v106, v32\n"
      "v_mul_f32 v107, v107, v33\n"
      "v_cvt_pk_bf16_f32 v61, v106, v107\n"
      "v_mfma_f32_16x16x32_bf16 v[124:127], a[220:223], v[224:227], v[124:127]\n"
      "ds_read_b64_tr_b16 a[248:249], v12 offset:49152\n"
      "ds_read_b64_tr_b16 a[250:251], v12 offset:49664\n"
      "v_mfma_f32_16x16x32_bf16 v[140:143], a[220:223], v[228:231], v[140:143]\n"
      "s_add_u32 s35, s68, 84992\n"
      "buffer_load_dwordx4 v5, s[36:39], s35 offen offset:1024 lds\n"
      "v_mul_f32 v32, v100, v100\n"
      "v_mul_f32 v33, v101, v101\n"
      "s_waitcnt lgkmcnt(6)\n"
      "v_mfma_f32_16x16x32_bf16 v[112:115], a[224:227], v[232:235], v[112:115]\n"
      "ds_read_b64_tr_b16 a[252:253], v12 offset:57344\n"
      "ds_read_b64_tr_b16 a[254:255], v12 offset:57856\n"
      "v_mfma_f32_16x16x32_bf16 v[128:131], a[224:227], v[236:239], v[128:131]\n"
      "v_fma_f32 v32, v32, v26, v25\n"
      "v_fma_f32 v33, v33, v26, v25\n"
      "v_mul_f32 v32, v32, v100\n"
      "v_mul_f32 v33, v33, v101\n"
      "v_mfma_f32_16x16x32_bf16 v[120:123], a[228:231], v[232:235], v[120:123]\n"
      "ds_read_b64_tr_b16 a[216:217], v11 offset:51200\n"
      "ds_read_b64_tr_b16 a[218:219], v11 offset:51712\n"
      "v_mfma_f32_16x16x32_bf16 v[136:139], a[228:231], v[236:239], v[136:139]\n"
      "s_add_u32 s35, s69, 26624\n"
      "buffer_load_dwordx4 v5, s[36:39], s35 offen offset:2048 lds\n"
      "v_exp_f32 v32, v32\n"
      "v_mfma_f32_16x16x32_bf16 v[116:119], a[232:235], v[232:235], v[116:119]\n"
      "ds_read_b64_tr_b16 a[220:221], v11 offset:59392\n"
      "ds_read_b64_tr_b16 a[222:223], v11 offset:59904\n"
      "v_mfma_f32_16x16x32_bf16 v[132:135], a[232:235], v[236:239], v[132:135]\n"
      "v_exp_f32 v33, v33\n"
      "v_mul_f32 v108, v100, v108\n"
      "v_mul_f32 v109, v101, v109\n"
      "v_fma_f32 v32, v32, v27, v27\n"
      "v_mfma_f32_16x16x32_bf16 v[124:127], a[236:239], v[232:235], v[124:127]\n"
      "ds_read_b64_tr_b16 a[224:225], v12 offset:51200\n"
      "ds_read_b64_tr_b16 a[226:227], v12 offset:51712\n"
      "v_mfma_f32_16x16x32_bf16 v[140:143], a[236:239], v[236:239], v[140:143]\n"
      "s_add_u32 s35, s70, 25600\n"
      "buffer_load_dwordx4 v5, s[36:39], s35 offen offset:3072 lds\n"
      "v_fma_f32 v33, v33, v27, v27\n"
      "v_rcp_f32 v32, v32\n"
      "s_waitcnt lgkmcnt(6)\n"
      "v_mfma_f32_16x16x32_bf16 v[112:115], a[240:243], v[240:243], v[112:115]\n"
      "ds_read_b64_tr_b16 a[228:229], v12 offset:59392\n"
      "ds_read_b64_tr_b16 a[230:231], v12 offset:59904\n"
      "v_mfma_f32_16x16x32_bf16 v[128:131], a[240:243], v[244:247], v[128:131]\n"
      "buffer_store_dwordx4 v[72:75], v1, s[48:51], s91 offen offset:1024 sc1\n"
      "v_xor_b32 v9, 0x18040, v7\n"
      "v_mfma_f32_16x16x32_bf16 v[120:123], a[244:247], v[240:243], v[120:123]\n"
      "ds_read_b64_tr_b16 a[232:233], v9 offset:16384\n"
      "ds_read_b64_tr_b16 a[234:235], v9 offset:16896\n"
      "v_mfma_f32_16x16x32_bf16 v[136:139], a[244:247], v[244:247], v[136:139]\n"
      "v_rcp_f32 v33, v33\n"
      "v_mul_f32 v108, v108, v32\n"
      "v_mul_f32 v109, v109, v33\n"
      "v_cvt_pk_bf16_f32 v62, v108, v109\n"
      "v_mfma_f32_16x16x32_bf16 v[116:119], a[248:251], v[240:243], v[116:119]\n"
      "ds_read_b64_tr_b16 a[236:237], v9 offset:24576\n"
      "ds_read_b64_tr_b16 a[238:239], v9 offset:25088\n"
      "v_mfma_f32_16x16x32_bf16 v[132:135], a[248:251], v[244:247], v[132:135]\n"
      "v_xor_b32 v10, 0x18060, v7\n"
      "v_mul_f32 v32, v102, v102\n"
      "v_mul_f32 v33, v103, v103\n"
      "v_fma_f32 v32, v32, v26, v25\n"
      "v_fma_f32 v33, v33, v26, v25\n"
      "v_mfma_f32_16x16x32_bf16 v[124:127], a[252:255], v[240:243], v[124:127]\n"
      "ds_read_b64_tr_b16 a[240:241], v10 offset:16384\n"
      "ds_read_b64_tr_b16 a[242:243], v10 offset:16896\n"
      "v_mfma_f32_16x16x32_bf16 v[140:143], a[252:255], v[244:247], v[140:143]\n"
      "v_mul_f32 v32, v32, v102\n"
      "v_mul_f32 v33, v33, v103\n"
      "v_exp_f32 v32, v32\n"
      "v_exp_f32 v33, v33\n"
      "s_waitcnt lgkmcnt(6)\n"
      "v_mfma_f32_16x16x32_bf16 v[112:115], a[216:219], v[248:251], v[112:115]\n"
      "ds_read_b64_tr_b16 a[244:245], v10 offset:24576\n"
      "ds_read_b64_tr_b16 a[246:247], v10 offset:25088\n"
      "v_mfma_f32_16x16x32_bf16 v[128:131], a[216:219], v[252:255], v[128:131]\n"
      "v_mul_f32 v110, v102, v110\n"
      "v_mul_f32 v111, v103, v111\n"
      "v_fma_f32 v32, v32, v27, v27\n"
      "v_mfma_f32_16x16x32_bf16 v[120:123], a[220:223], v[248:251], v[120:123]\n"
      "ds_read_b64_tr_b16 a[248:249], v9 offset:18432\n"
      "ds_read_b64_tr_b16 a[250:251], v9 offset:18944\n"
      "v_mfma_f32_16x16x32_bf16 v[136:139], a[220:223], v[252:255], v[136:139]\n"
      "v_fma_f32 v33, v33, v27, v27\n"
      "v_rcp_f32 v32, v32\n"
      "v_rcp_f32 v33, v33\n"
      "v_mfma_f32_16x16x32_bf16 v[116:119], a[224:227], v[248:251], v[116:119]\n"
      "ds_read_b64_tr_b16 a[252:253], v9 offset:26624\n"
      "ds_read_b64_tr_b16 a[254:255], v9 offset:27136\n"
      "v_mfma_f32_16x16x32_bf16 v[132:135], a[224:227], v[252:255], v[132:135]\n"
      "v_mov_b32 v18, 0\n"
      "v_mov_b32 v19, 0\n"
      "v_mul_f32 v110, v110, v32\n"
      "v_mul_f32 v111, v111, v33\n"
      "v_cvt_pk_bf16_f32 v63, v110, v111\n"
      "v_mfma_f32_16x16x32_bf16 v[124:127], a[228:231], v[248:251], v[124:127]\n"
      "ds_read_b64_tr_b16 a[216:217], v10 offset:18432\n"
      "ds_read_b64_tr_b16 a[218:219], v10 offset:18944\n"
      "v_mfma_f32_16x16x32_bf16 v[140:143], a[228:231], v[252:255], v[140:143]\n"
      "buffer_store_dwordx4 v[60:63], v1, s[48:51], s75 offen offset:3072 sc1\n"
      "buffer_atomic_or_x2 v[18:19], v14, s[48:51], s89 offen offset:0 sc0 sc1\n"
      "s_waitcnt lgkmcnt(6)\n"
      "v_mfma_f32_16x16x32_bf16 v[80:83], a[232:235], v[144:147], 0\n"
      "ds_read_b64_tr_b16 a[220:221], v10 offset:26624\n"
      "ds_read_b64_tr_b16 a[222:223], v10 offset:27136\n"
      "v_mfma_f32_16x16x32_bf16 v[96:99], a[232:235], v[148:151], 0\n"
      "v_mfma_f32_16x16x32_bf16 v[88:91], a[236:239], v[144:147], 0\n"
      "ds_read_b64_tr_b16 a[224:225], v9 offset:32768\n"
      "ds_read_b64_tr_b16 a[226:227], v9 offset:33280\n"
      "v_mfma_f32_16x16x32_bf16 v[104:107], a[236:239], v[148:151], 0\n"
      "v_mfma_f32_16x16x32_bf16 v[84:87], a[240:243], v[144:147], 0\n"
      "ds_read_b64_tr_b16 a[228:229], v9 offset:40960\n"
      "ds_read_b64_tr_b16 a[230:231], v9 offset:41472\n"
      "v_mfma_f32_16x16x32_bf16 v[100:103], a[240:243], v[148:151], 0\n"
      "v_mfma_f32_16x16x32_bf16 v[92:95], a[244:247], v[144:147], 0\n"
      "ds_read_b64_tr_b16 a[232:233], v10 offset:32768\n"
      "ds_read_b64_tr_b16 a[234:235], v10 offset:33280\n"
      "v_mfma_f32_16x16x32_bf16 v[108:111], a[244:247], v[148:151], 0\n"
      "v_mul_f32 v32, v112, v112\n"
      "v_mul_f32 v33, v113, v113\n"
      "v_fma_f32 v32, v32, v21, v20\n"
      "v_fma_f32 v33, v33, v21, v20\n"
      "v_mul_f32 v32, v32, v112\n"
      "s_waitcnt lgkmcnt(6)\n"
      "v_mfma_f32_16x16x32_bf16 v[80:83], a[248:251], v[152:155], v[80:83]\n"
      "ds_read_b64_tr_b16 a[236:237], v10 offset:40960\n"
      "ds_read_b64_tr_b16 a[238:239], v10 offset:41472\n"
      "v_mfma_f32_16x16x32_bf16 v[96:99], a[248:251], v[156:159], v[96:99]\n"
      "s_mov_b32 s35, 28672\n"
      "buffer_load_dwordx4 a[184:187], v40, s[40:43], s35 offen\n"
      "v_mul_f32 v33, v33, v113\n"
      "v_mfma_f32_16x16x32_bf16 v[88:91], a[252:255], v[152:155], v[88:91]\n"
      "ds_read_b64_tr_b16 a[240:241], v9 offset:34816\n"
      "ds_read_b64_tr_b16 a[242:243], v9 offset:35328\n"
      "v_mfma_f32_16x16x32_bf16 v[104:107], a[252:255], v[156:159], v[104:107]\n"
      "v_exp_f32 v32, v32\n"
      "v_exp_f32 v33, v33\n"
      "v_mul_f32 v120, v112, v120\n"
      "v_mfma_f32_16x16x32_bf16 v[84:87], a[216:219], v[152:155], v[84:87]\n"
      "ds_read_b64_tr_b16 a[244:245], v9 offset:43008\n"
      "ds_read_b64_tr_b16 a[246:247], v9 offset:43520\n"
      "v_mfma_f32_16x16x32_bf16 v[100:103], a[216:219], v[156:159], v[100:103]\n"
      "s_mov_b32 s35, 57344\n"
      "buffer_load_dwordx4 a[188:191], v40, s[40:43], s35 offen\n"
      "v_mul_f32 v121, v113, v121\n"
      "v_fma_f32 v32, v32, v22, v22\n"
      "v_mfma_f32_16x16x32_bf16 v[92:95], a[220:223], v[152:155], v[92:95]\n"
      "ds_read_b64_tr_b16 a[248:249], v10 offset:34816\n"
      "ds_read_b64_tr_b16 a[250:251], v10 offset:35328\n"
      "v_mfma_f32_16x16x32_bf16 v[108:111], a[220:223], v[156:159], v[108:111]\n"
      "v_fma_f32 v33, v33, v22, v22\n"
      "v_rcp_f32 v32, v32\n"
      "v_rcp_f32 v33, v33\n"
      "s_waitcnt lgkmcnt(6)\n"
      "v_mfma_f32_16x16x32_bf16 v[80:83], a[224:227], v[160:163], v[80:83]\n"
      "ds_read_b64_tr_b16 a[252:253], v10 offset:43008\n"
      "ds_read_b64_tr_b16 a[254:255], v10 offset:43520\n"
      "v_mfma_f32_16x16x32_bf16 v[96:99], a[224:227], v[164:167], v[96:99]\n"
      "v_mul_f32 v120, v120, v32\n"
      "v_mul_f32 v121, v121, v33\n"
      "v_cvt_pk_bf16_f32 v64, v120, v121\n"
      "v_mul_f32 v32, v114, v114\n"
      "v_mfma_f32_16x16x32_bf16 v[88:91], a[228:231], v[160:163], v[88:91]\n"
      "ds_read_b64_tr_b16 a[216:217], v9 offset:49152\n"
      "ds_read_b64_tr_b16 a[218:219], v9 offset:49664\n"
      "v_mfma_f32_16x16x32_bf16 v[104:107], a[228:231], v[164:167], v[104:107]\n"
      "s_mov_b32 s35, 86016\n"
      "buffer_load_dwordx4 a[192:195], v40, s[40:43], s35 offen\n"
      "v_mul_f32 v33, v115, v115\n"
      "v_mfma_f32_16x16x32_bf16 v[84:87], a[232:235], v[160:163], v[84:87]\n"
      "ds_read_b64_tr_b16 a[220:221], v9 offset:57344\n"
      "ds_read_b64_tr_b16 a[222:223], v9 offset:57856\n"
      "v_mfma_f32_16x16x32_bf16 v[100:103], a[232:235], v[164:167], v[100:103]\n"
      "v_fma_f32 v32, v32, v21, v20\n"
      "v_fma_f32 v33, v33, v21, v20\n"
      "v_mul_f32 v32, v32, v114\n"
      "v_mul_f32 v33, v33, v115\n"
      "v_exp_f32 v32, v32\n"
      "v_mfma_f32_16x16x32_bf16 v[92:95], a[236:239], v[160:163], v[92:95]\n"
      "ds_read_b64_tr_b16 a[224:225], v10 offset:49152\n"
      "ds_read_b64_tr_b16 a[226:227], v10 offset:49664\n"
      "v_mfma_f32_16x16x32_bf16 v[108:111], a[236:239], v[164:167], v[108:111]\n"
      "v_exp_f32 v33, v33\n"
      "v_mul_f32 v122, v114, v122\n"
      "v_mul_f32 v123, v115, v123\n"
      "s_waitcnt lgkmcnt(6)\n"
      "v_mfma_f32_16x16x32_bf16 v[80:83], a[240:243], v[168:171], v[80:83]\n"
      "ds_read_b64_tr_b16 a[228:229], v10 offset:57344\n"
      "ds_read_b64_tr_b16 a[230:231], v10 offset:57856\n"
      "v_mfma_f32_16x16x32_bf16 v[96:99], a[240:243], v[172:175], v[96:99]\n"
      "s_mov_b32 s35, 114688\n"
      "buffer_load_dwordx4 a[196:199], v40, s[40:43], s35 offen\n"
      "v_fma_f32 v32, v32, v22, v22\n"
      "v_mfma_f32_16x16x32_bf16 v[88:91], a[244:247], v[168:171], v[88:91]\n"
      "ds_read_b64_tr_b16 a[232:233], v9 offset:51200\n"
      "ds_read_b64_tr_b16 a[234:235], v9 offset:51712\n"
      "v_mfma_f32_16x16x32_bf16 v[104:107], a[244:247], v[172:175], v[104:107]\n"
      "v_fma_f32 v33, v33, v22, v22\n"
      "v_rcp_f32 v32, v32\n"
      "v_rcp_f32 v33, v33\n"
      "v_mfma_f32_16x16x32_bf16 v[84:87], a[248:251], v[168:171], v[84:87]\n"
      "ds_read_b64_tr_b16 a[236:237], v9 offset:59392\n"
      "ds_read_b64_tr_b16 a[238:239], v9 offset:59904\n"
      "v_mfma_f32_16x16x32_bf16 v[100:103], a[248:251], v[172:175], v[100:103]\n"
      "s_mov_b32 s35, 143360\n"
      "buffer_load_dwordx4 a[200:203], v40, s[40:43], s35 offen\n"
      "v_mul_f32 v122, v122, v32\n"
      "v_mul_f32 v123, v123, v33\n"
      "v_mfma_f32_16x16x32_bf16 v[92:95], a[252:255], v[168:171], v[92:95]\n"
      "ds_read_b64_tr_b16 a[240:241], v10 offset:51200\n"
      "ds_read_b64_tr_b16 a[242:243], v10 offset:51712\n"
      "v_mfma_f32_16x16x32_bf16 v[108:111], a[252:255], v[172:175], v[108:111]\n"
      "v_cvt_pk_bf16_f32 v65, v122, v123\n"
      "v_mul_f32 v32, v116, v116\n"
      "v_mul_f32 v33, v117, v117\n"
      "v_fma_f32 v32, v32, v21, v20\n"
      "v_fma_f32 v33, v33, v21, v20\n"
      "s_waitcnt lgkmcnt(6)\n"
      "v_mfma_f32_16x16x32_bf16 v[80:83], a[216:219], v[176:179], v[80:83]\n"
      "ds_read_b64_tr_b16 a[244:245], v10 offset:59392\n"
      "ds_read_b64_tr_b16 a[246:247], v10 offset:59904\n"
      "v_mfma_f32_16x16x32_bf16 v[96:99], a[216:219], v[180:183], v[96:99]\n"
      "v_xor_b32 v11, 0x40, v7\n"
      "v_mul_f32 v32, v32, v116\n"
      "v_mul_f32 v33, v33, v117\n"
      "v_mfma_f32_16x16x32_bf16 v[88:91], a[220:223], v[176:179], v[88:91]\n"
      "ds_read_b64_tr_b16 a[248:249], v11 offset:0\n"
      "ds_read_b64_tr_b16 a[250:251], v11 offset:512\n"
      "v_mfma_f32_16x16x32_bf16 v[104:107], a[220:223], v[180:183], v[104:107]\n"
      "s_mov_b32 s35, 172032\n"
      "buffer_load_dwordx4 a[204:207], v40, s[40:43], s35 offen\n"
      "v_exp_f32 v32, v32\n"
      "v_mfma_f32_16x16x32_bf16 v[84:87], a[224:227], v[176:179], v[84:87]\n"
      "ds_read_b64_tr_b16 a[252:253], v11 offset:8192\n"
      "ds_read_b64_tr_b16 a[254:255], v11 offset:8704\n"
      "v_mfma_f32_16x16x32_bf16 v[100:103], a[224:227], v[180:183], v[100:103]\n"
      "v_xor_b32 v12, 0x60, v7\n"
      "v_exp_f32 v33, v33\n"
      "v_mul_f32 v124, v116, v124\n"
      "v_mul_f32 v125, v117, v125\n"
      "v_fma_f32 v32, v32, v22, v22\n"
      "v_mfma_f32_16x16x32_bf16 v[92:95], a[228:231], v[176:179], v[92:95]\n"
      "ds_read_b64_tr_b16 a[216:217], v12 offset:0\n"
      "ds_read_b64_tr_b16 a[218:219], v12 offset:512\n"
      "v_mfma_f32_16x16x32_bf16 v[108:111], a[228:231], v[180:183], v[108:111]\n"
      "s_waitcnt vmcnt(7)\n"
      "buffer_store_dwordx2 v[16:17], v13, s[48:51], s90 offen offset:1024 sc1\n"
      "s_nop 0\n"
      ".LPAD4:\n"
      "s_cbranch_execz .LRET\n"
      "v_fma_f32 v33, v33, v22, v22\n"
      "s_waitcnt lgkmcnt(6)\n"
      "v_mfma_f32_16x16x32_bf16 v[80:83], a[232:235], v[184:187], v[80:83]\n"
      "ds_read_b64_tr_b16 a[220:221], v12 offset:8192\n"
      "ds_read_b64_tr_b16 a[222:223], v12 offset:8704\n"
      "v_mfma_f32_16x16x32_bf16 v[96:99], a[232:235], v[188:191], v[96:99]\n"
      "v_rcp_f32 v32, v32\n"
      "v_rcp_f32 v33, v33\n"
      "v_mul_f32 v124, v124, v32\n"
      "v_mfma_f32_16x16x32_bf16 v[88:91], a[236:239], v[184:187], v[88:91]\n"
      "ds_read_b64_tr_b16 a[224:225], v11 offset:2048\n"
      "ds_read_b64_tr_b16 a[226:227], v11 offset:2560\n"
      "v_mfma_f32_16x16x32_bf16 v[104:107], a[236:239], v[188:191], v[104:107]\n"
      "s_waitcnt vmcnt(7)\n"
      "v_cmp_ne_u64 vcc, s[96:97], v[18:19]\n"
      "s_and_b64 vcc, vcc, 63\n"
      "s_cbranch_scc1 .LS_H0\n"
      ".LK_H0:\n"
      "v_mul_f32 v125, v125, v33\n"
      "v_mfma_f32_16x16x32_bf16 v[84:87], a[240:243], v[184:187], v[84:87]\n"
      "ds_read_b64_tr_b16 a[228:229], v11 offset:10240\n"
      "ds_read_b64_tr_b16 a[230:231], v11 offset:10752\n"
      "v_mfma_f32_16x16x32_bf16 v[100:103], a[240:243], v[188:191], v[100:103]\n"
      "buffer_load_dwordx4 a[0:3], v1, s[48:51], s76 offen offset:0 sc1\n"
      "v_cvt_pk_bf16_f32 v66, v124, v125\n"
      "v_mul_f32 v32, v118, v118\n"
      "v_mfma_f32_16x16x32_bf16 v[92:95], a[244:247], v[184:187], v[92:95]\n"
      "ds_read_b64_tr_b16 a[232:233], v12 offset:2048\n"
      "ds_read_b64_tr_b16 a[234:235], v12 offset:2560\n"
      "v_mfma_f32_16x16x32_bf16 v[108:111], a[244:247], v[188:191], v[108:111]\n"
      "buffer_load_dwordx4 a[4:7], v1, s[48:51], s76 offen offset:1024 sc1\n"
      "v_mul_f32 v33, v119, v119\n"
      "v_fma_f32 v32, v32, v21, v20\n"
      "v_fma_f32 v33, v33, v21, v20\n"
      "s_waitcnt lgkmcnt(6)\n"
      "v_mfma_f32_16x16x32_bf16 v[80:83], a[248:251], v[192:195], v[80:83]\n"
      "ds_read_b64_tr_b16 a[236:237], v12 offset:10240\n"
      "ds_read_b64_tr_b16 a[238:239], v12 offset:10752\n"
      "v_mfma_f32_16x16x32_bf16 v[96:99], a[248:251], v[196:199], v[96:99]\n"
      "v_mul_f32 v32, v32, v118\n"
      "v_mul_f32 v33, v33, v119\n"
      "v_exp_f32 v32, v32\n"
      "v_mfma_f32_16x16x32_bf16 v[88:91], a[252:255], v[192:195], v[88:91]\n"
      "ds_read_b64_tr_b16 a[240:241], v11 offset:16384\n"
      "ds_read_b64_tr_b16 a[242:243], v11 offset:16896\n"
      "v_mfma_f32_16x16x32_bf16 v[104:107], a[252:255], v[196:199], v[104:107]\n"
      "buffer_load_dwordx4 a[8:11], v1, s[48:51], s77 offen offset:0 sc1\n"
      "v_exp_f32 v33, v33\n"
      "v_mul_f32 v126, v118, v126\n"
      "v_mfma_f32_16x16x32_bf16 v[84:87], a[216:219], v[192:195], v[84:87]\n"
      "ds_read_b64_tr_b16 a[244:245], v11 offset:24576\n"
      "ds_read_b64_tr_b16 a[246:247], v11 offset:25088\n"
      "v_mfma_f32_16x16x32_bf16 v[100:103], a[216:219], v[196:199], v[100:103]\n"
      "v_mul_f32 v127, v119, v127\n"
      "v_fma_f32 v32, v32, v22, v22\n"
      "v_fma_f32 v33, v33, v22, v22\n"
      "v_rcp_f32 v32, v32\n"
      "v_mfma_f32_16x16x32_bf16 v[92:95], a[220:223], v[192:195], v[92:95]\n"
      "ds_read_b64_tr_b16 a[248:249], v12 offset:16384\n"
      "ds_read_b64_tr_b16 a[250:251], v12 offset:16896\n"
      "v_mfma_f32_16x16x32_bf16 v[108:111], a[220:223], v[196:199], v[108:111]\n"
      "buffer_load_dwordx4 a[12:15], v1, s[48:51], s77 offen offset:1024 sc1\n"
      "v_rcp_f32 v33, v33\n"
      "v_mul_f32 v126, v126, v32\n"
      "s_waitcnt lgkmcnt(6)\n"
      "v_mfma_f32_16x16x32_bf16 v[80:83], a[224:227], v[200:203], v[80:83]\n"
      "ds_read_b64_tr_b16 a[252:253], v12 offset:24576\n"
      "ds_read_b64_tr_b16 a[254:255], v12 offset:25088\n"
      "v_mfma_f32_16x16x32_bf16 v[96:99], a[224:227], v[204:207], v[96:99]\n"
      "buffer_load_dwordx4 a[16:19], v1, s[48:51], s78 offen offset:0 sc1\n"
      "v_mul_f32 v127, v127, v33\n"
      "v_cvt_pk_bf16_f32 v67, v126, v127\n"
      "v_mfma_f32_16x16x32_bf16 v[88:91], a[228:231], v[200:203], v[88:91]\n"
      "ds_read_b64_tr_b16 a[216:217], v11 offset:18432\n"
      "ds_read_b64_tr_b16 a[218:219], v11 offset:18944\n"
      "v_mfma_f32_16x16x32_bf16 v[104:107], a[228:231], v[204:207], v[104:107]\n"
      "buffer_store_dwordx4 v[64:67], v2, s[48:51], s75 offen offset:0 sc1\n"
      "v_mul_f32 v32, v128, v128\n"
      "v_mul_f32 v33, v129, v129\n"
      "v_fma_f32 v32, v32, v26, v25\n"
      "v_mfma_f32_16x16x32_bf16 v[84:87], a[232:235], v[200:203], v[84:87]\n"
      "ds_read_b64_tr_b16 a[220:221], v11 offset:26624\n"
      "ds_read_b64_tr_b16 a[222:223], v11 offset:27136\n"
      "v_mfma_f32_16x16x32_bf16 v[100:103], a[232:235], v[204:207], v[100:103]\n"
      "buffer_load_dwordx4 a[20:23], v1, s[48:51], s78 offen offset:1024 sc1\n"
      "v_fma_f32 v33, v33, v26, v25\n"
      "v_mul_f32 v32, v32, v128\n"
      "v_mul_f32 v33, v33, v129\n"
      "v_mfma_f32_16x16x32_bf16 v[92:95], a[236:239], v[200:203], v[92:95]\n"
      "ds_read_b64_tr_b16 a[224:225], v12 offset:18432\n"
      "ds_read_b64_tr_b16 a[226:227], v12 offset:18944\n"
      "v_mfma_f32_16x16x32_bf16 v[108:111], a[236:239], v[204:207], v[108:111]\n"
      "v_exp_f32 v32, v32\n"
      "v_exp_f32 v33, v33\n"
      "v_mul_f32 v136, v128, v136\n"
      "v_mul_f32 v137, v129, v137\n"
      "s_waitcnt lgkmcnt(6)\n"
      "v_mfma_f32_16x16x32_bf16 v[80:83], a[240:243], v[208:211], v[80:83]\n"
      "ds_read_b64_tr_b16 a[228:229], v12 offset:26624\n"
      "ds_read_b64_tr_b16 a[230:231], v12 offset:27136\n"
      "v_mfma_f32_16x16x32_bf16 v[96:99], a[240:243], v[212:215], v[96:99]\n"
      "buffer_load_dwordx4 a[24:27], v1, s[48:51], s79 offen offset:0 sc1\n"
      "v_fma_f32 v32, v32, v27, v27\n"
      "v_fma_f32 v33, v33, v27, v27\n"
      "v_mfma_f32_16x16x32_bf16 v[88:91], a[244:247], v[208:211], v[88:91]\n"
      "ds_read_b64_tr_b16 a[232:233], v11 offset:32768\n"
      "ds_read_b64_tr_b16 a[234:235], v11 offset:33280\n"
      "v_mfma_f32_16x16x32_bf16 v[104:107], a[244:247], v[212:215], v[104:107]\n"
      "buffer_load_dwordx4 a[28:31], v1, s[48:51], s79 offen offset:1024 sc1\n"
      "v_rcp_f32 v32, v32\n"
      "v_mfma_f32_16x16x32_bf16 v[84:87], a[248:251], v[208:211], v[84:87]\n"
      "ds_read_b64_tr_b16 a[236:237], v11 offset:40960\n"
      "ds_read_b64_tr_b16 a[238:239], v11 offset:41472\n"
      "v_mfma_f32_16x16x32_bf16 v[100:103], a[248:251], v[212:215], v[100:103]\n"
      "v_rcp_f32 v33, v33\n"
      "v_mul_f32 v136, v136, v32\n"
      "v_mul_f32 v137, v137, v33\n"
      "v_cvt_pk_bf16_f32 v68, v136, v137\n"
      "v_mul_f32 v32, v130, v130\n"
      "v_mfma_f32_16x16x32_bf16 v[92:95], a[252:255], v[208:211], v[92:95]\n"
      "ds_read_b64_tr_b16 a[240:241], v12 offset:32768\n"
      "ds_read_b64_tr_b16 a[242:243], v12 offset:33280\n"
      "v_mfma_f32_16x16x32_bf16 v[108:111], a[252:255], v[212:215], v[108:111]\n"
      "buffer_load_dwordx4 a[32:35], v1, s[48:51], s80 offen offset:0 sc1\n"
      "v_mul_f32 v33, v131, v131\n"
      "v_fma_f32 v32, v32, v26, v25\n"
      "v_fma_f32 v33, v33, v26, v25\n"
      "s_waitcnt lgkmcnt(6)\n"
      "v_mfma_f32_16x16x32_bf16 v[80:83], a[216:219], v[216:219], v[80:83]\n"
      "ds_read_b64_tr_b16 a[244:245], v12 offset:40960\n"
      "ds_read_b64_tr_b16 a[246:247], v12 offset:41472\n"
      "v_mfma_f32_16x16x32_bf16 v[96:99], a[216:219], v[220:223], v[96:99]\n"
      "v_mul_f32 v32, v32, v130\n"
      "v_mul_f32 v33, v33, v131\n"
      "v_exp_f32 v32, v32\n"
      "v_mfma_f32_16x16x32_bf16 v[88:91], a[220:223], v[216:219], v[88:91]\n"
      "ds_read_b64_tr_b16 a[248:249], v11 offset:34816\n"
      "ds_read_b64_tr_b16 a[250:251], v11 offset:35328\n"
      "v_mfma_f32_16x16x32_bf16 v[104:107], a[220:223], v[220:223], v[104:107]\n"
      "buffer_load_dwordx4 a[36:39], v1, s[48:51], s80 offen offset:1024 sc1\n"
      "v_exp_f32 v33, v33\n"
      "v_mul_f32 v138, v130, v138\n"
      "v_mul_f32 v139, v131, v139\n"
      "v_mfma_f32_16x16x32_bf16 v[84:87], a[224:227], v[216:219], v[84:87]\n"
      "ds_read_b64_tr_b16 a[252:253], v11 offset:43008\n"
      "ds_read_b64_tr_b16 a[254:255], v11 offset:43520\n"
      "v_mfma_f32_16x16x32_bf16 v[100:103], a[224:227], v[220:223], v[100:103]\n"
      "buffer_load_dwordx4 a[40:43], v1, s[48:51], s81 offen offset:0 sc1\n"
      "v_fma_f32 v32, v32, v27, v27\n"
      "v_fma_f32 v33, v33, v27, v27\n"
      "v_mfma_f32_16x16x32_bf16 v[92:95], a[228:231], v[216:219], v[92:95]\n"
      "ds_read_b64_tr_b16 a[216:217], v12 offset:34816\n"
      "ds_read_b64_tr_b16 a[218:219], v12 offset:35328\n"
      "v_mfma_f32_16x16x32_bf16 v[108:111], a[228:231], v[220:223], v[108:111]\n"
      "v_rcp_f32 v32, v32\n"
      "v_rcp_f32 v33, v33\n"
      "v_mul_f32 v138, v138, v32\n"
      "v_mul_f32 v139, v139, v33\n"
      "s_waitcnt lgkmcnt(6)\n"
      "v_mfma_f32_16x16x32_bf16 v[80:83], a[232:235], v[224:227], v[80:83]\n"
      "ds_read_b64_tr_b16 a[220:221], v12 offset:43008\n"
      "ds_read_b64_tr_b16 a[222:223], v12 offset:43520\n"
      "v_mfma_f32_16x16x32_bf16 v[96:99], a[232:235], v[228:231], v[96:99]\n"
      "buffer_load_dwordx4 a[44:47], v1, s[48:51], s81 offen offset:1024 sc1\n"
      "v_cvt_pk_bf16_f32 v69, v138, v139\n"
      "v_mul_f32 v32, v132, v132\n"
      "v_mfma_f32_16x16x32_bf16 v[88:91], a[236:239], v[224:227], v[88:91]\n"
      "ds_read_b64_tr_b16 a[224:225], v11 offset:49152\n"
      "ds_read_b64_tr_b16 a[226:227], v11 offset:49664\n"
      "v_mfma_f32_16x16x32_bf16 v[104:107], a[236:239], v[228:231], v[104:107]\n"
      "v_mul_f32 v33, v133, v133\n"
      "v_fma_f32 v32, v32, v26, v25\n"
      "v_fma_f32 v33, v33, v26, v25\n"
      "v_mul_f32 v32, v32, v132\n"
      "v_mul_f32 v33, v33, v133\n"
      "v_mfma_f32_16x16x32_bf16 v[84:87], a[240:243], v[224:227], v[84:87]\n"
      "ds_read_b64_tr_b16 a[228:229], v11 offset:57344\n"
      "ds_read_b64_tr_b16 a[230:231], v11 offset:57856\n"
      "v_mfma_f32_16x16x32_bf16 v[100:103], a[240:243], v[228:231], v[100:103]\n"
      "v_exp_f32 v32, v32\n"
      "v_exp_f32 v33, v33\n"
      "v_mul_f32 v140, v132, v140\n"
      "v_mfma_f32_16x16x32_bf16 v[92:95], a[244:247], v[224:227], v[92:95]\n"
      "ds_read_b64_tr_b16 a[232:233], v12 offset:49152\n"
      "ds_read_b64_tr_b16 a[234:235], v12 offset:49664\n"
      "v_mfma_f32_16x16x32_bf16 v[108:111], a[244:247], v[228:231], v[108:111]\n"
      "v_mul_f32 v141, v133, v141\n"
      "v_fma_f32 v32, v32, v27, v27\n"
      "v_fma_f32 v33, v33, v27, v27\n"
      "v_rcp_f32 v32, v32\n"
      "s_waitcnt vmcnt(23) lgkmcnt(6)\n"
      "s_barrier\n"
      "v_mfma_f32_16x16x32_bf16 v[80:83], a[248:251], v[232:235], v[80:83]\n"
      "ds_read_b64_tr_b16 a[236:237], v12 offset:57344\n"
      "ds_read_b64_tr_b16 a[238:239], v12 offset:57856\n"
      "v_mfma_f32_16x16x32_bf16 v[96:99], a[248:251], v[236:239], v[96:99]\n"
      "s_add_u32 m0, s95, 114688\n"
      "s_add_u32 s35, s71, 28672\n"
      "buffer_load_dwordx4 v5, s[36:39], s35 offen offset:0 lds\n"
      "v_mfma_f32_16x16x32_bf16 v[88:91], a[252:255], v[232:235], v[88:91]\n"
      "ds_read_b64_tr_b16 a[240:241], v11 offset:51200\n"
      "ds_read_b64_tr_b16 a[242:243], v11 offset:51712\n"
      "v_mfma_f32_16x16x32_bf16 v[104:107], a[252:255], v[236:239], v[104:107]\n"
      "s_add_u32 s35, s72, 27648\n"
      "buffer_load_dwordx4 v5, s[36:39], s35 offen offset:1024 lds\n"
      "v_rcp_f32 v33, v33\n"
      "v_mul_f32 v140, v140, v32\n"
      "v_mfma_f32_16x16x32_bf16 v[84:87], a[216:219], v[232:235], v[84:87]\n"
      "ds_read_b64_tr_b16 a[244:245], v11 offset:59392\n"
      "ds_read_b64_tr_b16 a[246:247], v11 offset:59904\n"
      "v_mfma_f32_16x16x32_bf16 v[100:103], a[216:219], v[236:239], v[100:103]\n"
      "v_mul_f32 v141, v141, v33\n"
      "v_cvt_pk_bf16_f32 v70, v140, v141\n"
      "v_mul_f32 v32, v134, v134\n"
      "v_mul_f32 v33, v135, v135\n"
      "v_fma_f32 v32, v32, v26, v25\n"
      "v_mfma_f32_16x16x32_bf16 v[92:95], a[220:223], v[232:235], v[92:95]\n"
      "ds_read_b64_tr_b16 a[248:249], v12 offset:51200\n"
      "ds_read_b64_tr_b16 a[250:251], v12 offset:51712\n"
      "v_mfma_f32_16x16x32_bf16 v[108:111], a[220:223], v[236:239], v[108:111]\n"
      "s_add_u32 s35, s73, 26624\n"
      "buffer_load_dwordx4 v5, s[36:39], s35 offen offset:2048 lds\n"
      "v_fma_f32 v33, v33, v26, v25\n"
      "v_mul_f32 v32, v32, v134\n"
      "s_waitcnt lgkmcnt(6)\n"
      "v_mfma_f32_16x16x32_bf16 v[80:83], a[224:227], v[240:243], v[80:83]\n"
      "ds_read_b64_tr_b16 a[252:253], v12 offset:59392\n"
      "ds_read_b64_tr_b16 a[254:255], v12 offset:59904\n"
      "v_mfma_f32_16x16x32_bf16 v[96:99], a[224:227], v[244:247], v[96:99]\n"
      "v_xor_b32 v9, 0x10000, v8\n"
      "v_mul_f32 v33, v33, v135\n"
      "v_exp_f32 v32, v32\n"
      "v_mfma_f32_16x16x32_bf16 v[88:91], a[228:231], v[240:243], v[88:91]\n"
      "ds_read_b64_tr_b16 a[216:217], v9 offset:0\n"
      "ds_read_b64_tr_b16 a[218:219], v9 offset:8192\n"
      "v_mfma_f32_16x16x32_bf16 v[104:107], a[228:231], v[244:247], v[104:107]\n"
      "s_add_u32 s35, s74, 25600\n"
      "buffer_load_dwordx4 v5, s[36:39], s35 offen offset:3072 lds\n"
      "v_xor_b32 v10, 0x10040, v8\n"
      "v_mov_b32 v18, 0\n"
      "v_mov_b32 v19, 0\n"
      "v_mfma_f32_16x16x32_bf16 v[84:87], a[232:235], v[240:243], v[84:87]\n"
      "ds_read_b64_tr_b16 a[220:221], v10 offset:0\n"
      "ds_read_b64_tr_b16 a[222:223], v10 offset:8192\n"
      "v_mfma_f32_16x16x32_bf16 v[100:103], a[232:235], v[244:247], v[100:103]\n"
      "s_add_u32 m0, s95, 131072\n"
      "s_add_u32 s35, s69, 57344\n"
      "buffer_load_dwordx4 v5, s[36:39], s35 offen offset:0 lds\n"
      "buffer_atomic_or_x2 v[18:19], v14, s[48:51], s89 offen offset:1024 sc0 sc1\n"
      "v_xor_b32 v11, 0x10020, v8\n"
      "v_mfma_f32_16x16x32_bf16 v[92:95], a[236:239], v[240:243], v[92:95]\n"
      "ds_read_b64_tr_b16 a[224:225], v11 offset:0\n"
      "ds_read_b64_tr_b16 a[226:227], v11 offset:8192\n"
      "v_mfma_f32_16x16x32_bf16 v[108:111], a[236:239], v[244:247], v[108:111]\n"
      "v_xor_b32 v12, 0x10060, v8\n"
      "v_exp_f32 v33, v33\n"
      "v_mul_f32 v142, v134, v142\n"
      "v_mul_f32 v143, v135, v143\n"
      "s_waitcnt lgkmcnt(6)\n"
      "v_mfma_f32_16x16x32_bf16 v[80:83], a[240:243], v[248:251], v[80:83]\n"
      "ds_read_b64_tr_b16 a[228:229], v12 offset:0\n"
      "ds_read_b64_tr_b16 a[230:231], v12 offset:8192\n"
      "v_mfma_f32_16x16x32_bf16 v[96:99], a[240:243], v[252:255], v[96:99]\n"
      "s_add_u32 s35, s70, 56320\n"
      "buffer_load_dwordx4 v5, s[36:39], s35 offen offset:1024 lds\n"
      "v_fma_f32 v32, v32, v27, v27\n"
      "v_fma_f32 v33, v33, v27, v27\n"
      "v_mfma_f32_16x16x32_bf16 v[88:91], a[244:247], v[248:251], v[88:91]\n"
      "ds_read_b64_tr_b16 a[232:233], v9 offset:1024\n"
      "ds_read_b64_tr_b16 a[234:235], v9 offset:9216\n"
      "v_mfma_f32_16x16x32_bf16 v[104:107], a[244:247], v[252:255], v[104:107]\n"
      "v_rcp_f32 v32, v32\n"
      "v_rcp_f32 v33, v33\n"
      "v_mul_f32 v142, v142, v32\n"
      "v_mfma_f32_16x16x32_bf16 v[84:87], a[248:251], v[248:251], v[84:87]\n"
      "ds_read_b64_tr_b16 a[236:237], v10 offset:1024\n"
      "ds_read_b64_tr_b16 a[238:239], v10 offset:9216\n"
      "v_mfma_f32_16x16x32_bf16 v[100:103], a[248:251], v[252:255], v[100:103]\n"
      "s_add_u32 s35, s71, 55296\n"
      "buffer_load_dwordx4 v5, s[36:39], s35 offen offset:2048 lds\n"
      "v_mul_f32 v143, v143, v33\n"
      "v_cvt_pk_bf16_f32 v71, v142, v143\n"
      "v_mfma_f32_16x16x32_bf16 v[92:95], a[252:255], v[248:251], v[92:95]\n"
      "ds_read_b64_tr_b16 a[240:241], v11 offset:1024\n"
      "ds_read_b64_tr_b16 a[242:243], v11 offset:9216\n"
      "v_mfma_f32_16x16x32_bf16 v[108:111], a[252:255], v[252:255], v[108:111]\n"
      "buffer_store_dwordx4 v[68:71], v2, s[48:51], s75 offen offset:1024 sc1\n"
      "s_add_u32 s35, s72, 54272\n"
      "buffer_load_dwordx4 v5, s[36:39], s35 offen offset:3072 lds\n"
      "v_mfma_f32_4x4x4_16b_bf16 v[128:131], v[28:29], a[208:209], 0\n"
      "v_mfma_f32_4x4x4_16b_bf16 v[132:135], v[28:29], a[210:211], 0\n"
      "v_mfma_f32_4x4x4_16b_bf16 v[136:139], v[28:29], a[212:213], 0\n"
      "v_mfma_f32_4x4x4_16b_bf16 v[140:143], v[28:29], a[214:215], 0\n"
      "s_waitcnt lgkmcnt(6)\n"
      "v_mfma_f32_16x16x32_bf16 v[112:115], a[216:219], v[48:51], 0\n"
      "ds_read_b64_tr_b16 a[244:245], v12 offset:1024\n"
      "ds_read_b64_tr_b16 a[246:247], v12 offset:9216\n"
      "v_mfma_f32_16x16x32_bf16 v[120:123], a[216:219], v[52:55], 0\n"
      "v_mfma_f32_16x16x32_bf16 v[128:131], a[220:223], v[48:51], v[128:131]\n"
      "ds_read_b64_tr_b16 a[248:249], v9 offset:2048\n"
      "ds_read_b64_tr_b16 a[250:251], v9 offset:10240\n"
      "v_mfma_f32_16x16x32_bf16 v[136:139], a[220:223], v[52:55], v[136:139]\n"
      "s_add_u32 m0, s95, 147456\n"
      "s_add_u32 s35, s73, 57344\n"
      "buffer_load_dwordx4 v5, s[36:39], s35 offen offset:0 lds\n"
      "v_mul_f32 v32, v80, v80\n"
      "v_mul_f32 v33, v81, v81\n"
      "v_fma_f32 v32, v32, v21, v20\n"
      "v_fma_f32 v33, v33, v21, v20\n"
      "v_mul_f32 v32, v32, v80\n"
      "v_mul_f32 v33, v33, v81\n"
      "v_exp_f32 v32, v32\n"
      "v_mfma_f32_16x16x32_bf16 v[116:119], a[224:227], v[48:51], 0\n"
      "ds_read_b64_tr_b16 a[252:253], v10 offset:2048\n"
      "ds_read_b64_tr_b16 a[254:255], v10 offset:10240\n"
      "v_mfma_f32_16x16x32_bf16 v[124:127], a[224:227], v[52:55], 0\n"
      "v_exp_f32 v33, v33\n"
      "v_mul_f32 v88, v80, v88\n"
      "v_mul_f32 v89, v81, v89\n"
      "v_fma_f32 v32, v32, v22, v22\n"
      "v_fma_f32 v33, v33, v22, v22\n"
      "v_rcp_f32 v32, v32\n"
      "v_rcp_f32 v33, v33\n"
      "v_mfma_f32_16x16x32_bf16 v[132:135], a[228:231], v[48:51], v[132:135]\n"
      "ds_read_b64_tr_b16 a[216:217], v11 offset:2048\n"
      "ds_read_b64_tr_b16 a[218:219], v11 offset:10240\n"
      "v_mfma_f32_16x16x32_bf16 v[140:143], a[228:231], v[52:55], v[140:143]\n"
      "s_add_u32 s35, s74, 56320\n"
      "buffer_load_dwordx4 v5, s[36:39], s35 offen offset:1024 lds\n"
      "v_mul_f32 v88, v88, v32\n"
      "v_mul_f32 v89, v89, v33\n"
      "v_cvt_pk_bf16_f32 v72, v88, v89\n"
      "v_mul_f32 v32, v82, v82\n"
      "v_mul_f32 v33, v83, v83\n"
      "v_fma_f32 v32, v32, v21, v20\n"
      "v_fma_f32 v33, v33, v21, v20\n"
      "v_mul_f32 v32, v32, v82\n"
      "s_waitcnt vmcnt(12) lgkmcnt(6)\n"
      "v_mfma_f32_16x16x32_bf16 v[112:115], a[232:235], a[0:3], v[112:115]\n"
      "ds_read_b64_tr_b16 a[220:221], v12 offset:2048\n"
      "ds_read_b64_tr_b16 a[222:223], v12 offset:10240\n"
      "v_mfma_f32_16x16x32_bf16 v[120:123], a[232:235], a[4:7], v[120:123]\n"
      "s_add_u32 s35, s69, 83968\n"
      "buffer_load_dwordx4 v5, s[36:39], s35 offen offset:2048 lds\n"
      "v_mul_f32 v33, v33, v83\n"
      "v_exp_f32 v32, v32\n"
      "v_exp_f32 v33, v33\n"
      "v_mul_f32 v90, v82, v90\n"
      "v_mul_f32 v91, v83, v91\n"
      "v_mfma_f32_16x16x32_bf16 v[128:131], a[236:239], a[0:3], v[128:131]\n"
      "ds_read_b64_tr_b16 a[224:225], v9 offset:3072\n"
      "ds_read_b64_tr_b16 a[226:227], v9 offset:11264\n"
      "v_mfma_f32_16x16x32_bf16 v[136:139], a[236:239], a[4:7], v[136:139]\n"
      "v_fma_f32 v32, v32, v22, v22\n"
      "v_fma_f32 v33, v33, v22, v22\n"
      "v_rcp_f32 v32, v32\n"
      "v_rcp_f32 v33, v33\n"
      "v_mul_f32 v90, v90, v32\n"
      "v_mul_f32 v91, v91, v33\n"
      "v_cvt_pk_bf16_f32 v73, v90, v91\n"
      "v_mul_f32 v32, v84, v84\n"
      "v_mul_f32 v33, v85, v85\n"
      "v_mfma_f32_16x16x32_bf16 v[116:119], a[240:243], a[0:3], v[116:119]\n"
      "ds_read_b64_tr_b16 a[228:229], v10 offset:3072\n"
      "ds_read_b64_tr_b16 a[230:231], v10 offset:11264\n"
      "v_mfma_f32_16x16x32_bf16 v[124:127], a[240:243], a[4:7], v[124:127]\n"
      "s_add_u32 s35, s70, 82944\n"
      "buffer_load_dwordx4 v5, s[36:39], s35 offen offset:3072 lds\n"
      "v_fma_f32 v32, v32, v21, v20\n"
      "v_fma_f32 v33, v33, v21, v20\n"
      "v_mul_f32 v32, v32, v84\n"
      "v_mul_f32 v33, v33, v85\n"
      "v_exp_f32 v32, v32\n"
      "v_exp_f32 v33, v33\n"
      "v_mfma_f32_16x16x32_bf16 v[132:135], a[244:247], a[0:3], v[132:135]\n"
      "ds_read_b64_tr_b16 a[232:233], v11 offset:3072\n"
      "ds_read_b64_tr_b16 a[234:235], v11 offset:11264\n"
      "v_mfma_f32_16x16x32_bf16 v[140:143], a[244:247], a[4:7], v[140:143]\n"
      "v_mul_f32 v92, v84, v92\n"
      "v_mul_f32 v93, v85, v93\n"
      "v_fma_f32 v32, v32, v22, v22\n"
      "v_fma_f32 v33, v33, v22, v22\n"
      "v_rcp_f32 v32, v32\n"
      "v_rcp_f32 v33, v33\n"
      "v_mul_f32 v92, v92, v32\n"
      "v_mul_f32 v93, v93, v33\n"
      "v_cvt_pk_bf16_f32 v74, v92, v93\n"
      "s_waitcnt lgkmcnt(6)\n"
      "v_mfma_f32_16x16x32_bf16 v[112:115], a[248:251], a[8:11], v[112:115]\n"
      "ds_read_b64_tr_b16 a[236:237], v12 offset:3072\n"
      "ds_read_b64_tr_b16 a[238:239], v12 offset:11264\n"
      "v_mfma_f32_16x16x32_bf16 v[120:123], a[248:251], a[12:15], v[120:123]\n"
      "v_mul_f32 v32, v86, v86\n"
      "v_mul_f32 v33, v87, v87\n"
      "v_fma_f32 v32, v32, v21, v20\n"
      "v_fma_f32 v33, v33, v21, v20\n"
      "v_mul_f32 v32, v32, v86\n"
      "v_mul_f32 v33, v33, v87\n"
      "v_exp_f32 v32, v32\n"
      "v_exp_f32 v33, v33\n"
      "v_mfma_f32_16x16x32_bf16 v[128:131], a[252:255], a[8:11], v[128:131]\n"
      "ds_read_b64_tr_b16 a[240:241], v9 offset:16384\n"
      "ds_read_b64_tr_b16 a[242:243], v9 offset:24576\n"
      "v_mfma_f32_16x16x32_bf16 v[136:139], a[252:255], a[12:15], v[136:139]\n"
      "v_mul_f32 v94, v86, v94\n"
      "v_mul_f32 v95, v87, v95\n"
      "v_fma_f32 v32, v32, v22, v22\n"
      "v_fma_f32 v33, v33, v22, v22\n"
      "v_rcp_f32 v32, v32\n"
      "v_rcp_f32 v33, v33\n"
      "v_mul_f32 v94, v94, v32\n"
      "v_mul_f32 v95, v95, v33\n"
      "v_cvt_pk_bf16_f32 v75, v94, v95\n"
      "v_mfma_f32_16x16x32_bf16 v[116:119], a[216:219], a[8:11], v[116:119]\n"
      "ds_read_b64_tr_b16 a[244:245], v10 offset:16384\n"
      "ds_read_b64_tr_b16 a[246:247], v10 offset:24576\n"
      "v_mfma_f32_16x16x32_bf16 v[124:127], a[216:219], a[12:15], v[124:127]\n"
      "buffer_store_dwordx4 v[72:75], v2, s[48:51], s75 offen offset:2048 sc1\n"
      "v_mul_f32 v32, v96, v96\n"
      "v_mul_f32 v33, v97, v97\n"
      "v_fma_f32 v32, v32, v26, v25\n"
      "v_fma_f32 v33, v33, v26, v25\n"
      "v_mul_f32 v32, v32, v96\n"
      "v_mul_f32 v33, v33, v97\n"
      "v_exp_f32 v32, v32\n"
      "v_mfma_f32_16x16x32_bf16 v[132:135], a[220:223], a[8:11], v[132:135]\n"
      "ds_read_b64_tr_b16 a[248:249], v11 offset:16384\n"
      "ds_read_b64_tr_b16 a[250:251], v11 offset:24576\n"
      "v_mfma_f32_16x16x32_bf16 v[140:143], a[220:223], a[12:15], v[140:143]\n"
      "v_exp_f32 v33, v33\n"
      "v_mul_f32 v104, v96, v104\n"
      "v_mul_f32 v105, v97, v105\n"
      "v_fma_f32 v32, v32, v27, v27\n"
      "v_fma_f32 v33, v33, v27, v27\n"
      "v_rcp_f32 v32, v32\n"
      "v_rcp_f32 v33, v33\n"
      "v_mul_f32 v104, v104, v32\n"
      "v_mul_f32 v105, v105, v33\n"
      "s_waitcnt lgkmcnt(6)\n"
      "v_mfma_f32_16x16x32_bf16 v[112:115], a[224:227], a[16:19], v[112:115]\n"
      "ds_read_b64_tr_b16 a[252:253], v12 offset:16384\n"
      "ds_read_b64_tr_b16 a[254:255], v12 offset:24576\n"
      "v_mfma_f32_16x16x32_bf16 v[120:123], a[224:227], a[20:23], v[120:123]\n"
      "v_cvt_pk_bf16_f32 v76, v104, v105\n"
      "v_mul_f32 v32, v98, v98\n"
      "v_mul_f32 v33, v99, v99\n"
      "v_fma_f32 v32, v32, v26, v25\n"
      "v_fma_f32 v33, v33, v26, v25\n"
      "v_mul_f32 v32, v32, v98\n"
      "v_mul_f32 v33, v33, v99\n"
      "v_exp_f32 v32, v32\n"
      "v_mfma_f32_16x16x32_bf16 v[128:131], a[228:231], a[16:19], v[128:131]\n"
      "ds_read_b64_tr_b16 a[216:217], v9 offset:17408\n"
      "ds_read_b64_tr_b16 a[218:219], v9 offset:25600\n"
      "v_mfma_f32_16x16x32_bf16 v[136:139], a[228:231], a[20:23], v[136:139]\n"
      "v_exp_f32 v33, v33\n"
      "v_mul_f32 v106, v98, v106\n"
      "v_mul_f32 v107, v99, v107\n"
      "v_fma_f32 v32, v32, v27, v27\n"
      "v_fma_f32 v33, v33, v27, v27\n"
      "v_rcp_f32 v32, v32\n"
      "v_rcp_f32 v33, v33\n"
      "v_mul_f32 v106, v106, v32\n"
      "v_mul_f32 v107, v107, v33\n"
      "v_mfma_f32_16x16x32_bf16 v[116:119], a[232:235], a[16:19], v[116:119]\n"
      "ds_read_b64_tr_b16 a[220:221], v10 offset:17408\n"
      "ds_read_b64_tr_b16 a[222:223], v10 offset:25600\n"
      "v_mfma_f32_16x16x32_bf16 v[124:127], a[232:235], a[20:23], v[124:127]\n"
      "s_waitcnt vmcnt(9)\n"
      "v_cmp_ne_u64 vcc, s[96:97], v[18:19]\n"
      "s_and_b64 vcc, vcc, 63\n"
      "s_cbranch_scc1 .LS_H1\n"
      ".LK_H1:\n"
      "v_cvt_pk_bf16_f32 v77, v106, v107\n"
      "v_mul_f32 v32, v100, v100\n"
      "v_mul_f32 v33, v101, v101\n"
      "v_fma_f32 v32, v32, v26, v25\n"
      "v_fma_f32 v33, v33, v26, v25\n"
      "v_mul_f32 v32, v32, v100\n"
      "v_mfma_f32_16x16x32_bf16 v[132:135], a[236:239], a[16:19], v[132:135]\n"
      "ds_read_b64_tr_b16 a[224:225], v11 offset:17408\n"
      "ds_read_b64_tr_b16 a[226:227], v11 offset:25600\n"
      "v_mfma_f32_16x16x32_bf16 v[140:143], a[236:239], a[20:23], v[140:143]\n"
      "buffer_load_dwordx4 a[48:51], v1, s[48:51], s76 offen offset:2048 sc1\n"
      "v_mul_f32 v33, v33, v101\n"
      "v_exp_f32 v32, v32\n"
      "v_exp_f32 v33, v33\n"
      "v_mul_f32 v108, v100, v108\n"
      "v_mul_f32 v109, v101, v109\n"
      "v_fma_f32 v32, v32, v27, v27\n"
      "v_fma_f32 v33, v33, v27, v27\n"
      "s_waitcnt lgkmcnt(6)\n"
      "v_mfma_f32_16x16x32_bf16 v[112:115], a[240:243], a[24:27], v[112:115]\n"
      "ds_read_b64_tr_b16 a[228:229], v12 offset:17408\n"
      "ds_read_b64_tr_b16 a[230:231], v12 offset:25600\n"
      "v_mfma_f32_16x16x32_bf16 v[120:123], a[240:243], a[28:31], v[120:123]\n"
      "buffer_load_dwordx4 a[52:55], v1, s[48:51], s76 offen offset:3072 sc1\n"
      "v_rcp_f32 v32, v32\n"
      "v_rcp_f32 v33, v33\n"
      "v_mul_f32 v108, v108, v32\n"
      "v_mul_f32 v109, v109, v33\n"
      "v_cvt_pk_bf16_f32 v78, v108, v109\n"
      "v_mul_f32 v32, v102, v102\n"
      "v_mfma_f32_16x16x32_bf16 v[128:131], a[244:247], a[24:27], v[128:131]\n"
      "ds_read_b64_tr_b16 a[232:233], v9 offset:18432\n"
      "ds_read_b64_tr_b16 a[234:235], v9 offset:26624\n"
      "v_mfma_f32_16x16x32_bf16 v[136:139], a[244:247], a[28:31], v[136:139]\n"
      "v_mul_f32 v33, v103, v103\n"
      "v_fma_f32 v32, v32, v26, v25\n"
      "v_fma_f32 v33, v33, v26, v25\n"
      "v_mul_f32 v32, v32, v102\n"
      "v_mul_f32 v33, v33, v103\n"
      "v_exp_f32 v32, v32\n"
      "v_exp_f32 v33, v33\n"
      "v_mul_f32 v110, v102, v110\n"
      "v_mul_f32 v111, v103, v111\n"
      "v_mfma_f32_16x16x32_bf16 v[116:119], a[248:251], a[24:27], v[116:119]\n"
      "ds_read_b64_tr_b16 a[236:237], v10 offset:18432\n"
      "ds_read_b64_tr_b16 a[238:239], v10 offset:26624\n"
      "v_mfma_f32_16x16x32_bf16 v[124:127], a[248:251], a[28:31], v[124:127]\n"
      "buffer_load_dwordx4 a[56:59], v1, s[48:51], s77 offen offset:2048 sc1\n"
      "v_fma_f32 v32, v32, v27, v27\n"
      "v_fma_f32 v33, v33, v27, v27\n"
      "v_rcp_f32 v32, v32\n"
      "v_rcp_f32 v33, v33\n"
      "v_mul_f32 v110, v110, v32\n"
      "v_mul_f32 v111, v111, v33\n"
      "v_cvt_pk_bf16_f32 v79, v110, v111\n"
      "v_mfma_f32_16x16x32_bf16 v[132:135], a[252:255], a[24:27], v[132:135]\n"
      "ds_read_b64_tr_b16 a[240:241], v11 offset:18432\n"
      "ds_read_b64_tr_b16 a[242:243], v11 offset:26624\n"
      "v_mfma_f32_16x16x32_bf16 v[140:143], a[252:255], a[28:31], v[140:143]\n"
      "buffer_store_dwordx4 v[76:79], v2, s[48:51], s75 offen offset:3072 sc1\n"
      "s_waitcnt lgkmcnt(6)\n"
      "v_mfma_f32_16x16x32_bf16 v[112:115], a[216:219], a[32:35], v[112:115]\n"
      "ds_read_b64_tr_b16 a[244:245], v12 offset:18432\n"
      "ds_read_b64_tr_b16 a[246:247], v12 offset:26624\n"
      "v_mfma_f32_16x16x32_bf16 v[120:123], a[216:219], a[36:39], v[120:123]\n"
      "buffer_load_dwordx4 a[60:63], v1, s[48:51], s77 offen offset:3072 sc1\n"
      "v_mfma_f32_16x16x32_bf16 v[128:131], a[220:223], a[32:35], v[128:131]\n"
      "ds_read_b64_tr_b16 a[248:249], v9 offset:19456\n"
      "ds_read_b64_tr_b16 a[250:251], v9 offset:27648\n"
      "v_mfma_f32_16x16x32_bf16 v[136:139], a[220:223], a[36:39], v[136:139]\n"
      "v_mfma_f32_16x16x32_bf16 v[116:119], a[224:227], a[32:35], v[116:119]\n"
      "ds_read_b64_tr_b16 a[252:253], v10 offset:19456\n"
      "ds_read_b64_tr_b16 a[254:255], v10 offset:27648\n"
      "v_mfma_f32_16x16x32_bf16 v[124:127], a[224:227], a[36:39], v[124:127]\n"
      "buffer_load_dwordx4 a[64:67], v1, s[48:51], s78 offen offset:2048 sc1\n"
      "v_mfma_f32_16x16x32_bf16 v[132:135], a[228:231], a[32:35], v[132:135]\n"
      "ds_read_b64_tr_b16 a[216:217], v11 offset:19456\n"
      "ds_read_b64_tr_b16 a[218:219], v11 offset:27648\n"
      "v_mfma_f32_16x16x32_bf16 v[140:143], a[228:231], a[36:39], v[140:143]\n"
      "s_waitcnt lgkmcnt(6)\n"
      "v_mfma_f32_16x16x32_bf16 v[112:115], a[232:235], a[40:43], v[112:115]\n"
      "ds_read_b64_tr_b16 a[220:221], v12 offset:19456\n"
      "ds_read_b64_tr_b16 a[222:223], v12 offset:27648\n"
      "v_mfma_f32_16x16x32_bf16 v[120:123], a[232:235], a[44:47], v[120:123]\n"
      "buffer_load_dwordx4 a[68:71], v1, s[48:51], s78 offen offset:3072 sc1\n"
      "v_mfma_f32_16x16x32_bf16 v[128:131], a[236:239], a[40:43], v[128:131]\n"
      "ds_read_b64_tr_b16 a[224:225], v9 offset:32768\n"
      "ds_read_b64_tr_b16 a[226:227], v9 offset:40960\n"
      "v_mfma_f32_16x16x32_bf16 v[136:139], a[236:239], a[44:47], v[136:139]\n"
      "v_mfma_f32_16x16x32_bf16 v[116:119], a[240:243], a[40:43], v[116:119]\n"
      "ds_read_b64_tr_b16 a[228:229], v10 offset:32768\n"
      "ds_read_b64_tr_b16 a[230:231], v10 offset:40960\n"
      "v_mfma_f32_16x16x32_bf16 v[124:127], a[240:243], a[44:47], v[124:127]\n"
      "buffer_load_dwordx4 a[72:75], v1, s[48:51], s79 offen offset:2048 sc1\n"
      "v_mfma_f32_16x16x32_bf16 v[132:135], a[244:247], a[40:43], v[132:135]\n"
      "ds_read_b64_tr_b16 a[232:233], v11 offset:32768\n"
      "ds_read_b64_tr_b16 a[234:235], v11 offset:40960\n"
      "v_mfma_f32_16x16x32_bf16 v[140:143], a[244:247], a[44:47], v[140:143]\n"
      "buffer_load_dwordx4 a[76:79], v1, s[48:51], s79 offen offset:3072 sc1\n"
      "s_waitcnt lgkmcnt(6)\n"
      "v_mfma_f32_16x16x32_bf16 v[112:115], a[248:251], v[56:59], v[112:115]\n"
      "ds_read_b64_tr_b16 a[236:237], v12 offset:32768\n"
      "ds_read_b64_tr_b16 a[238:239], v12 offset:40960\n"
      "v_mfma_f32_16x16x32_bf16 v[120:123], a[248:251], v[60:63], v[120:123]\n"
      "v_mfma_f32_16x16x32_bf16 v[128:131], a[252:255], v[56:59], v[128:131]\n"
      "ds_read_b64_tr_b16 a[240:241], v9 offset:33792\n"
      "ds_read_b64_tr_b16 a[242:243], v9 offset:41984\n"
      "v_mfma_f32_16x16x32_bf16 v[136:139], a[252:255], v[60:63], v[136:139]\n"
      "buffer_load_dwordx4 a[80:83], v1, s[48:51], s80 offen offset:2048 sc1\n"
      "v_mfma_f32_16x16x32_bf16 v[116:119], a[216:219], v[56:59], v[116:119]\n"
      "ds_read_b64_tr_b16 a[244:245], v10 offset:33792\n"
      "ds_read_b64_tr_b16 a[246:247], v10 offset:41984\n"
      "v_mfma_f32_16x16x32_bf16 v[124:127], a[216:219], v[60:63], v[124:127]\n"
      "v_mfma_f32_16x16x32_bf16 v[132:135], a[220:223], v[56:59], v[132:135]\n"
      "ds_read_b64_tr_b16 a[248:249], v11 offset:33792\n"
      "ds_read_b64_tr_b16 a[250:251], v11 offset:41984\n"
      "v_mfma_f32_16x16x32_bf16 v[140:143], a[220:223], v[60:63], v[140:143]\n"
      "buffer_load_dwordx4 a[84:87], v1, s[48:51], s80 offen offset:3072 sc1\n"
      "s_waitcnt lgkmcnt(6)\n"
      "v_mfma_f32_16x16x32_bf16 v[112:115], a[224:227], v[64:67], v[112:115]\n"
      "ds_read_b64_tr_b16 a[252:253], v12 offset:33792\n"
      "ds_read_b64_tr_b16 a[254:255], v12 offset:41984\n"
      "v_mfma_f32_16x16x32_bf16 v[120:123], a[224:227], v[68:71], v[120:123]\n"
      "v_mfma_f32_16x16x32_bf16 v[128:131], a[228:231], v[64:67], v[128:131]\n"
      "ds_read_b64_tr_b16 a[216:217], v9 offset:34816\n"
      "ds_read_b64_tr_b16 a[218:219], v9 offset:43008\n"
      "v_mfma_f32_16x16x32_bf16 v[136:139], a[228:231], v[68:71], v[136:139]\n"
      "buffer_load_dwordx4 a[88:91], v1, s[48:51], s81 offen offset:2048 sc1\n"
      "v_mfma_f32_16x16x32_bf16 v[116:119], a[232:235], v[64:67], v[116:119]\n"
      "ds_read_b64_tr_b16 a[220:221], v10 offset:34816\n"
      "ds_read_b64_tr_b16 a[222:223], v10 offset:43008\n"
      "v_mfma_f32_16x16x32_bf16 v[124:127], a[232:235], v[68:71], v[124:127]\n"
      "v_mfma_f32_16x16x32_bf16 v[132:135], a[236:239], v[64:67], v[132:135]\n"
      "ds_read_b64_tr_b16 a[224:225], v11 offset:34816\n"
      "ds_read_b64_tr_b16 a[226:227], v11 offset:43008\n"
      ".LPAD5:\n"
      "s_cbranch_execz .LRET\n"
      "v_mfma_f32_16x16x32_bf16 v[140:143], a[236:239], v[68:71], v[140:143]\n"
      "buffer_load_dwordx4 a[92:95], v1, s[48:51], s81 offen offset:3072 sc1\n"
      "buffer_load_dwordx4 v[144:147], v2, s[48:51], s76 offen offset:0 sc1\n"
      "s_waitcnt lgkmcnt(6)\n"
      "s_waitcnt vmcnt(43)\n"
      "v_mfma_f32_16x16x32_bf16 v[112:115], a[240:243], v[72:75], v[112:115]\n"
      "ds_read_b64_tr_b16 a[228:229], v12 offset:34816\n"
      "ds_read_b64_tr_b16 a[230:231], v12 offset:43008\n"
      "v_mfma_f32_16x16x32_bf16 v[120:123], a[240:243], v[76:79], v[120:123]\n"
      "ds_write_b128 v36, a[164:167] offset:0\n"
      "buffer_load_dwordx4 v[148:151], v2, s[48:51], s76 offen offset:1024 sc1\n"
      "v_mfma_f32_16x16x32_bf16 v[128:131], a[244:247], v[72:75], v[128:131]\n"
      "ds_read_b64_tr_b16 a[232:233], v9 offset:35840\n"
      "ds_read_b64_tr_b16 a[234:235], v9 offset:44032\n"
      "v_mfma_f32_16x16x32_bf16 v[136:139], a[244:247], v[76:79], v[136:139]\n"
      "s_waitcnt lgkmcnt(9)\n"
      "v_mfma_f32_16x16x32_bf16 v[116:119], a[248:251], v[72:75], v[116:119]\n"
      "ds_read_b64_tr_b16 a[236:237], v10 offset:35840\n"
      "ds_read_b64_tr_b16 a[238:239], v10 offset:44032\n"
      "v_mfma_f32_16x16x32_bf16 v[124:127], a[248:251], v[76:79], v[124:127]\n"
      "buffer_load_dwordx4 v[152:155], v2, s[48:51], s77 offen offset:0 sc1\n"
      "v_mfma_f32_16x16x32_bf16 v[132:135], a[252:255], v[72:75], v[132:135]\n"
      "ds_read_b64_tr_b16 a[240:241], v11 offset:35840\n"
      "ds_read_b64_tr_b16 a[242:243], v11 offset:44032\n"
      "v_mfma_f32_16x16x32_bf16 v[140:143], a[252:255], v[76:79], v[140:143]\n"
      "ds_write_b128 v36, a[168:171] offset:1024\n"
      "s_waitcnt vmcnt(17) lgkmcnt(8)\n"
      "s_barrier\n"
      "s_waitcnt vmcnt(14)\n"
      "v_mfma_f32_16x16x32_bf16 v[112:115], a[216:219], a[48:51], v[112:115]\n"
      "ds_read_b64_tr_b16 a[244:245], v12 offset:35840\n"
      "ds_read_b64_tr_b16 a[246:247], v12 offset:44032\n"
      "v_mfma_f32_16x16x32_bf16 v[120:123], a[216:219], a[52:55], v[120:123]\n"
      "buffer_load_dwordx4 v[156:159], v2, s[48:51], s77 offen offset:1024 sc1\n"
      "v_mfma_f32_16x16x32_bf16 v[128:131], a[220:223], a[48:51], v[128:131]\n"
      "ds_read_b64_tr_b16 a[248:249], v9 offset:49152\n"
      "ds_read_b64_tr_b16 a[250:251], v9 offset:57344\n"
      "v_mfma_f32_16x16x32_bf16 v[136:139], a[220:223], a[52:55], v[136:139]\n"
      "buffer_load_dwordx4 v[160:163], v2, s[48:51], s78 offen offset:0 sc1\n"
      "s_waitcnt lgkmcnt(9)\n"
      "v_mfma_f32_16x16x32_bf16 v[116:119], a[224:227], a[48:51], v[116:119]\n"
      "ds_read_b64_tr_b16 a[252:253], v10 offset:49152\n"
      "ds_read_b64_tr_b16 a[254:255], v10 offset:57344\n"
      "v_mfma_f32_16x16x32_bf16 v[124:127], a[224:227], a[52:55], v[124:127]\n"
      "ds_write_b128 v36, a[172:175] offset:2048\n"
      "v_mfma_f32_16x16x32_bf16 v[132:135], a[228:231], a[48:51], v[132:135]\n"
      "ds_read_b64_tr_b16 a[216:217], v11 offset:49152\n"
      "ds_read_b64_tr_b16 a[218:219], v11 offset:57344\n"
      "v_mfma_f32_16x16x32_bf16 v[140:143], a[228:231], a[52:55], v[140:143]\n"
      "buffer_load_dwordx4 v[164:167], v2, s[48:51], s78 offen offset:1024 sc1\n"
      "s_waitcnt vmcnt(14) lgkmcnt(7)\n"
      "v_mfma_f32_16x16x32_bf16 v[112:115], a[232:235], a[56:59], v[112:115]\n"
      "ds_read_b64_tr_b16 a[220:221], v12 offset:49152\n"
      "ds_read_b64_tr_b16 a[222:223], v12 offset:57344\n"
      "v_mfma_f32_16x16x32_bf16 v[120:123], a[232:235], a[60:63], v[120:123]\n"
      "v_mfma_f32_16x16x32_bf16 v[128:131], a[236:239], a[56:59], v[128:131]\n"
      "ds_read_b64_tr_b16 a[224:225], v9 offset:50176\n"
      "ds_read_b64_tr_b16 a[226:227], v9 offset:58368\n"
      "v_mfma_f32_16x16x32_bf16 v[136:139], a[236:239], a[60:63], v[136:139]\n"
      "ds_write_b128 v36, a[176:179] offset:3072\n"
      "buffer_load_dwordx4 v[168:171], v2, s[48:51], s79 offen offset:0 sc1\n"
      "s_waitcnt lgkmcnt(10)\n"
      "v_mfma_f32_16x16x32_bf16 v[116:119], a[240:243], a[56:59], v[116:119]\n"
      "ds_read_b64_tr_b16 a[228:229], v10 offset:50176\n"
      "ds_read_b64_tr_b16 a[230:231], v10 offset:58368\n"
      "v_mfma_f32_16x16x32_bf16 v[124:127], a[240:243], a[60:63], v[124:127]\n"
      "buffer_load_dwordx4 v[172:175], v2, s[48:51], s79 offen offset:1024 sc1\n"
      "v_mfma_f32_16x16x32_bf16 v[132:135], a[244:247], a[56:59], v[132:135]\n"
      "ds_read_b64_tr_b16 a[232:233], v11 offset:50176\n"
      "ds_read_b64_tr_b16 a[234:235], v11 offset:58368\n"
      "v_mfma_f32_16x16x32_bf16 v[140:143], a[244:247], a[60:63], v[140:143]\n"
      "s_waitcnt vmcnt(14) lgkmcnt(7)\n"
      "v_mfma_f32_16x16x32_bf16 v[112:115], a[248:251], a[64:67], v[112:115]\n"
      "ds_read_b64_tr_b16 a[236:237], v12 offset:50176\n"
      "ds_read_b64_tr_b16 a[238:239], v12 offset:58368\n"
      "v_mfma_f32_16x16x32_bf16 v[120:123], a[248:251], a[68:71], v[120:123]\n"
      "ds_write_b128 v36, a[180:183] offset:16384\n"
      "buffer_load_dwordx4 v[176:179], v2, s[48:51], s80 offen offset:0 sc1\n"
      "v_mfma_f32_16x16x32_bf16 v[128:131], a[252:255], a[64:67], v[128:131]\n"
      "ds_read_b64_tr_b16 a[240:241], v9 offset:51200\n"
      "ds_read_b64_tr_b16 a[242:243], v9 offset:59392\n"
      "v_mfma_f32_16x16x32_bf16 v[136:139], a[252:255], a[68:71], v[136:139]\n"
      "s_waitcnt lgkmcnt(10)\n"
      "v_mfma_f32_16x16x32_bf16 v[116:119], a[216:219], a[64:67], v[116:119]\n"
      "ds_read_b64_tr_b16 a[244:245], v10 offset:51200\n"
      "ds_read_b64_tr_b16 a[246:247], v10 offset:59392\n"
      "v_mfma_f32_16x16x32_bf16 v[124:127], a[216:219], a[68:71], v[124:127]\n"
      "buffer_load_dwordx4 v[180:183], v2, s[48:51], s80 offen offset:1024 sc1\n"
      "v_mfma_f32_16x16x32_bf16 v[132:135], a[220:223], a[64:67], v[132:135]\n"
      "ds_read_b64_tr_b16 a[248:249], v11 offset:51200\n"
      "ds_read_b64_tr_b16 a[250:251], v11 offset:59392\n"
      "v_mfma_f32_16x16x32_bf16 v[140:143], a[220:223], a[68:71], v[140:143]\n"
      "ds_write_b128 v36, a[184:187] offset:17408\n"
      "buffer_load_dwordx4 v[184:187], v2, s[48:51], s81 offen offset:0 sc1\n"
      "s_waitcnt vmcnt(15) lgkmcnt(8)\n"
      "v_mfma_f32_16x16x32_bf16 v[112:115], a[224:227], a[72:75], v[112:115]\n"
      "ds_read_b64_tr_b16 a[252:253], v12 offset:51200\n"
      "ds_read_b64_tr_b16 a[254:255], v12 offset:59392\n"
      "v_mfma_f32_16x16x32_bf16 v[120:123], a[224:227], a[76:79], v[120:123]\n"
      "v_mfma_f32_16x16x32_bf16 v[128:131], a[228:231], a[72:75], v[128:131]\n"
      "ds_read_b64_tr_b16 a[216:217], v9 offset:52224\n"
      "ds_read_b64_tr_b16 a[218:219], v9 offset:60416\n"
      "v_mfma_f32_16x16x32_bf16 v[136:139], a[228:231], a[76:79], v[136:139]\n"
      "buffer_load_dwordx4 v[188:191], v2, s[48:51], s81 offen offset:1024 sc1\n"
      "v_mov_b32 v18, 0\n"
      "v_mov_b32 v19, 0\n"
      "s_waitcnt lgkmcnt(9)\n"
      "v_mfma_f32_16x16x32_bf16 v[116:119], a[232:235], a[72:75], v[116:119]\n"
      "ds_read_b64_tr_b16 a[220:221], v10 offset:52224\n"
      "ds_read_b64_tr_b16 a[222:223], v10 offset:60416\n"
      "v_mfma_f32_16x16x32_bf16 v[124:127], a[232:235], a[76:79], v[124:127]\n"
      "ds_write_b128 v36, a[188:191] offset:18432\n"
      "buffer_atomic_or_x2 v[18:19], v24, s[48:51], s93 offen offset:0 sc0 sc1\n"
      "v_mfma_f32_16x16x32_bf16 v[132:135], a[236:239], a[72:75], v[132:135]\n"
      "ds_read_b64_tr_b16 a[224:225], v11 offset:52224\n"
      "ds_read_b64_tr_b16 a[226:227], v11 offset:60416\n"
      "v_mfma_f32_16x16x32_bf16 v[140:143], a[236:239], a[76:79], v[140:143]\n"
      "buffer_load_dwordx4 v[192:195], v2, s[48:51], s76 offen offset:2048 sc1\n"
      "s_waitcnt vmcnt(16) lgkmcnt(7)\n"
      "v_mfma_f32_16x16x32_bf16 v[112:115], a[240:243], a[80:83], v[112:115]\n"
      "ds_read_b64_tr_b16 a[228:229], v12 offset:52224\n"
      "ds_read_b64_tr_b16 a[230:231], v12 offset:60416\n"
      "v_mfma_f32_16x16x32_bf16 v[120:123], a[240:243], a[84:87], v[120:123]\n"
      "v_xor_b32 v9, 0x18000, v8\n"
      "v_mfma_f32_16x16x32_bf16 v[128:131], a[244:247], a[80:83], v[128:131]\n"
      "ds_read_b64_tr_b16 a[232:233], v9 offset:32768\n"
      "ds_read_b64_tr_b16 a[234:235], v9 offset:40960\n"
      "v_mfma_f32_16x16x32_bf16 v[136:139], a[244:247], a[84:87], v[136:139]\n"
      "ds_write_b128 v36, a[192:195] offset:19456\n"
      "buffer_load_dwordx4 v[196:199], v2, s[48:51], s76 offen offset:3072 sc1\n"
      "v_xor_b32 v10, 0x18040, v8\n"
      "s_waitcnt lgkmcnt(10)\n"
      "v_mfma_f32_16x16x32_bf16 v[116:119], a[248:251], a[80:83], v[116:119]\n"
      "ds_read_b64_tr_b16 a[236:237], v10 offset:32768\n"
      "ds_read_b64_tr_b16 a[238:239], v10 offset:40960\n"
      "v_mfma_f32_16x16x32_bf16 v[124:127], a[248:251], a[84:87], v[124:127]\n"
      "v_xor_b32 v11, 0x18020, v8\n"
      "v_mfma_f32_16x16x32_bf16 v[132:135], a[252:255], a[80:83], v[132:135]\n"
      "ds_read_b64_tr_b16 a[240:241], v11 offset:32768\n"
      "ds_read_b64_tr_b16 a[242:243], v11 offset:40960\n"
      "v_mfma_f32_16x16x32_bf16 v[140:143], a[252:255], a[84:87], v[140:143]\n"
      "buffer_load_dwordx4 v[200:203], v2, s[48:51], s77 offen offset:2048 sc1\n"
      "v_xor_b32 v12, 0x18060, v8\n"
      "s_waitcnt vmcnt(16) lgkmcnt(7)\n"
      "v_mfma_f32_16x16x32_bf16 v[112:115], a[216:219], a[88:91], v[112:115]\n"
      "ds_read_b64_tr_b16 a[244:245], v12 offset:32768\n"
      "ds_read_b64_tr_b16 a[246:247], v12 offset:40960\n"
      "v_mfma_f32_16x16x32_bf16 v[120:123], a[216:219], a[92:95], v[120:123]\n"
      "ds_write_b128 v36, a[196:199] offset:32768\n"
      "v_mfma_f32_16x16x32_bf16 v[128:131], a[220:223], a[88:91], v[128:131]\n"
      "ds_read_b64_tr_b16 a[248:249], v9 offset:33792\n"
      "ds_read_b64_tr_b16 a[250:251], v9 offset:41984\n"
      "v_mfma_f32_16x16x32_bf16 v[136:139], a[220:223], a[92:95], v[136:139]\n"
      "buffer_load_dwordx4 v[204:207], v2, s[48:51], s77 offen offset:3072 sc1\n"
      "s_waitcnt vmcnt(11)\n"
      "v_max3_u32 v34, v144, v145, v146\n"
      "v_max3_u32 v34, v34, v147, v148\n"
      "v_max3_u32 v34, v34, v149, v150\n"
      "v_max3_u32 v34, v34, v151, v152\n"
      "v_max3_u32 v34, v34, v153, v154\n"
      "s_waitcnt lgkmcnt(10)\n"
      "v_mfma_f32_16x16x32_bf16 v[116:119], a[224:227], a[88:91], v[116:119]\n"
      "ds_read_b64_tr_b16 a[252:253], v10 offset:33792\n"
      "ds_read_b64_tr_b16 a[254:255], v10 offset:41984\n"
      "v_mfma_f32_16x16x32_bf16 v[124:127], a[224:227], a[92:95], v[124:127]\n"
      "v_max3_u32 v34, v34, v155, v156\n"
      "v_max3_u32 v34, v34, v157, v158\n"
      "v_max3_u32 v34, v34, v159, v160\n"
      "v_max3_u32 v34, v34, v161, v162\n"
      "v_max3_u32 v34, v34, v163, v164\n"
      "v_max3_u32 v34, v34, v165, v166\n"
      "v_max3_u32 v34, v34, v167, v167\n"
      "v_mfma_f32_16x16x32_bf16 v[132:135], a[228:231], a[88:91], v[132:135]\n"
      "ds_read_b64_tr_b16 a[216:217], v11 offset:33792\n"
      "ds_read_b64_tr_b16 a[218:219], v11 offset:41984\n"
      "v_mfma_f32_16x16x32_bf16 v[140:143], a[228:231], a[92:95], v[140:143]\n"
      "ds_write_b128 v36, a[200:203] offset:33792\n"
      "buffer_load_dwordx4 v[208:211], v2, s[48:51], s78 offen offset:2048 sc1\n"
      "v_cmp_eq_u32 vcc, -1, v34\n"
      "s_cbranch_vccnz .LS_C2_0\n"
      ".LK_C2_0:\n"
      "s_waitcnt lgkmcnt(8)\n"
      "v_mfma_f32_16x16x32_bf16 v[112:115], a[232:235], v[144:147], v[112:115]\n"
      "ds_read_b64_tr_b16 a[220:221], v12 offset:33792\n"
      "ds_read_b64_tr_b16 a[222:223], v12 offset:41984\n"
      "v_mfma_f32_16x16x32_bf16 v[120:123], a[232:235], v[148:151], v[120:123]\n"
      "v_mfma_f32_16x16x32_bf16 v[128:131], a[236:239], v[144:147], v[128:131]\n"
      "ds_read_b64_tr_b16 a[224:225], v9 offset:34816\n"
      "ds_read_b64_tr_b16 a[226:227], v9 offset:43008\n"
      "v_mfma_f32_16x16x32_bf16 v[136:139], a[236:239], v[148:151], v[136:139]\n"
      "buffer_load_dwordx4 v[212:215], v2, s[48:51], s78 offen offset:3072 sc1\n"
      "s_waitcnt lgkmcnt(9)\n"
      "v_mfma_f32_16x16x32_bf16 v[116:119], a[240:243], v[144:147], v[116:119]\n"
      "ds_read_b64_tr_b16 a[228:229], v10 offset:34816\n"
      "ds_read_b64_tr_b16 a[230:231], v10 offset:43008\n"
      "v_mfma_f32_16x16x32_bf16 v[124:127], a[240:243], v[148:151], v[124:127]\n"
      "ds_write_b128 v36, a[204:207] offset:34816\n"
      "v_mfma_f32_16x16x32_bf16 v[132:135], a[244:247], v[144:147], v[132:135]\n"
      "ds_read_b64_tr_b16 a[232:233], v11 offset:34816\n"
      "ds_read_b64_tr_b16 a[234:235], v11 offset:43008\n"
      "v_mfma_f32_16x16x32_bf16 v[140:143], a[244:247], v[148:151], v[140:143]\n"
      "buffer_load_dwordx4 v[216:219], v2, s[48:51], s79 offen offset:2048 sc1\n"
      "s_waitcnt lgkmcnt(7)\n"
      "v_mfma_f32_16x16x32_bf16 v[112:115], a[248:251], v[152:155], v[112:115]\n"
      "ds_read_b64_tr_b16 a[236:237], v12 offset:34816\n"
      "ds_read_b64_tr_b16 a[238:239], v12 offset:43008\n"
      "v_mfma_f32_16x16x32_bf16 v[120:123], a[248:251], v[156:159], v[120:123]\n"
      "v_mfma_f32_16x16x32_bf16 v[128:131], a[252:255], v[152:155], v[128:131]\n"
      "ds_read_b64_tr_b16 a[240:241], v9 offset:35840\n"
      "ds_read_b64_tr_b16 a[242:243], v9 offset:44032\n"
      "v_mfma_f32_16x16x32_bf16 v[136:139], a[252:255], v[156:159], v[136:139]\n"
      "buffer_load_dwordx4 v[220:223], v2, s[48:51], s79 offen offset:3072 sc1\n"
      "s_waitcnt lgkmcnt(9)\n"
      "v_mfma_f32_16x16x32_bf16 v[116:119], a[216:219], v[152:155], v[116:119]\n"
      "ds_read_b64_tr_b16 a[244:245], v10 offset:35840\n"
      "ds_read_b64_tr_b16 a[246:247], v10 offset:44032\n"
      "v_mfma_f32_16x16x32_bf16 v[124:127], a[216:219], v[156:159], v[124:127]\n"
      "v_mfma_f32_16x16x32_bf16 v[132:135], a[220:223], v[152:155], v[132:135]\n"
      "ds_read_b64_tr_b16 a[248:249], v11 offset:35840\n"
      "ds_read_b64_tr_b16 a[250:251], v11 offset:44032\n"
      "v_mfma_f32_16x16x32_bf16 v[140:143], a[220:223], v[156:159], v[140:143]\n"
      "buffer_load_dwordx4 v[224:227], v2, s[48:51], s80 offen offset:2048 sc1\n"
      "s_waitcnt lgkmcnt(6)\n"
      "v_mfma_f32_16x16x32_bf16 v[112:115], a[224:227], v[160:163], v[112:115]\n"
      "ds_read_b64_tr_b16 a[252:253], v12 offset:35840\n"
      "ds_read_b64_tr_b16 a[254:255], v12 offset:44032\n"
      "v_mfma_f32_16x16x32_bf16 v[120:123], a[224:227], v[164:167], v[120:123]\n"
      "v_mfma_f32_16x16x32_bf16 v[128:131], a[228:231], v[160:163], v[128:131]\n"
      "ds_read_b64_tr_b16 a[216:217], v9 offset:49152\n"
      "ds_read_b64_tr_b16 a[218:219], v9 offset:57344\n"
      "v_mfma_f32_16x16x32_bf16 v[136:139], a[228:231], v[164:167], v[136:139]\n"
      "buffer_load_dwordx4 v[228:231], v2, s[48:51], s80 offen offset:3072 sc1\n"
      "s_waitcnt vmcnt(11)\n"
      "v_max3_u32 v34, v168, v169, v170\n"
      "v_max3_u32 v34, v34, v171, v172\n"
      "v_max3_u32 v34, v34, v173, v174\n"
      "v_max3_u32 v34, v34, v175, v176\n"
      "v_max3_u32 v34, v34, v177, v178\n"
      "v_mfma_f32_16x16x32_bf16 v[116:119], a[232:235], v[160:163], v[116:119]\n"
      "ds_read_b64_tr_b16 a[220:221], v10 offset:49152\n"
      "ds_read_b64_tr_b16 a[222:223], v10 offset:57344\n"
      "v_mfma_f32_16x16x32_bf16 v[124:127], a[232:235], v[164:167], v[124:127]\n"
      "s_waitcnt vmcnt(10)\n"
      "v_cmp_ne_u64 vcc, s[96:97], v[18:19]\n"
      "s_and_b64 vcc, vcc, 3\n"
      "s_cbranch_scc1 .LS_H4\n"
      ".LK_H4:\n"
      "v_max3_u32 v34, v34, v179, v180\n"
      "v_max3_u32 v34, v34, v181, v182\n"
      "v_max3_u32 v34, v34, v183, v184\n"
      "v_max3_u32 v34, v34, v185, v186\n"
      "v_max3_u32 v34, v34, v187, v188\n"
      "v_mfma_f32_16x16x32_bf16 v[132:135], a[236:239], v[160:163], v[132:135]\n"
      "ds_read_b64_tr_b16 a[224:225], v11 offset:49152\n"
      "ds_read_b64_tr_b16 a[226:227], v11 offset:57344\n"
      "v_mfma_f32_16x16x32_bf16 v[140:143], a[236:239], v[164:167], v[140:143]\n"
      "buffer_load_dwordx4 v[232:235], v2, s[48:51], s81 offen offset:2048 sc1\n"
      "buffer_load_dwordx4 a[96:99], v41, s[48:51], s82 offen offset:0 sc1\n"
      "v_max3_u32 v34, v34, v189, v190\n"
      "v_max3_u32 v34, v34, v191, v191\n"
      "v_cmp_eq_u32 vcc, -1, v34\n"
      "s_cbranch_vccnz .LS_C2_6\n"
      ".LK_C2_6:\n"
      "s_waitcnt lgkmcnt(6)\n"
      "v_mfma_f32_16x16x32_bf16 v[112:115], a[240:243], v[168:171], v[112:115]\n"
      "ds_read_b64_tr_b16 a[228:229], v12 offset:49152\n"
      "ds_read_b64_tr_b16 a[230:231], v12 offset:57344\n"
      "v_mfma_f32_16x16x32_bf16 v[120:123], a[240:243], v[172:175], v[120:123]\n"
      "buffer_load_dwordx4 a[100:103], v41, s[48:51], s82 offen offset:256 sc1\n"
      "v_mfma_f32_16x16x32_bf16 v[128:131], a[244:247], v[168:171], v[128:131]\n"
      "ds_read_b64_tr_b16 a[232:233], v9 offset:50176\n"
      "ds_read_b64_tr_b16 a[234:235], v9 offset:58368\n"
      "v_mfma_f32_16x16x32_bf16 v[136:139], a[244:247], v[172:175], v[136:139]\n"
      "buffer_load_dwordx4 v[236:239], v2, s[48:51], s81 offen offset:3072 sc1\n"
      "v_mfma_f32_16x16x32_bf16 v[116:119], a[248:251], v[168:171], v[116:119]\n"
      "ds_read_b64_tr_b16 a[236:237], v11 offset:50176\n"
      "ds_read_b64_tr_b16 a[238:239], v11 offset:58368\n"
      "v_mfma_f32_16x16x32_bf16 v[124:127], a[248:251], v[172:175], v[124:127]\n"
      "buffer_load_dwordx4 a[104:107], v41, s[48:51], s82 offen offset:2048 sc1\n"
      "v_mfma_f32_16x16x32_bf16 v[132:135], a[252:255], v[168:171], v[132:135]\n"
      "ds_read_b64_tr_b16 a[240:241], v9 offset:51200\n"
      "ds_read_b64_tr_b16 a[242:243], v9 offset:59392\n"
      "v_mfma_f32_16x16x32_bf16 v[140:143], a[252:255], v[172:175], v[140:143]\n"
      "s_waitcnt lgkmcnt(6)\n"
      "s_barrier\n"
      "v_mfma_f32_16x16x32_bf16 v[112:115], a[216:219], v[176:179], v[112:115]\n"
      "ds_read_b64_tr_b16 a[244:245], v11 offset:51200\n"
      "ds_read_b64_tr_b16 a[246:247], v11 offset:59392\n"
      "v_mfma_f32_16x16x32_bf16 v[120:123], a[216:219], v[180:183], v[120:123]\n"
      "buffer_load_dwordx4 a[108:111], v41, s[48:51], s82 offen offset:2304 sc1\n"
      "v_mfma_f32_16x16x32_bf16 v[128:131], a[220:223], v[176:179], v[128:131]\n"
      "ds_read_b64_tr_b16 a[248:249], v9 offset:52224\n"
      "ds_read_b64_tr_b16 a[250:251], v9 offset:60416\n"
      "v_mfma_f32_16x16x32_bf16 v[136:139], a[220:223], v[180:183], v[136:139]\n"
      "v_mfma_f32_16x16x32_bf16 v[116:119], a[224:227], v[176:179], v[116:119]\n"
      "ds_read_b64_tr_b16 a[252:253], v11 offset:52224\n"
      "ds_read_b64_tr_b16 a[254:255], v11 offset:60416\n"
      "v_mfma_f32_16x16x32_bf16 v[124:127], a[224:227], v[180:183], v[124:127]\n"
      "buffer_load_dwordx4 a[112:115], v42, s[48:51], s82 offen offset:0 sc1\n"
      "v_xor_b32 v10, 0x0, v8\n"
      "v_mfma_f32_16x16x32_bf16 v[132:135], a[228:231], v[176:179], v[132:135]\n"
      "ds_read_b64_tr_b16 a[216:217], v10 offset:0\n"
      "ds_read_b64_tr_b16 a[218:219], v10 offset:8192\n"
      "v_mfma_f32_16x16x32_bf16 v[140:143], a[228:231], v[180:183], v[140:143]\n"
      "v_xor_b32 v12, 0x20, v8\n"
      "s_waitcnt vmcnt(11)\n"
      "v_max3_u32 v34, v192, v193, v194\n"
      "v_max3_u32 v34, v34, v195, v196\n"
      "v_max3_u32 v34, v34, v197, v198\n"
      "v_max3_u32 v34, v34, v199, v200\n"
      "s_waitcnt lgkmcnt(6)\n"
      "v_mfma_f32_16x16x32_bf16 v[112:115], a[232:235], v[184:187], v[112:115]\n"
      "ds_read_b64_tr_b16 a[220:221], v12 offset:0\n"
      "ds_read_b64_tr_b16 a[222:223], v12 offset:8192\n"
      "v_mfma_f32_16x16x32_bf16 v[120:123], a[232:235], v[188:191], v[120:123]\n"
      "buffer_load_dwordx4 a[116:119], v42, s[48:51], s82 offen offset:256 sc1\n"
      "v_max3_u32 v34, v34, v201, v202\n"
      "v_max3_u32 v34, v34, v203, v204\n"
      "v_max3_u32 v34, v34, v205, v206\n"
      "v_max3_u32 v34, v34, v207, v208\n"
      "v_mfma_f32_16x16x32_bf16 v[116:119], a[236:239], v[184:187], v[116:119]\n"
      "ds_read_b64_tr_b16 a[224:225], v10 offset:1024\n"
      "ds_read_b64_tr_b16 a[226:227], v10 offset:9216\n"
      "v_mfma_f32_16x16x32_bf16 v[124:127], a[236:239], v[188:191], v[124:127]\n"
      "v_max3_u32 v34, v34, v209, v210\n"
      "v_max3_u32 v34, v34, v211, v212\n"
      "v_max3_u32 v34, v34, v213, v214\n"
      "v_max3_u32 v34, v34, v215, v215\n"
      "v_cmp_eq_u32 vcc, -1, v34\n"
      "s_cbranch_vccnz .LS_C3_0\n"
      ".LK_C3_0:\n"
      "v_mfma_f32_16x16x32_bf16 v[112:115], a[240:243], v[192:195], v[112:115]\n"
      "ds_read_b64_tr_b16 a[228:229], v12 offset:1024\n"
      "ds_read_b64_tr_b16 a[230:231], v12 offset:9216\n"
      "v_mfma_f32_16x16x32_bf16 v[120:123], a[240:243], v[196:199], v[120:123]\n"
      "buffer_load_dwordx4 a[120:123], v42, s[48:51], s82 offen offset:2048 sc1\n"
      "v_mfma_f32_16x16x32_bf16 v[116:119], a[244:247], v[192:195], v[116:119]\n"
      "ds_read_b64_tr_b16 a[232:233], v10 offset:2048\n"
      "ds_read_b64_tr_b16 a[234:235], v10 offset:10240\n"
      "v_mfma_f32_16x16x32_bf16 v[124:127], a[244:247], v[196:199], v[124:127]\n"
      "s_waitcnt lgkmcnt(6)\n"
      "v_mfma_f32_16x16x32_bf16 v[112:115], a[248:251], v[200:203], v[112:115]\n"
      "ds_read_b64_tr_b16 a[236:237], v12 offset:2048\n"
      "ds_read_b64_tr_b16 a[238:239], v12 offset:10240\n"
      "v_mfma_f32_16x16x32_bf16 v[120:123], a[248:251], v[204:207], v[120:123]\n"
      "buffer_load_dwordx4 a[124:127], v42, s[48:51], s82 offen offset:2304 sc1\n"
      "v_mfma_f32_16x16x32_bf16 v[116:119], a[252:255], v[200:203], v[116:119]\n"
      "ds_read_b64_tr_b16 a[240:241], v10 offset:3072\n"
      "ds_read_b64_tr_b16 a[242:243], v10 offset:11264\n"
      "v_mfma_f32_16x16x32_bf16 v[124:127], a[252:255], v[204:207], v[124:127]\n"
      "buffer_load_dwordx4 a[128:131], v41, s[48:51], s83 offen offset:0 sc1\n"
      "s_waitcnt vmcnt(7)\n"
      "v_max3_u32 v34, v216, v217, v218\n"
      "v_max3_u32 v34, v34, v219, v220\n"
      "v_max3_u32 v34, v34, v221, v222\n"
      "v_mfma_f32_16x16x32_bf16 v[112:115], a[216:219], v[208:211], v[112:115]\n"
      "ds_read_b64_tr_b16 a[244:245], v12 offset:3072\n"
      "ds_read_b64_tr_b16 a[246:247], v12 offset:11264\n"
      "v_mfma_f32_16x16x32_bf16 v[120:123], a[216:219], v[212:215], v[120:123]\n"
      "v_max3_u32 v34, v34, v223, v224\n"
      "v_max3_u32 v34, v34, v225, v226\n"
      "v_max3_u32 v34, v34, v227, v228\n"
      "v_max3_u32 v34, v34, v229, v230\n"
      "v_max3_u32 v34, v34, v231, v232\n"
      "v_max3_u32 v34, v34, v233, v234\n"
      "v_max3_u32 v34, v34, v235, v236\n"
      "v_mfma_f32_16x16x32_bf16 v[116:119], a[220:223], v[208:211], v[116:119]\n"
      "ds_read_b64_tr_b16 a[248:249], v10 offset:16384\n"
      "ds_read_b64_tr_b16 a[250:251], v10 offset:24576\n"
      "v_mfma_f32_16x16x32_bf16 v[124:127], a[220:223], v[212:215], v[124:127]\n"
      "buffer_load_dwordx4 a[132:135], v41, s[48:51], s83 offen offset:256 sc1\n"
      "v_max3_u32 v34, v34, v237, v238\n"
      "v_max3_u32 v34, v34, v239, v239\n"
      "v_cmp_eq_u32 vcc, -1, v34\n"
      "s_cbranch_vccnz .LS_C3_6\n"
      ".LK_C3_6:\n"
      "s_waitcnt lgkmcnt(6)\n"
      "v_mfma_f32_16x16x32_bf16 v[112:115], a[224:227], v[216:219], v[112:115]\n"
      "ds_read_b64_tr_b16 a[252:253], v12 offset:16384\n"
      "ds_read_b64_tr_b16 a[254:255], v12 offset:24576\n"
      "v_mfma_f32_16x16x32_bf16 v[120:123], a[224:227], v[220:223], v[120:123]\n"
      "v_mfma_f32_16x16x32_bf16 v[116:119], a[228:231], v[216:219], v[116:119]\n"
      "ds_read_b64_tr_b16 a[216:217], v10 offset:17408\n"
      "ds_read_b64_tr_b16 a[218:219], v10 offset:25600\n"
      "v_mfma_f32_16x16x32_bf16 v[124:127], a[228:231], v[220:223], v[124:127]\n"
      "buffer_load_dwordx4 a[136:139], v41, s[48:51], s83 offen offset:2048 sc1\n"
      "v_mfma_f32_16x16x32_bf16 v[112:115], a[232:235], v[224:227], v[112:115]\n"
      "ds_read_b64_tr_b16 a[220:221], v12 offset:17408\n"
      "ds_read_b64_tr_b16 a[222:223], v12 offset:25600\n"
      "v_mfma_f32_16x16x32_bf16 v[120:123], a[232:235], v[228:231], v[120:123]\n"
      "v_mfma_f32_16x16x32_bf16 v[116:119], a[236:239], v[224:227], v[116:119]\n"
      "ds_read_b64_tr_b16 a[224:225], v10 offset:18432\n"
      "ds_read_b64_tr_b16 a[226:227], v10 offset:26624\n"
      "v_mfma_f32_16x16x32_bf16 v[124:127], a[236:239], v[228:231], v[124:127]\n"
      "buffer_load_dwordx4 a[140:143], v41, s[48:51], s83 offen offset:2304 sc1\n"
      "s_waitcnt lgkmcnt(6)\n"
      "v_mfma_f32_16x16x32_bf16 v[112:115], a[240:243], v[232:235], v[112:115]\n"
      "ds_read_b64_tr_b16 a[228:229], v12 offset:18432\n"
      "ds_read_b64_tr_b16 a[230:231], v12 offset:26624\n"
      "v_mfma_f32_16x16x32_bf16 v[120:123], a[240:243], v[236:239], v[120:123]\n"
      "v_mfma_f32_16x16x32_bf16 v[116:119], a[244:247], v[232:235], v[116:119]\n"
      "ds_read_b64_tr_b16 a[232:233], v10 offset:19456\n"
      "ds_read_b64_tr_b16 a[234:235], v10 offset:27648\n"
      "v_mfma_f32_16x16x32_bf16 v[124:127], a[244:247], v[236:239], v[124:127]\n"
      "buffer_load_dwordx4 a[144:147], v42, s[48:51], s83 offen offset:0 sc1\n"
      "s_waitcnt vmcnt(12)\n"
      "v_mfma_f32_16x16x32_bf16 v[112:115], a[248:251], a[96:99], v[112:115]\n"
      "ds_read_b64_tr_b16 a[236:237], v12 offset:19456\n"
      "ds_read_b64_tr_b16 a[238:239], v12 offset:27648\n"
      "v_mfma_f32_16x16x32_bf16 v[120:123], a[248:251], a[100:103], v[120:123]\n"
      "v_mfma_f32_16x16x32_bf16 v[116:119], a[252:255], a[96:99], v[116:119]\n"
      "ds_read_b64_tr_b16 a[240:241], v10 offset:32768\n"
      "ds_read_b64_tr_b16 a[242:243], v10 offset:40960\n"
      "v_mfma_f32_16x16x32_bf16 v[124:127], a[252:255], a[100:103], v[124:127]\n"
      "buffer_load_dwordx4 a[148:151], v42, s[48:51], s83 offen offset:256 sc1\n"
      "s_waitcnt vmcnt(10) lgkmcnt(6)\n"
      "v_mfma_f32_16x16x32_bf16 v[112:115], a[216:219], a[104:107], v[112:115]\n"
      "ds_read_b64_tr_b16 a[244:245], v12 offset:32768\n"
      "ds_read_b64_tr_b16 a[246:247], v12 offset:40960\n"
      "v_mfma_f32_16x16x32_bf16 v[120:123], a[216:219], a[108:111], v[120:123]\n"
      "v_mfma_f32_16x16x32_bf16 v[116:119], a[220:223], a[104:107], v[116:119]\n"
      "ds_read_b64_tr_b16 a[248:249], v10 offset:33792\n"
      "ds_read_b64_tr_b16 a[250:251], v10 offset:41984\n"
      "v_mfma_f32_16x16x32_bf16 v[124:127], a[220:223], a[108:111], v[124:127]\n"
      "s_waitcnt vmcnt(8)\n"
      "v_mfma_f32_16x16x32_bf16 v[112:115], a[224:227], a[112:115], v[112:115]\n"
      "ds_read_b64_tr_b16 a[252:253], v12 offset:33792\n"
      "ds_read_b64_tr_b16 a[254:255], v12 offset:41984\n"
      "v_mfma_f32_16x16x32_bf16 v[120:123], a[224:227], a[116:119], v[120:123]\n"
      "v_mfma_f32_16x16x32_bf16 v[116:119], a[228:231], a[112:115], v[116:119]\n"
      "ds_read_b64_tr_b16 a[216:217], v10 offset:34816\n"
      "ds_read_b64_tr_b16 a[218:219], v10 offset:43008\n"
      "v_mfma_f32_16x16x32_bf16 v[124:127], a[228:231], a[116:119], v[124:127]\n"
      "s_waitcnt vmcnt(6) lgkmcnt(6)\n"
      "v_mfma_f32_16x16x32_bf16 v[112:115], a[232:235], a[120:123], v[112:115]\n"
      "ds_read_b64_tr_b16 a[220:221], v12 offset:34816\n"
      "ds_read_b64_tr_b16 a[222:223], v12 offset:43008\n"
      "v_mfma_f32_16x16x32_bf16 v[120:123], a[232:235], a[124:127], v[120:123]\n"
      "v_xor_b32 v9, 0x18040, v8\n"
      "v_mfma_f32_16x16x32_bf16 v[116:119], a[236:239], a[120:123], v[116:119]\n"
      "ds_read_b64_tr_b16 a[224:225], v9 offset:50176\n"
      "ds_read_b64_tr_b16 a[226:227], v9 offset:58368\n"
      "v_mfma_f32_16x16x32_bf16 v[124:127], a[236:239], a[124:127], v[124:127]\n"
      "v_xor_b32 v11, 0x18060, v8\n"
      "s_waitcnt vmcnt(4)\n"
      "v_mfma_f32_16x16x32_bf16 v[112:115], a[240:243], a[128:131], v[112:115]\n"
      "ds_read_b64_tr_b16 a[228:229], v11 offset:50176\n"
      "ds_read_b64_tr_b16 a[230:231], v11 offset:58368\n"
      "v_mfma_f32_16x16x32_bf16 v[120:123], a[240:243], a[132:135], v[120:123]\n"
      "v_mfma_f32_16x16x32_bf16 v[116:119], a[244:247], a[128:131], v[116:119]\n"
      "ds_read_b64_tr_b16 a[232:233], v9 offset:51200\n"
      "ds_read_b64_tr_b16 a[234:235], v9 offset:59392\n"
      "v_mfma_f32_16x16x32_bf16 v[124:127], a[244:247], a[132:135], v[124:127]\n"
      "s_waitcnt vmcnt(2) lgkmcnt(6)\n"
      "v_mfma_f32_16x16x32_bf16 v[112:115], a[248:251], a[136:139], v[112:115]\n"
      "ds_read_b64_tr_b16 a[236:237], v11 offset:51200\n"
      "ds_read_b64_tr_b16 a[238:239], v11 offset:59392\n"
      "v_mfma_f32_16x16x32_bf16 v[120:123], a[248:251], a[140:143], v[120:123]\n"
      "v_mfma_f32_16x16x32_bf16 v[116:119], a[252:255], a[136:139], v[116:119]\n"
      "ds_read_b64_tr_b16 a[240:241], v9 offset:52224\n"
      "ds_read_b64_tr_b16 a[242:243], v9 offset:60416\n"
      "v_mfma_f32_16x16x32_bf16 v[124:127], a[252:255], a[140:143], v[124:127]\n"
      "s_waitcnt vmcnt(0)\n"
      "v_mfma_f32_16x16x32_bf16 v[112:115], a[216:219], a[144:147], v[112:115]\n"
      "ds_read_b64_tr_b16 a[244:245], v11 offset:52224\n"
      "ds_read_b64_tr_b16 a[246:247], v11 offset:60416\n"
      "v_mfma_f32_16x16x32_bf16 v[120:123], a[216:219], a[148:151], v[120:123]\n"
      "v_xor_b32 v10, 0x40, v8\n"
      "v_mfma_f32_16x16x32_bf16 v[116:119], a[220:223], a[144:147], v[116:119]\n"
      "ds_read_b64_tr_b16 a[248:249], v10 offset:0\n"
      "ds_read_b64_tr_b16 a[250:251], v10 offset:8192\n"
      "v_mfma_f32_16x16x32_bf16 v[124:127], a[220:223], a[148:151], v[124:127]\n"
      "v_xor_b32 v12, 0x60, v8\n"
      "s_waitcnt lgkmcnt(6)\n"
      "v_mfma_f32_16x16x32_bf16 v[128:131], a[224:227], v[184:187], v[128:131]\n"
      "ds_read_b64_tr_b16 a[252:253], v12 offset:0\n"
      "ds_read_b64_tr_b16 a[254:255], v12 offset:8192\n"
      "v_mfma_f32_16x16x32_bf16 v[136:139], a[224:227], v[188:191], v[136:139]\n"
      "v_cvt_pk_bf16_f32 v80, v112, v113\n"
      "v_cvt_pk_bf16_f32 v81, v114, v115\n"
      "v_cvt_pk_bf16_f32 v82, v116, v117\n"
      "v_cvt_pk_bf16_f32 v83, v118, v119\n"
      "buffer_store_dwordx4 v[80:83], v1, s[48:51], s91 offen offset:0 sc1\n"
      "v_cvt_pk_bf16_f32 v84, v120, v121\n"
      "v_cvt_pk_bf16_f32 v85, v122, v123\n"
      "v_cvt_pk_bf16_f32 v86, v124, v125\n"
      "v_cvt_pk_bf16_f32 v87, v126, v127\n"
      "buffer_store_dwordx4 v[84:87], v1, s[48:51], s91 offen offset:1024 sc1\n"
      "v_mfma_f32_16x16x32_bf16 v[132:135], a[228:231], v[184:187], v[132:135]\n"
      "ds_read_b64_tr_b16 a[216:217], v10 offset:1024\n"
      "ds_read_b64_tr_b16 a[218:219], v10 offset:9216\n"
      "v_mfma_f32_16x16x32_bf16 v[140:143], a[228:231], v[188:191], v[140:143]\n"
      "v_mfma_f32_16x16x32_bf16 v[128:131], a[232:235], v[192:195], v[128:131]\n"
      "ds_read_b64_tr_b16 a[220:221], v12 offset:1024\n"
      "ds_read_b64_tr_b16 a[222:223], v12 offset:9216\n"
      "v_mfma_f32_16x16x32_bf16 v[136:139], a[232:235], v[196:199], v[136:139]\n"
      "v_mfma_f32_16x16x32_bf16 v[132:135], a[236:239], v[192:195], v[132:135]\n"
      "ds_read_b64_tr_b16 a[224:225], v10 offset:2048\n"
      "ds_read_b64_tr_b16 a[226:227], v10 offset:10240\n"
      "v_mfma_f32_16x16x32_bf16 v[140:143], a[236:239], v[196:199], v[140:143]\n"
      "s_waitcnt lgkmcnt(6)\n"
      "v_mfma_f32_16x16x32_bf16 v[128:131], a[240:243], v[200:203], v[128:131]\n"
      "ds_read_b64_tr_b16 a[228:229], v12 offset:2048\n"
      "ds_read_b64_tr_b16 a[230:231], v12 offset:10240\n"
      "v_mfma_f32_16x16x32_bf16 v[136:139], a[240:243], v[204:207], v[136:139]\n"
      "v_mfma_f32_16x16x32_bf16 v[132:135], a[244:247], v[200:203], v[132:135]\n"
      "ds_read_b64_tr_b16 a[232:233], v10 offset:3072\n"
      "ds_read_b64_tr_b16 a[234:235], v10 offset:11264\n"
      "v_mfma_f32_16x16x32_bf16 v[140:143], a[244:247], v[204:207], v[140:143]\n"
      "v_mfma_f32_16x16x32_bf16 v[128:131], a[248:251], v[208:211], v[128:131]\n"
      "ds_read_b64_tr_b16 a[236:237], v12 offset:3072\n"
      "ds_read_b64_tr_b16 a[238:239], v12 offset:11264\n"
      "v_mfma_f32_16x16x32_bf16 v[136:139], a[248:251], v[212:215], v[136:139]\n"
      "v_mfma_f32_16x16x32_bf16 v[132:135], a[252:255], v[208:211], v[132:135]\n"
      "ds_read_b64_tr_b16 a[240:241], v10 offset:16384\n"
      "ds_read_b64_tr_b16 a[242:243], v10 offset:24576\n"
      "v_mfma_f32_16x16x32_bf16 v[140:143], a[252:255], v[212:215], v[140:143]\n"
      "s_waitcnt lgkmcnt(6)\n"
      "v_mfma_f32_16x16x32_bf16 v[128:131], a[216:219], v[216:219], v[128:131]\n"
      "ds_read_b64_tr_b16 a[244:245], v12 offset:16384\n"
      "ds_read_b64_tr_b16 a[246:247], v12 offset:24576\n"
      "v_mfma_f32_16x16x32_bf16 v[136:139], a[216:219], v[220:223], v[136:139]\n"
      "v_mfma_f32_16x16x32_bf16 v[132:135], a[220:223], v[216:219], v[132:135]\n"
      "ds_read_b64_tr_b16 a[248:249], v10 offset:17408\n"
      "ds_read_b64_tr_b16 a[250:251], v10 offset:25600\n"
      "v_mfma_f32_16x16x32_bf16 v[140:143], a[220:223], v[220:223], v[140:143]\n"
      "v_mfma_f32_16x16x32_bf16 v[128:131], a[224:227], v[224:227], v[128:131]\n"
      "ds_read_b64_tr_b16 a[252:253], v12 offset:17408\n"
      "ds_read_b64_tr_b16 a[254:255], v12 offset:25600\n"
      "v_mfma_f32_16x16x32_bf16 v[136:139], a[224:227], v[228:231], v[136:139]\n"
      "v_mfma_f32_16x16x32_bf16 v[132:135], a[228:231], v[224:227], v[132:135]\n"
      "ds_read_b64_tr_b16 a[216:217], v10 offset:18432\n"
      "ds_read_b64_tr_b16 a[218:219], v10 offset:26624\n"
      "v_mfma_f32_16x16x32_bf16 v[140:143], a[228:231], v[228:231], v[140:143]\n"
      "s_waitcnt lgkmcnt(6)\n"
      "v_mfma_f32_16x16x32_bf16 v[128:131], a[232:235], v[232:235], v[128:131]\n"
      "ds_read_b64_tr_b16 a[220:221], v12 offset:18432\n"
      "ds_read_b64_tr_b16 a[222:223], v12 offset:26624\n"
      "v_mfma_f32_16x16x32_bf16 v[136:139], a[232:235], v[236:239], v[136:139]\n"
      "v_mfma_f32_16x16x32_bf16 v[132:135], a[236:239], v[232:235], v[132:135]\n"
      "ds_read_b64_tr_b16 a[224:225], v10 offset:19456\n"
      "ds_read_b64_tr_b16 a[226:227], v10 offset:27648\n"
      "v_mfma_f32_16x16x32_bf16 v[140:143], a[236:239], v[236:239], v[140:143]\n"
      "v_mfma_f32_16x16x32_bf16 v[128:131], a[240:243], a[96:99], v[128:131]\n"
      "ds_read_b64_tr_b16 a[228:229], v12 offset:19456\n"
      "ds_read_b64_tr_b16 a[230:231], v12 offset:27648\n"
      "v_mfma_f32_16x16x32_bf16 v[136:139], a[240:243], a[100:103], v[136:139]\n"
      ".LPAD6:\n"
      "s_cbranch_execz .LRET\n"
      "v_mfma_f32_16x16x32_bf16 v[132:135], a[244:247], a[96:99], v[132:135]\n"
      "ds_read_b64_tr_b16 a[232:233], v10 offset:32768\n"
      "ds_read_b64_tr_b16 a[234:235], v10 offset:40960\n"
      "v_mfma_f32_16x16x32_bf16 v[140:143], a[244:247], a[100:103], v[140:143]\n"
      "s_waitcnt lgkmcnt(6)\n"
      "v_mfma_f32_16x16x32_bf16 v[128:131], a[248:251], a[104:107], v[128:131]\n"
      "ds_read_b64_tr_b16 a[236:237], v12 offset:32768\n"
      "ds_read_b64_tr_b16 a[238:239], v12 offset:40960\n"
      "v_mfma_f32_16x16x32_bf16 v[136:139], a[248:251], a[108:111], v[136:139]\n"
      "v_mfma_f32_16x16x32_bf16 v[132:135], a[252:255], a[104:107], v[132:135]\n"
      "ds_read_b64_tr_b16 a[240:241], v10 offset:33792\n"
      "ds_read_b64_tr_b16 a[242:243], v10 offset:41984\n"
      "v_mfma_f32_16x16x32_bf16 v[140:143], a[252:255], a[108:111], v[140:143]\n"
      "v_mfma_f32_16x16x32_bf16 v[128:131], a[216:219], a[112:115], v[128:131]\n"
      "ds_read_b64_tr_b16 a[244:245], v12 offset:33792\n"
      "ds_read_b64_tr_b16 a[246:247], v12 offset:41984\n"
      "v_mfma_f32_16x16x32_bf16 v[136:139], a[216:219], a[116:119], v[136:139]\n"
      "v_mfma_f32_16x16x32_bf16 v[132:135], a[220:223], a[112:115], v[132:135]\n"
      "ds_read_b64_tr_b16 a[248:249], v10 offset:34816\n"
      "ds_read_b64_tr_b16 a[250:251], v10 offset:43008\n"
      "v_mfma_f32_16x16x32_bf16 v[140:143], a[220:223], a[116:119], v[140:143]\n"
      "s_waitcnt lgkmcnt(6)\n"
      "v_mfma_f32_16x16x32_bf16 v[128:131], a[224:227], a[120:123], v[128:131]\n"
      "ds_read_b64_tr_b16 a[252:253], v12 offset:34816\n"
      "ds_read_b64_tr_b16 a[254:255], v12 offset:43008\n"
      "v_mfma_f32_16x16x32_bf16 v[136:139], a[224:227], a[124:127], v[136:139]\n"
      "v_mfma_f32_16x16x32_bf16 v[132:135], a[228:231], a[120:123], v[132:135]\n"
      "v_mfma_f32_16x16x32_bf16 v[140:143], a[228:231], a[124:127], v[140:143]\n"
      "v_mfma_f32_16x16x32_bf16 v[128:131], a[232:235], a[128:131], v[128:131]\n"
      "v_mfma_f32_16x16x32_bf16 v[136:139], a[232:235], a[132:135], v[136:139]\n"
      "v_mfma_f32_16x16x32_bf16 v[132:135], a[236:239], a[128:131], v[132:135]\n"
      "v_mfma_f32_16x16x32_bf16 v[140:143], a[236:239], a[132:135], v[140:143]\n"
      "s_waitcnt lgkmcnt(0)\n"
      "v_mfma_f32_16x16x32_bf16 v[128:131], a[240:243], a[136:139], v[128:131]\n"
      "v_mfma_f32_16x16x32_bf16 v[136:139], a[240:243], a[140:143], v[136:139]\n"
      "v_mfma_f32_16x16x32_bf16 v[132:135], a[244:247], a[136:139], v[132:135]\n"
      "v_mfma_f32_16x16x32_bf16 v[140:143], a[244:247], a[140:143], v[140:143]\n"
      "buffer_load_dwordx4 v[88:91], v1, s[48:51], s92 offen offset:0 sc1\n"
      "buffer_load_dwordx4 v[92:95], v1, s[48:51], s92 offen offset:1024 sc1\n"
      "v_mfma_f32_16x16x32_bf16 v[128:131], a[248:251], a[144:147], v[128:131]\n"
      "v_mfma_f32_16x16x32_bf16 v[136:139], a[248:251], a[148:151], v[136:139]\n"
      "v_mfma_f32_16x16x32_bf16 v[132:135], a[252:255], a[144:147], v[132:135]\n"
      "v_mfma_f32_16x16x32_bf16 v[140:143], a[252:255], a[148:151], v[140:143]\n"
      "s_nop 9\n"
      "s_waitcnt vmcnt(0)\n"
      "v_mfma_f32_4x4x4_16b_bf16 v[96:99], v[28:29], v[88:89], v[128:131]\n"
      "v_mfma_f32_4x4x4_16b_bf16 v[100:103], v[28:29], v[90:91], v[132:135]\n"
      "v_mfma_f32_4x4x4_16b_bf16 v[104:107], v[28:29], v[92:93], v[136:139]\n"
      "v_mfma_f32_4x4x4_16b_bf16 v[108:111], v[28:29], v[94:95], v[140:143]\n"
      "v_max3_u32 v34, v88, v89, v90\n"
      "v_max3_u32 v34, v34, v91, v92\n"
      "v_max3_u32 v34, v34, v93, v94\n"
      "v_max3_u32 v34, v34, v95, v95\n"
      "v_cmp_eq_u32 vcc, -1, v34\n"
      "s_cbranch_vccnz .LS_X\n"
      ".LK_X:\n"
      "v_cvt_pk_bf16_f32 v112, v96, v97\n"
      "v_cvt_pk_bf16_f32 v114, v100, v101\n"
      "v_cvt_pk_bf16_f32 v113, v98, v99\n"
      "v_cvt_pk_bf16_f32 v115, v102, v103\n"
      "v_cvt_pk_bf16_f32 v116, v104, v105\n"
      "v_cvt_pk_bf16_f32 v118, v108, v109\n"
      "v_cvt_pk_bf16_f32 v117, v106, v107\n"
      "v_cvt_pk_bf16_f32 v119, v110, v111\n"
      "v_permlane16_swap_b32 v112, v114\n"
      "v_permlane16_swap_b32 v113, v115\n"
      "v_permlane16_swap_b32 v116, v118\n"
      "v_permlane16_swap_b32 v117, v119\n"
      "buffer_store_dwordx4 v[112:115], v15, s[44:47], s98 offen nt\n"
      "s_addk_i32 s98, 28672\n"
      "buffer_store_dwordx4 v[116:119], v15, s[44:47], s98 offen nt\n"
      "s_endpgm\n"
      ".LS_H0:\n"
      "s_mov_b32 s99, 200000\n"
      ".LR_H0:\n"
      "s_sleep 2\n"
      "v_mov_b32 v18, 0\n"
      "v_mov_b32 v19, 0\n"
      "buffer_atomic_or_x2 v[18:19], v14, s[48:51], s89 offen offset:0 sc0 sc1\n"
      "s_waitcnt vmcnt(0)\n"
      "v_cmp_ne_u64 vcc, s[96:97], v[18:19]\n"
      "s_nop 0\n"
      "s_and_b64 vcc, vcc, 63\n"
      "s_cbranch_scc0 .LK_H0\n"
      "s_sub_u32 s99, s99, 1\n"
      "s_cbranch_scc0 .LR_H0\n"
      "s_branch .LK_H0\n"
      ".LS_H1:\n"
      "s_mov_b32 s99, 200000\n"
      ".LR_H1:\n"
      "s_sleep 2\n"
      "v_mov_b32 v18, 0\n"
      "v_mov_b32 v19, 0\n"
      "buffer_atomic_or_x2 v[18:19], v14, s[48:51], s89 offen offset:1024 sc0 sc1\n"
      "s_waitcnt vmcnt(0)\n"
      "v_cmp_ne_u64 vcc, s[96:97], v[18:19]\n"
      "s_nop 0\n"
      "s_and_b64 vcc, vcc, 63\n"
      "s_cbranch_scc0 .LK_H1\n"
      "s_sub_u32 s99, s99, 1\n"
      "s_cbranch_scc0 .LR_H1\n"
      "s_branch .LK_H1\n"
      ".LS_C2_0:\n"
      "s_mov_b32 s99, 200000\n"
      ".LR_C2_0:\n"
      "s_sleep 2\n"
      "buffer_load_dwordx4 v[144:147], v2, s[48:51], s76 offen offset:0 sc1\n"
      "buffer_load_dwordx4 v[148:151], v2, s[48:51], s76 offen offset:1024 sc1\n"
      "buffer_load_dwordx4 v[152:155], v2, s[48:51], s77 offen offset:0 sc1\n"
      "buffer_load_dwordx4 v[156:159], v2, s[48:51], s77 offen offset:1024 sc1\n"
      "buffer_load_dwordx4 v[160:163], v2, s[48:51], s78 offen offset:0 sc1\n"
      "buffer_load_dwordx4 v[164:167], v2, s[48:51], s78 offen offset:1024 sc1\n"
      "s_waitcnt vmcnt(0)\n"
      "v_max3_u32 v34, v144, v145, v146\n"
      "v_max3_u32 v34, v34, v147, v148\n"
      "v_max3_u32 v34, v34, v149, v150\n"
      "v_max3_u32 v34, v34, v151, v152\n"
      "v_max3_u32 v34, v34, v153, v154\n"
      "v_max3_u32 v34, v34, v155, v156\n"
      "v_max3_u32 v34, v34, v157, v158\n"
      "v_max3_u32 v34, v34, v159, v160\n"
      "v_max3_u32 v34, v34, v161, v162\n"
      "v_max3_u32 v34, v34, v163, v164\n"
      "v_max3_u32 v34, v34, v165, v166\n"
      "v_max3_u32 v34, v34, v167, v167\n"
      "v_cmp_eq_u32 vcc, -1, v34\n"
      "s_nop 0\n"
      "s_cbranch_vccz .LK_C2_0\n"
      "s_sub_u32 s99, s99, 1\n"
      "s_cbranch_scc0 .LR_C2_0\n"
      "s_branch .LK_C2_0\n"
      ".LS_C2_6:\n"
      "s_mov_b32 s99, 200000\n"
      ".LR_C2_6:\n"
      "s_sleep 2\n"
      "buffer_load_dwordx4 v[168:171], v2, s[48:51], s79 offen offset:0 sc1\n"
      "buffer_load_dwordx4 v[172:175], v2, s[48:51], s79 offen offset:1024 sc1\n"
      "buffer_load_dwordx4 v[176:179], v2, s[48:51], s80 offen offset:0 sc1\n"
      "buffer_load_dwordx4 v[180:183], v2, s[48:51], s80 offen offset:1024 sc1\n"
      "buffer_load_dwordx4 v[184:187], v2, s[48:51], s81 offen offset:0 sc1\n"
      "buffer_load_dwordx4 v[188:191], v2, s[48:51], s81 offen offset:1024 sc1\n"
      "s_waitcnt vmcnt(0)\n"
      "v_max3_u32 v34, v168, v169, v170\n"
      "v_max3_u32 v34, v34, v171, v172\n"
      "v_max3_u32 v34, v34, v173, v174\n"
      "v_max3_u32 v34, v34, v175, v176\n"
      "v_max3_u32 v34, v34, v177, v178\n"
      "v_max3_u32 v34, v34, v179, v180\n"
      "v_max3_u32 v34, v34, v181, v182\n"
      "v_max3_u32 v34, v34, v183, v184\n"
      "v_max3_u32 v34, v34, v185, v186\n"
      "v_max3_u32 v34, v34, v187, v188\n"
      "v_max3_u32 v34, v34, v189, v190\n"
      "v_max3_u32 v34, v34, v191, v191\n"
      "v_cmp_eq_u32 vcc, -1, v34\n"
      "s_nop 0\n"
      "s_cbranch_vccz .LK_C2_6\n"
      "s_sub_u32 s99, s99, 1\n"
      "s_cbranch_scc0 .LR_C2_6\n"
      "s_branch .LK_C2_6\n"
      ".LS_C3_0:\n"
      "s_mov_b32 s99, 200000\n"
      ".LR_C3_0:\n"
      "s_sleep 2\n"
      "buffer_load_dwordx4 v[192:195], v2, s[48:51], s76 offen offset:2048 sc1\n"
      "buffer_load_dwordx4 v[196:199], v2, s[48:51], s76 offen offset:3072 sc1\n"
      "buffer_load_dwordx4 v[200:203], v2, s[48:51], s77 offen offset:2048 sc1\n"
      "buffer_load_dwordx4 v[204:207], v2, s[48:51], s77 offen offset:3072 sc1\n"
      "buffer_load_dwordx4 v[208:211], v2, s[48:51], s78 offen offset:2048 sc1\n"
      "buffer_load_dwordx4 v[212:215], v2, s[48:51], s78 offen offset:3072 sc1\n"
      "s_waitcnt vmcnt(0)\n"
      "v_max3_u32 v34, v192, v193, v194\n"
      "v_max3_u32 v34, v34, v195, v196\n"
      "v_max3_u32 v34, v34, v197, v198\n"
      "v_max3_u32 v34, v34, v199, v200\n"
      "v_max3_u32 v34, v34, v201, v202\n"
      "v_max3_u32 v34, v34, v203, v204\n"
      "v_max3_u32 v34, v34, v205, v206\n"
      "v_max3_u32 v34, v34, v207, v208\n"
      "v_max3_u32 v34, v34, v209, v210\n"
      "v_max3_u32 v34, v34, v211, v212\n"
      "v_max3_u32 v34, v34, v213, v214\n"
      "v_max3_u32 v34, v34, v215, v215\n"
      "v_cmp_eq_u32 vcc, -1, v34\n"
      "s_nop 0\n"
      "s_cbranch_vccz .LK_C3_0\n"
      "s_sub_u32 s99, s99, 1\n"
      "s_cbranch_scc0 .LR_C3_0\n"
      "s_branch .LK_C3_0\n"
      ".LS_C3_6:\n"
      "s_mov_b32 s99, 200000\n"
      ".LR_C3_6:\n"
      "s_sleep 2\n"
      "buffer_load_dwordx4 v[216:219], v2, s[48:51], s79 offen offset:2048 sc1\n"
      "buffer_load_dwordx4 v[220:223], v2, s[48:51], s79 offen offset:3072 sc1\n"
      "buffer_load_dwordx4 v[224:227], v2, s[48:51], s80 offen offset:2048 sc1\n"
      "buffer_load_dwordx4 v[228:231], v2, s[48:51], s80 offen offset:3072 sc1\n"
      "buffer_load_dwordx4 v[232:235], v2, s[48:51], s81 offen offset:2048 sc1\n"
      "buffer_load_dwordx4 v[236:239], v2, s[48:51], s81 offen offset:3072 sc1\n"
      "s_waitcnt vmcnt(0)\n"
      "v_max3_u32 v34, v216, v217, v218\n"
      "v_max3_u32 v34, v34, v219, v220\n"
      "v_max3_u32 v34, v34, v221, v222\n"
      "v_max3_u32 v34, v34, v223, v224\n"
      "v_max3_u32 v34, v34, v225, v226\n"
      "v_max3_u32 v34, v34, v227, v228\n"
      "v_max3_u32 v34, v34, v229, v230\n"
      "v_max3_u32 v34, v34, v231, v232\n"
      "v_max3_u32 v34, v34, v233, v234\n"
      "v_max3_u32 v34, v34, v235, v236\n"
      "v_max3_u32 v34, v34, v237, v238\n"
      "v_max3_u32 v34, v34, v239, v239\n"
      "v_cmp_eq_u32 vcc, -1, v34\n"
      "s_nop 0\n"
      "s_cbranch_vccz .LK_C3_6\n"
      "s_sub_u32 s99, s99, 1\n"
      "s_cbranch_scc0 .LR_C3_6\n"
      "s_branch .LK_C3_6\n"
      ".LS_H4:\n"
      "s_mov_b32 s99, 200000\n"
      ".LR_H4:\n"
      "s_sleep 2\n"
      "v_mov_b32 v18, 0\n"
      "v_mov_b32 v19, 0\n"
      "buffer_atomic_or_x2 v[18:19], v24, s[48:51], s93 offen offset:0 sc0 sc1\n"
      "s_waitcnt vmcnt(0)\n"
      "v_cmp_ne_u64 vcc, s[96:97], v[18:19]\n"
      "s_nop 0\n"
      "s_and_b64 vcc, vcc, 3\n"
      "s_cbranch_scc0 .LK_H4\n"
      "s_sub_u32 s99, s99, 1\n"
      "s_cbranch_scc0 .LR_H4\n"
      "s_branch .LK_H4\n"
      ".LS_X:\n"
      "s_mov_b32 s99, 200000\n"
      ".LR_X:\n"
      "s_sleep 0\n"
      "buffer_load_dwordx4 v[88:91], v1, s[48:51], s92 offen offset:0 sc1\n"
      "buffer_load_dwordx4 v[92:95], v1, s[48:51], s92 offen offset:1024 sc1\n"
      "s_waitcnt vmcnt(0)\n"
      "v_mfma_f32_4x4x4_16b_bf16 v[96:99], v[28:29], v[88:89], v[128:131]\n"
      "v_mfma_f32_4x4x4_16b_bf16 v[100:103], v[28:29], v[90:91], v[132:135]\n"
      "v_mfma_f32_4x4x4_16b_bf16 v[104:107], v[28:29], v[92:93], v[136:139]\n"
      "v_mfma_f32_4x4x4_16b_bf16 v[108:111], v[28:29], v[94:95], v[140:143]\n"
      "v_max3_u32 v34, v88, v89, v90\n"
      "v_max3_u32 v34, v34, v91, v92\n"
      "v_max3_u32 v34, v34, v93, v94\n"
      "v_max3_u32 v34, v34, v95, v95\n"
      "v_cmp_eq_u32 vcc, -1, v34\n"
      "s_nop 0\n"
      "s_cbranch_vccz .LK_X\n"
      "s_sub_u32 s99, s99, 1\n"
      "s_cbranch_scc0 .LR_X\n"
      "s_branch .LK_X\n"
      ".LMV:\n"
      "v_bfe_u32 v36, v0, 1, 3\n"
      "v_lshrrev_b32 v74, 4, v0\n"
      "v_xor_b32 v75, v36, v74\n"
      "v_and_b32 v76, 15, v0\n"
      "v_lshl_add_u32 v75, v75, 4, s95\n"
      "v_lshl_add_u32 v32, v76, 7, v75\n"
      "v_xor_b32 v33, 64, v32\n"
      "v_xor_b32 v34, 0x10000, v32\n"
      "v_xor_b32 v35, 64, v34\n"
      "v_and_b32 v66, 1, v74\n"
      "v_lshrrev_b32 v67, 5, v0\n"
      "v_and_b32 v68, 31, v0\n"
      "v_mov_b32 v30, 0x3f80\n"
      "v_mov_b32 v31, 0\n"
      "v_lshlrev_b32 v75, 4, v69\n"
      "v_lshlrev_b64 v[28:29], v75, v[30:31]\n"
      "v_mov_b32 v30, 0x3f803f80\n"
      "v_mov_b32 v31, v30\n"
      "s_movk_i32 s62, 20480\n"
      "s_lshl_b32 s63, s62, 1\n"
      "s_add_u32 s64, s62, s63\n"
      "s_lshl_b32 s58, s55, 7\n"
      "s_add_u32 s56, s3, s59\n"
      "s_lshl1_add_u32 s56, s56, s60\n"
      "s_sub_u32 s57, s56, 14\n"
      "s_min_u32 s56, s56, s57\n"
      "s_mul_i32 s34, s56, 86016\n"
      "s_add_u32 s56, s56, 4\n"
      "s_sub_u32 s57, s56, 14\n"
      "s_min_u32 s56, s56, s57\n"
      "s_mul_i32 s86, s56, 86016\n"
      "s_add_u32 s56, s56, 4\n"
      "s_sub_u32 s57, s56, 14\n"
      "s_min_u32 s56, s56, s57\n"
      "s_mul_i32 s87, s56, 86016\n"
      "s_sub_u32 s56, s3, 1\n"
      "s_min_u32 s56, s56, 6\n"
      "s_lshl2_add_u32 s56, s56, s52\n"
      "s_mul_i32 s99, s56, 43008\n"
      "v_lshlrev_b32 v75, 1, v66\n"
      "v_xor_b32 v75, v75, v65\n"
      "v_lshlrev_b32 v75, 4, v75\n"
      "v_mul_u32_u24 v3, 2688, v64\n"
      "s_add_u32 s56, s58, 1792\n"
      "v_add3_u32 v3, v3, v75, s56\n"
      "v_bfe_u32 v71, v0, 2, 2\n"
      "v_lshlrev_b32 v70, 7, v71\n"
      "v_lshrrev_b32 v71, 1, v71\n"
      "v_lshl_or_b32 v75, v66, 1, v71\n"
      "v_lshl_add_u32 v75, v75, 5, v70\n"
      "v_lshl_add_u32 v37, v69, 3, v75\n"
      "v_lshl_add_u32 v6, v74, 10, v37\n"
      "v_lshlrev_b32 v1, 4, v0\n"
      "v_cmp_gt_u32 vcc, 1, v0\n"
      "v_bfrev_b32 v73, 1\n"
      "v_lshlrev_b32 v75, 3, v0\n"
      "v_cndmask_b32 v13, v73, v75, vcc\n"
      "s_sub_u32 s56, s55, s3\n"
      "s_add_u32 s57, s56, 7\n"
      "s_min_u32 s56, s56, s57\n"
      "s_lshl_b32 s56, s56, 14\n"
      "s_add_u32 s56, s56, s95\n"
      "v_xor_b32 v75, v36, v67\n"
      "v_lshl_add_u32 v75, v75, 4, s56\n"
      "v_lshl_add_u32 v44, v68, 7, v75\n"
      "v_mul_u32_u24 v75, 2040, v66\n"
      "v_sub_u32 v44, v44, v75\n"
      "v_xor_b32 v44, 64, v44\n"
      "v_xor_b32 v46, 32, v44\n"
      "v_lshl_add_u32 v75, v66, 12, v37\n"
      "v_lshl_add_u32 v7, v67, 10, v75\n"
      "v_lshl_add_u32 v75, v66, 9, v37\n"
      "v_lshl_add_u32 v8, v67, 12, v75\n"
      "v_mul_u32_u24 v75, 6912, v66\n"
      "v_lshlrev_b32 v76, 8, v67\n"
      "v_sub_u32 v75, v75, v76\n"
      "v_add_u32 v15, v75, v1\n"
      "s_bitcmp1_b32 s52, 1\n"
      "s_cselect_b64 s[28:29], s[16:17], s[28:29]\n"
      "v_lshl_or_b32 v75, s60, 1, v66\n"
      "buffer_load_dwordx4 v40, s[20:23], s61 offen offset:0 lds\n"
      "buffer_load_dwordx4 v41, s[20:23], s61 offen offset:1024 lds\n"
      "buffer_load_dwordx4 v42, s[20:23], s61 offen offset:2048 lds\n"
      "buffer_load_dwordx4 v43, s[20:23], s61 offen offset:3072 lds\n"
      "s_addk_i32 s61, 128\n"
      "s_addk_i32 m0, 0x4000\n"
      "s_cmp_eq_u32 s61, s11\n"
      "s_cselect_b32 s61, s98, s61\n"
      "s_getpc_b64 s[14:15]\n"
      ".LHGV:\n"
      "s_add_u32 s14, s14, .LHBV-.LHGV\n"
      "s_addc_u32 s15, s15, 0\n"
      "s_mov_b64 exec, 0\n"
      "s_cmp_eq_u32 s52, 0\n"
      "s_cbranch_scc1 .LPAD7\n"
      "s_cmp_eq_u32 s52, 1\n"
      "s_cbranch_scc1 .LPAD8\n"
      "s_cmp_eq_u32 s52, 2\n"
      "s_cbranch_scc1 .LJOIN\n"
      "s_cmp_eq_u32 s52, 3\n"
      "s_cbranch_scc1 .LPAD6\n"
      ".LHBV:\n"
      "s_mov_b64 exec, -1\n"
      "v_lshlrev_b32 v75, 1, v75\n"
      "v_xor_b32 v75, v75, v65\n"
      "v_mul_u32_u24 v76, 3584, v64\n"
      "v_lshl_add_u32 v76, v75, 4, v76\n"
      "s_lshl1_add_u32 s94, s58, 1792\n"
      "s_lshl3_add_u32 s56, s3, s60\n"
      "s_mul_i32 s56, s56, 28672\n"
      "s_add_u32 s56, s56, s94\n"
      "v_add_u32 v4, s56, v76\n"
      "s_mov_b32 s65, 56320\n"
      "s_lshl_b32 s66, s65, 1\n"
      "s_add_u32 s67, s65, s66\n"
      "s_mul_i32 s56, s52, 7168\n"
      "s_add_u32 s56, s56, s58\n"
      "v_lshlrev_b32 v76, 1, v74\n"
      "v_xor_b32 v76, v76, v65\n"
      "v_lshlrev_b32 v76, 4, v76\n"
      "v_mul_u32_u24 v75, 896, v64\n"
      "v_add3_u32 v5, v75, v76, s56\n"
      "v_add3_u32 v75, v0, s55, 1\n"
      "s_waitcnt vmcnt(8)\n"
      "ds_read_b128 a[0:3], v32 offset:0\n"
      "ds_read_b128 a[4:7], v32 offset:2048\n"
      "ds_read_b128 a[8:11], v33 offset:0\n"
      "ds_read_b128 a[12:15], v33 offset:2048\n"
      "buffer_load_dwordx4 v40, s[20:23], s61 offen offset:0 lds\n"
      "buffer_load_dwordx4 v41, s[20:23], s61 offen offset:1024 lds\n"
      "buffer_load_dwordx4 v42, s[20:23], s61 offen offset:2048 lds\n"
      "buffer_load_dwordx4 v43, s[20:23], s61 offen offset:3072 lds\n"
      "s_addk_i32 s61, 128\n"
      "s_addk_i32 m0, 0x4000\n"
      "s_cmp_eq_u32 s61, s11\n"
      "s_cselect_b32 s61, s98, s61\n"
      "v_add_u32 v76, -7, v75\n"
      "v_min_u32 v75, v75, v76\n"
      "v_cmp_gt_u32 vcc, 6, v0\n"
      "v_lshlrev_b32 v75, 3, v75\n"
      "v_add_u32 v2, 4096, v1\n"
      "v_cndmask_b32 v14, v73, v75, vcc\n"
      "s_mov_b32 s56, 229376\n"
      "s_cmp_eq_u32 s3, 6\n"
      "s_cselect_b32 s16, 4293591040, s56\n"
      "s_cmp_eq_u32 s3, 5\n"
      "s_cselect_b32 s17, 4293591040, s56\n"
      "s_cmp_eq_u32 s3, 4\n"
      "s_cselect_b32 s18, 4293591040, s56\n"
      "s_cmp_eq_u32 s3, 3\n"
      "s_cselect_b32 s19, 4293591040, s56\n"
      "s_waitcnt vmcnt(8) lgkmcnt(0)\n"
      "ds_read_b128 a[16:19], v32 offset:16384\n"
      "ds_read_b128 a[20:23], v32 offset:18432\n"
      "ds_read_b128 a[24:27], v33 offset:16384\n"
      "ds_read_b128 a[28:31], v33 offset:18432\n"
      "buffer_load_dwordx4 v40, s[20:23], s61 offen offset:0 lds\n"
      "buffer_load_dwordx4 v41, s[20:23], s61 offen offset:1024 lds\n"
      "buffer_load_dwordx4 v42, s[20:23], s61 offen offset:2048 lds\n"
      "buffer_load_dwordx4 v43, s[20:23], s61 offen offset:3072 lds\n"
      "s_addk_i32 s61, 128\n"
      "s_addk_i32 m0, 0x4000\n"
      "s_cmp_eq_u32 s61, s11\n"
      "s_cselect_b32 s61, s98, s61\n"
      "v_mfma_f32_4x4x4_16b_bf16 v[48:51], v[30:31], a[0:1], 0\n"
      "v_mfma_f32_4x4x4_16b_bf16 v[56:59], a[0:1], a[0:1], 0\n"
      "v_mfma_f32_4x4x4_16b_bf16 v[52:55], v[30:31], a[4:5], 0\n"
      "v_mfma_f32_4x4x4_16b_bf16 v[60:63], a[4:5], a[4:5], 0\n"
      "v_mfma_f32_4x4x4_16b_bf16 v[48:51], v[30:31], a[2:3], v[48:51]\n"
      "v_mfma_f32_4x4x4_16b_bf16 v[56:59], a[2:3], a[2:3], v[56:59]\n"
      "v_mfma_f32_4x4x4_16b_bf16 v[52:55], v[30:31], a[6:7], v[52:55]\n"
      "v_mfma_f32_4x4x4_16b_bf16 v[60:63], a[6:7], a[6:7], v[60:63]\n"
      "v_mfma_f32_4x4x4_16b_bf16 v[48:51], v[30:31], a[8:9], v[48:51]\n"
      "v_mfma_f32_4x4x4_16b_bf16 v[56:59], a[8:9], a[8:9], v[56:59]\n"
      "v_mfma_f32_4x4x4_16b_bf16 v[52:55], v[30:31], a[12:13], v[52:55]\n"
      "v_mfma_f32_4x4x4_16b_bf16 v[60:63], a[12:13], a[12:13], v[60:63]\n"
      "v_mfma_f32_4x4x4_16b_bf16 v[48:51], v[30:31], a[10:11], v[48:51]\n"
      "v_mfma_f32_4x4x4_16b_bf16 v[56:59], a[10:11], a[10:11], v[56:59]\n"
      "v_mfma_f32_4x4x4_16b_bf16 v[52:55], v[30:31], a[14:15], v[52:55]\n"
      "v_mfma_f32_4x4x4_16b_bf16 v[60:63], a[14:15], a[14:15], v[60:63]\n"
      "s_cmp_eq_u32 s3, 2\n"
      "s_cselect_b32 s8, 4293591040, s56\n"
      "s_cmp_eq_u32 s3, 1\n"
      "s_cselect_b32 s9, 4293591040, s56\n"
      "s_cmp_eq_u32 s3, 0\n"
      "s_cselect_b32 s10, 4293591040, s56\n"
      "s_mul_i32 s56, s12, 57344\n"
      "s_waitcnt vmcnt(8) lgkmcnt(0)\n"
      "ds_read_b128 a[32:35], v32 offset:32768\n"
      "ds_read_b128 a[36:39], v32 offset:34816\n"
      "ds_read_b128 a[40:43], v33 offset:32768\n"
      "ds_read_b128 a[44:47], v33 offset:34816\n"
      "buffer_load_dwordx4 v40, s[20:23], s61 offen offset:0 lds\n"
      "buffer_load_dwordx4 v41, s[20:23], s61 offen offset:1024 lds\n"
      "buffer_load_dwordx4 v42, s[20:23], s61 offen offset:2048 lds\n"
      "buffer_load_dwordx4 v43, s[20:23], s61 offen offset:3072 lds\n"
      "s_addk_i32 s61, 128\n"
      "s_addk_i32 m0, 0x4000\n"
      "s_cmp_eq_u32 s61, s11\n"
      "s_cselect_b32 s61, s98, s61\n"
      "v_mfma_f32_4x4x4_16b_bf16 v[48:51], v[30:31], a[16:17], v[48:51]\n"
      "v_mfma_f32_4x4x4_16b_bf16 v[56:59], a[16:17], a[16:17], v[56:59]\n"
      "v_mfma_f32_4x4x4_16b_bf16 v[52:55], v[30:31], a[20:21], v[52:55]\n"
      "v_mfma_f32_4x4x4_16b_bf16 v[60:63], a[20:21], a[20:21], v[60:63]\n"
      "v_mfma_f32_4x4x4_16b_bf16 v[48:51], v[30:31], a[18:19], v[48:51]\n"
      "v_mfma_f32_4x4x4_16b_bf16 v[56:59], a[18:19], a[18:19], v[56:59]\n"
      "v_mfma_f32_4x4x4_16b_bf16 v[52:55], v[30:31], a[22:23], v[52:55]\n"
      "v_mfma_f32_4x4x4_16b_bf16 v[60:63], a[22:23], a[22:23], v[60:63]\n"
      "v_mfma_f32_4x4x4_16b_bf16 v[48:51], v[30:31], a[24:25], v[48:51]\n"
      "v_mfma_f32_4x4x4_16b_bf16 v[56:59], a[24:25], a[24:25], v[56:59]\n"
      "v_mfma_f32_4x4x4_16b_bf16 v[52:55], v[30:31], a[28:29], v[52:55]\n"
      "v_mfma_f32_4x4x4_16b_bf16 v[60:63], a[28:29], a[28:29], v[60:63]\n"
      "v_mfma_f32_4x4x4_16b_bf16 v[48:51], v[30:31], a[26:27], v[48:51]\n"
      "v_mfma_f32_4x4x4_16b_bf16 v[56:59], a[26:27], a[26:27], v[56:59]\n"
      "v_mfma_f32_4x4x4_16b_bf16 v[52:55], v[30:31], a[30:31], v[52:55]\n"
      "v_mfma_f32_4x4x4_16b_bf16 v[60:63], a[30:31], a[30:31], v[60:63]\n"
      "s_lshl_b32 s57, s55, 10\n"
      "s_add_u32 s56, s56, s57\n"
      "s_add_u32 s84, s56, 1077248\n"
      "s_add_u32 s85, s84, 14336\n"
      "s_add_u32 s36, s36, s38\n"
      "s_addc_u32 s37, s37, 0\n"
      "s_add_u32 s40, s40, s42\n"
      "s_addc_u32 s41, s41, 0\n"
      "s_waitcnt vmcnt(8) lgkmcnt(0)\n"
      "ds_read_b128 a[48:51], v32 offset:49152\n"
      "ds_read_b128 a[52:55], v32 offset:51200\n"
      "ds_read_b128 a[56:59], v33 offset:49152\n"
      "ds_read_b128 a[60:63], v33 offset:51200\n"
      "buffer_load_dwordx4 v40, s[20:23], s61 offen offset:0 lds\n"
      "buffer_load_dwordx4 v41, s[20:23], s61 offen offset:1024 lds\n"
      "buffer_load_dwordx4 v42, s[20:23], s61 offen offset:2048 lds\n"
      "buffer_load_dwordx4 v43, s[20:23], s61 offen offset:3072 lds\n"
      "v_mfma_f32_4x4x4_16b_bf16 v[48:51], v[30:31], a[32:33], v[48:51]\n"
      "v_mfma_f32_4x4x4_16b_bf16 v[56:59], a[32:33], a[32:33], v[56:59]\n"
      "v_mfma_f32_4x4x4_16b_bf16 v[52:55], v[30:31], a[36:37], v[52:55]\n"
      "v_mfma_f32_4x4x4_16b_bf16 v[60:63], a[36:37], a[36:37], v[60:63]\n"
      "v_mfma_f32_4x4x4_16b_bf16 v[48:51], v[30:31], a[34:35], v[48:51]\n"
      "v_mfma_f32_4x4x4_16b_bf16 v[56:59], a[34:35], a[34:35], v[56:59]\n"
      "v_mfma_f32_4x4x4_16b_bf16 v[52:55], v[30:31], a[38:39], v[52:55]\n"
      "v_mfma_f32_4x4x4_16b_bf16 v[60:63], a[38:39], a[38:39], v[60:63]\n"
      "v_mfma_f32_4x4x4_16b_bf16 v[48:51], v[30:31], a[40:41], v[48:51]\n"
      "v_mfma_f32_4x4x4_16b_bf16 v[56:59], a[40:41], a[40:41], v[56:59]\n"
      "v_mfma_f32_4x4x4_16b_bf16 v[52:55], v[30:31], a[44:45], v[52:55]\n"
      "v_mfma_f32_4x4x4_16b_bf16 v[60:63], a[44:45], a[44:45], v[60:63]\n"
      "v_mfma_f32_4x4x4_16b_bf16 v[48:51], v[30:31], a[42:43], v[48:51]\n"
      "v_mfma_f32_4x4x4_16b_bf16 v[56:59], a[42:43], a[42:43], v[56:59]\n"
      "v_mfma_f32_4x4x4_16b_bf16 v[52:55], v[30:31], a[46:47], v[52:55]\n"
      "v_mfma_f32_4x4x4_16b_bf16 v[60:63], a[46:47], a[46:47], v[60:63]\n"
      "s_addk_i32 s10, 128\n"
      "s_waitcnt vmcnt(8) lgkmcnt(0)\n"
      "ds_read_b128 a[64:67], v34 offset:0\n"
      "ds_read_b128 a[68:71], v34 offset:2048\n"
      "ds_read_b128 a[72:75], v35 offset:0\n"
      "ds_read_b128 a[76:79], v35 offset:2048\n"
      "s_add_u32 m0, s95, 114688\n"
      "v_add_u32 v19, s34, v3\n"
      "v_xor_b32 v18, 64, v19\n"
      "buffer_load_dwordx4 v19, s[24:27], 0 offen offset:0 lds\n"
      "buffer_load_dwordx4 v18, s[24:27], s62 offen offset:1024 lds\n"
      "buffer_load_dwordx4 v19, s[24:27], s63 offen offset:2048 lds\n"
      "buffer_load_dwordx4 v18, s[24:27], s64 offen offset:3072 lds\n"
      "v_mfma_f32_4x4x4_16b_bf16 v[48:51], v[30:31], a[48:49], v[48:51]\n"
      "v_mfma_f32_4x4x4_16b_bf16 v[56:59], a[48:49], a[48:49], v[56:59]\n"
      "v_mfma_f32_4x4x4_16b_bf16 v[52:55], v[30:31], a[52:53], v[52:55]\n"
      "v_mfma_f32_4x4x4_16b_bf16 v[60:63], a[52:53], a[52:53], v[60:63]\n"
      "v_mfma_f32_4x4x4_16b_bf16 v[48:51], v[30:31], a[50:51], v[48:51]\n"
      "v_mfma_f32_4x4x4_16b_bf16 v[56:59], a[50:51], a[50:51], v[56:59]\n"
      "v_mfma_f32_4x4x4_16b_bf16 v[52:55], v[30:31], a[54:55], v[52:55]\n"
      "v_mfma_f32_4x4x4_16b_bf16 v[60:63], a[54:55], a[54:55], v[60:63]\n"
      "v_mfma_f32_4x4x4_16b_bf16 v[48:51], v[30:31], a[56:57], v[48:51]\n"
      "v_mfma_f32_4x4x4_16b_bf16 v[56:59], a[56:57], a[56:57], v[56:59]\n"
      "v_mfma_f32_4x4x4_16b_bf16 v[52:55], v[30:31], a[60:61], v[52:55]\n"
      "v_mfma_f32_4x4x4_16b_bf16 v[60:63], a[60:61], a[60:61], v[60:63]\n"
      "v_mfma_f32_4x4x4_16b_bf16 v[48:51], v[30:31], a[58:59], v[48:51]\n"
      "v_mfma_f32_4x4x4_16b_bf16 v[56:59], a[58:59], a[58:59], v[56:59]\n"
      "v_mfma_f32_4x4x4_16b_bf16 v[52:55], v[30:31], a[62:63], v[52:55]\n"
      "v_mfma_f32_4x4x4_16b_bf16 v[60:63], a[62:63], a[62:63], v[60:63]\n"
      "s_waitcnt vmcnt(8) lgkmcnt(0)\n"
      "ds_read_b128 a[80:83], v34 offset:16384\n"
      "ds_read_b128 a[84:87], v34 offset:18432\n"
      "ds_read_b128 a[88:91], v35 offset:16384\n"
      "ds_read_b128 a[92:95], v35 offset:18432\n"
      "s_add_u32 m0, s95, 131072\n"
      "v_add_u32 v19, s86, v3\n"
      "v_xor_b32 v18, 64, v19\n"
      "buffer_load_dwordx4 v19, s[24:27], 0 offen offset:0 lds\n"
      "buffer_load_dwordx4 v18, s[24:27], s62 offen offset:1024 lds\n"
      "buffer_load_dwordx4 v19, s[24:27], s63 offen offset:2048 lds\n"
      "buffer_load_dwordx4 v18, s[24:27], s64 offen offset:3072 lds\n"
      "v_mfma_f32_4x4x4_16b_bf16 v[48:51], v[30:31], a[64:65], v[48:51]\n"
      "v_mfma_f32_4x4x4_16b_bf16 v[56:59], a[64:65], a[64:65], v[56:59]\n"
      "v_mfma_f32_4x4x4_16b_bf16 v[52:55], v[30:31], a[68:69], v[52:55]\n"
      "v_mfma_f32_4x4x4_16b_bf16 v[60:63], a[68:69], a[68:69], v[60:63]\n"
      "v_mfma_f32_4x4x4_16b_bf16 v[48:51], v[30:31], a[66:67], v[48:51]\n"
      "v_mfma_f32_4x4x4_16b_bf16 v[56:59], a[66:67], a[66:67], v[56:59]\n"
      "v_mfma_f32_4x4x4_16b_bf16 v[52:55], v[30:31], a[70:71], v[52:55]\n"
      "v_mfma_f32_4x4x4_16b_bf16 v[60:63], a[70:71], a[70:71], v[60:63]\n"
      "v_mfma_f32_4x4x4_16b_bf16 v[48:51], v[30:31], a[72:73], v[48:51]\n"
      "v_mfma_f32_4x4x4_16b_bf16 v[56:59], a[72:73], a[72:73], v[56:59]\n"
      "v_mfma_f32_4x4x4_16b_bf16 v[52:55], v[30:31], a[76:77], v[52:55]\n"
      "v_mfma_f32_4x4x4_16b_bf16 v[60:63], a[76:77], a[76:77], v[60:63]\n"
      "v_mfma_f32_4x4x4_16b_bf16 v[48:51], v[30:31], a[74:75], v[48:51]\n"
      "v_mfma_f32_4x4x4_16b_bf16 v[56:59], a[74:75], a[74:75], v[56:59]\n"
      "v_mfma_f32_4x4x4_16b_bf16 v[52:55], v[30:31], a[78:79], v[52:55]\n"
      "v_mfma_f32_4x4x4_16b_bf16 v[60:63], a[78:79], a[78:79], v[60:63]\n"
      "s_waitcnt vmcnt(8) lgkmcnt(0)\n"
      "ds_read_b128 a[96:99], v34 offset:32768\n"
      "ds_read_b128 a[100:103], v34 offset:34816\n"
      "ds_read_b128 a[104:107], v35 offset:32768\n"
      "ds_read_b128 a[108:111], v35 offset:34816\n"
      "ds_read_b64 a[208:209], v44 offset:0\n"
      "ds_read_b64 a[212:213], v44 offset:2048\n"
      "ds_read_b64 a[210:211], v46 offset:0\n"
      "ds_read_b64 a[214:215], v46 offset:2048\n"
      "v_mfma_f32_4x4x4_16b_bf16 v[48:51], v[30:31], a[80:81], v[48:51]\n"
      "v_mfma_f32_4x4x4_16b_bf16 v[56:59], a[80:81], a[80:81], v[56:59]\n"
      "v_mfma_f32_4x4x4_16b_bf16 v[52:55], v[30:31], a[84:85], v[52:55]\n"
      "v_mfma_f32_4x4x4_16b_bf16 v[60:63], a[84:85], a[84:85], v[60:63]\n"
      "v_mfma_f32_4x4x4_16b_bf16 v[48:51], v[30:31], a[82:83], v[48:51]\n"
      "v_mfma_f32_4x4x4_16b_bf16 v[56:59], a[82:83], a[82:83], v[56:59]\n"
      "v_mfma_f32_4x4x4_16b_bf16 v[52:55], v[30:31], a[86:87], v[52:55]\n"
      "v_mfma_f32_4x4x4_16b_bf16 v[60:63], a[86:87], a[86:87], v[60:63]\n"
      "v_mfma_f32_4x4x4_16b_bf16 v[48:51], v[30:31], a[88:89], v[48:51]\n"
      "v_mfma_f32_4x4x4_16b_bf16 v[56:59], a[88:89], a[88:89], v[56:59]\n"
      "v_mfma_f32_4x4x4_16b_bf16 v[52:55], v[30:31], a[92:93], v[52:55]\n"
      "v_mfma_f32_4x4x4_16b_bf16 v[60:63], a[92:93], a[92:93], v[60:63]\n"
      "v_mfma_f32_4x4x4_16b_bf16 v[48:51], v[30:31], a[90:91], v[48:51]\n"
      "v_mfma_f32_4x4x4_16b_bf16 v[56:59], a[90:91], a[90:91], v[56:59]\n"
      "v_mfma_f32_4x4x4_16b_bf16 v[52:55], v[30:31], a[94:95], v[52:55]\n"
      "v_mfma_f32_4x4x4_16b_bf16 v[60:63], a[94:95], a[94:95], v[60:63]\n"
      "s_waitcnt lgkmcnt(4)\n"
      "v_mfma_f32_4x4x4_16b_bf16 v[48:51], v[30:31], a[96:97], v[48:51]\n"
      "v_mfma_f32_4x4x4_16b_bf16 v[56:59], a[96:97], a[96:97], v[56:59]\n"
      "v_mfma_f32_4x4x4_16b_bf16 v[52:55], v[30:31], a[100:101], v[52:55]\n"
      "v_mfma_f32_4x4x4_16b_bf16 v[60:63], a[100:101], a[100:101], v[60:63]\n"
      "v_mfma_f32_4x4x4_16b_bf16 v[48:51], v[30:31], a[98:99], v[48:51]\n"
      "v_mfma_f32_4x4x4_16b_bf16 v[56:59], a[98:99], a[98:99], v[56:59]\n"
      "v_mfma_f32_4x4x4_16b_bf16 v[52:55], v[30:31], a[102:103], v[52:55]\n"
      "v_mfma_f32_4x4x4_16b_bf16 v[60:63], a[102:103], a[102:103], v[60:63]\n"
      "v_mfma_f32_4x4x4_16b_bf16 v[48:51], v[30:31], a[104:105], v[48:51]\n"
      "v_mfma_f32_4x4x4_16b_bf16 v[56:59], a[104:105], a[104:105], v[56:59]\n"
      "v_mfma_f32_4x4x4_16b_bf16 v[52:55], v[30:31], a[108:109], v[52:55]\n"
      "v_mfma_f32_4x4x4_16b_bf16 v[60:63], a[108:109], a[108:109], v[60:63]\n"
      "v_mfma_f32_4x4x4_16b_bf16 v[48:51], v[30:31], a[106:107], v[48:51]\n"
      "v_mfma_f32_4x4x4_16b_bf16 v[56:59], a[106:107], a[106:107], v[56:59]\n"
      "v_mfma_f32_4x4x4_16b_bf16 v[52:55], v[30:31], a[110:111], v[52:55]\n"
      "v_mfma_f32_4x4x4_16b_bf16 v[60:63], a[110:111], a[110:111], v[60:63]\n"
      "s_nop 3\n"
      "v_permlane16_swap_b32 v48, v52\n"
      "v_add_f32 v32, v48, v52\n"
      "v_add_f32 v33, v48, v52\n"
      "v_xor_b32 v9, 0x18000, v6\n"
      "v_xor_b32 v10, 0x18020, v6\n"
      "v_permlane32_swap_b32 v32, v33\n"
      "v_add_f32 v32, v32, v33\n"
      "v_mul_f32 v34, 0xbb124925, v32\n"
      "v_fmamk_f32 v32, v34, 0x43e00000, v32\n"
      "v_fmamk_f32 v34, v32, 0xbb124925, v34\n"
      "v_cvt_pk_bf16_f32 v24, 0, v34\n"
      "v_cvt_pk_bf16_f32 v40, 0, v34\n"
      "v_xor_b32 v11, 0x18040, v6\n"
      "v_xor_b32 v12, 0x18060, v6\n"
      "v_permlane16_swap_b32 v24, v40\n"
      "v_mov_b32 v25, v24\n"
      "v_mov_b32 v26, v24\n"
      "v_mov_b32 v27, v24\n"
      "v_mov_b32 v41, v40\n"
      "v_mov_b32 v42, v40\n"
      "v_mov_b32 v43, v40\n"
      "v_mfma_f32_4x4x4_16b_bf16 v[112:115], v[28:29], a[0:1], v[24:27]\n"
      "v_mfma_f32_4x4x4_16b_bf16 v[116:119], v[28:29], a[2:3], v[24:27]\n"
      "v_mfma_f32_4x4x4_16b_bf16 v[120:123], v[28:29], a[4:5], v[40:43]\n"
      "v_mfma_f32_4x4x4_16b_bf16 v[124:127], v[28:29], a[6:7], v[40:43]\n"
      "v_mfma_f32_4x4x4_16b_bf16 v[128:131], v[28:29], a[8:9], v[24:27]\n"
      "v_mfma_f32_4x4x4_16b_bf16 v[132:135], v[28:29], a[10:11], v[24:27]\n"
      "v_mfma_f32_4x4x4_16b_bf16 v[136:139], v[28:29], a[12:13], v[40:43]\n"
      "v_mfma_f32_4x4x4_16b_bf16 v[140:143], v[28:29], a[14:15], v[40:43]\n"
      "v_cvt_pk_bf16_f32 v144, v112, v113\n"
      "v_cvt_pk_bf16_f32 v145, v114, v115\n"
      "v_cvt_pk_bf16_f32 v146, v116, v117\n"
      "v_cvt_pk_bf16_f32 v147, v118, v119\n"
      "v_cvt_pk_bf16_f32 v148, v120, v121\n"
      "v_cvt_pk_bf16_f32 v149, v122, v123\n"
      "v_cvt_pk_bf16_f32 v150, v124, v125\n"
      "v_cvt_pk_bf16_f32 v151, v126, v127\n"
      "v_mfma_f32_4x4x4_16b_bf16 v[112:115], v[28:29], a[16:17], v[24:27]\n"
      ".LPAD7:\n"
      "s_cbranch_execz .LPAD3\n"
      "v_mfma_f32_4x4x4_16b_bf16 v[116:119], v[28:29], a[18:19], v[24:27]\n"
      "v_mfma_f32_4x4x4_16b_bf16 v[120:123], v[28:29], a[20:21], v[40:43]\n"
      "v_mfma_f32_4x4x4_16b_bf16 v[124:127], v[28:29], a[22:23], v[40:43]\n"
      "v_cvt_pk_bf16_f32 v152, v128, v129\n"
      "v_cvt_pk_bf16_f32 v153, v130, v131\n"
      "v_cvt_pk_bf16_f32 v154, v132, v133\n"
      "v_cvt_pk_bf16_f32 v155, v134, v135\n"
      "v_cvt_pk_bf16_f32 v156, v136, v137\n"
      "v_cvt_pk_bf16_f32 v157, v138, v139\n"
      "v_cvt_pk_bf16_f32 v158, v140, v141\n"
      "v_cvt_pk_bf16_f32 v159, v142, v143\n"
      "v_mfma_f32_4x4x4_16b_bf16 v[128:131], v[28:29], a[24:25], v[24:27]\n"
      "v_mfma_f32_4x4x4_16b_bf16 v[132:135], v[28:29], a[26:27], v[24:27]\n"
      "v_mfma_f32_4x4x4_16b_bf16 v[136:139], v[28:29], a[28:29], v[40:43]\n"
      "v_mfma_f32_4x4x4_16b_bf16 v[140:143], v[28:29], a[30:31], v[40:43]\n"
      "s_waitcnt vmcnt(4) lgkmcnt(0)\n"
      "s_barrier\n"
      "ds_read_b64_tr_b16 a[216:217], v9 offset:16384\n"
      "ds_read_b64_tr_b16 a[218:219], v9 offset:16896\n"
      "ds_read_b64_tr_b16 a[220:221], v10 offset:16384\n"
      "ds_read_b64_tr_b16 a[222:223], v10 offset:16896\n"
      "ds_read_b64_tr_b16 a[224:225], v11 offset:16384\n"
      "ds_read_b64_tr_b16 a[226:227], v11 offset:16896\n"
      "ds_read_b64_tr_b16 a[228:229], v12 offset:16384\n"
      "ds_read_b64_tr_b16 a[230:231], v12 offset:16896\n"
      "ds_read_b64_tr_b16 a[232:233], v9 offset:20480\n"
      "ds_read_b64_tr_b16 a[234:235], v9 offset:20992\n"
      "ds_read_b64_tr_b16 a[236:237], v10 offset:20480\n"
      "ds_read_b64_tr_b16 a[238:239], v10 offset:20992\n"
      "ds_read_b64_tr_b16 a[240:241], v11 offset:20480\n"
      "ds_read_b64_tr_b16 a[242:243], v11 offset:20992\n"
      "s_waitcnt lgkmcnt(6)\n"
      "v_mfma_f32_16x16x32_bf16 v[80:83], v[144:147], a[216:219], 0\n"
      "ds_read_b64_tr_b16 a[244:245], v12 offset:20480\n"
      "ds_read_b64_tr_b16 a[246:247], v12 offset:20992\n"
      "v_mfma_f32_16x16x32_bf16 v[88:91], v[148:151], a[216:219], 0\n"
      "s_add_u32 m0, s95, 147456\n"
      "v_add_u32 v19, s87, v3\n"
      "v_xor_b32 v18, 64, v19\n"
      "buffer_load_dwordx4 v19, s[24:27], 0 offen offset:0 lds\n"
      "v_cvt_pk_bf16_f32 v160, v112, v113\n"
      "v_cvt_pk_bf16_f32 v161, v114, v115\n"
      "v_mfma_f32_16x16x32_bf16 v[84:87], v[144:147], a[220:223], 0\n"
      "ds_read_b64_tr_b16 a[248:249], v9 offset:24576\n"
      "ds_read_b64_tr_b16 a[250:251], v9 offset:25088\n"
      "v_mfma_f32_16x16x32_bf16 v[92:95], v[148:151], a[220:223], 0\n"
      "v_cvt_pk_bf16_f32 v162, v116, v117\n"
      "v_cvt_pk_bf16_f32 v163, v118, v119\n"
      "v_bfe_i32 v35, v0, 0, 1\n"
      "v_bfe_i32 v36, v0, 1, 1\n"
      "v_mfma_f32_16x16x32_bf16 v[96:99], v[144:147], a[224:227], 0\n"
      "ds_read_b64_tr_b16 a[252:253], v10 offset:24576\n"
      "ds_read_b64_tr_b16 a[254:255], v10 offset:25088\n"
      "v_mfma_f32_16x16x32_bf16 v[104:107], v[148:151], a[224:227], 0\n"
      "buffer_load_dwordx4 v18, s[24:27], s62 offen offset:1024 lds\n"
      "v_cvt_pk_bf16_f32 v164, v120, v121\n"
      "v_cvt_pk_bf16_f32 v165, v122, v123\n"
      "v_bfi_b32 v56, v35, v57, v56\n"
      "v_mfma_f32_16x16x32_bf16 v[100:103], v[144:147], a[228:231], 0\n"
      "ds_read_b64_tr_b16 a[216:217], v11 offset:24576\n"
      "ds_read_b64_tr_b16 a[218:219], v11 offset:25088\n"
      "v_mfma_f32_16x16x32_bf16 v[108:111], v[148:151], a[228:231], 0\n"
      "v_cvt_pk_bf16_f32 v166, v124, v125\n"
      "v_cvt_pk_bf16_f32 v167, v126, v127\n"
      "v_bfi_b32 v58, v35, v59, v58\n"
      "v_bfi_b32 v60, v35, v61, v60\n"
      "s_waitcnt lgkmcnt(6)\n"
      "v_mfma_f32_16x16x32_bf16 v[80:83], v[152:155], a[232:235], v[80:83]\n"
      "ds_read_b64_tr_b16 a[220:221], v12 offset:24576\n"
      "ds_read_b64_tr_b16 a[222:223], v12 offset:25088\n"
      "v_mfma_f32_16x16x32_bf16 v[88:91], v[156:159], a[232:235], v[88:91]\n"
      "buffer_load_dwordx4 v19, s[24:27], s63 offen offset:2048 lds\n"
      "v_cvt_pk_bf16_f32 v168, v128, v129\n"
      "v_cvt_pk_bf16_f32 v169, v130, v131\n"
      "v_mfma_f32_16x16x32_bf16 v[84:87], v[152:155], a[236:239], v[84:87]\n"
      "ds_read_b64_tr_b16 a[224:225], v9 offset:28672\n"
      "ds_read_b64_tr_b16 a[226:227], v9 offset:29184\n"
      "v_mfma_f32_16x16x32_bf16 v[92:95], v[156:159], a[236:239], v[92:95]\n"
      "v_cvt_pk_bf16_f32 v170, v132, v133\n"
      "v_cvt_pk_bf16_f32 v171, v134, v135\n"
      "v_bfi_b32 v62, v35, v63, v62\n"
      "v_bfi_b32 v56, v36, v58, v56\n"
      "v_bfi_b32 v60, v36, v62, v60\n"
      "v_mfma_f32_16x16x32_bf16 v[96:99], v[152:155], a[240:243], v[96:99]\n"
      "ds_read_b64_tr_b16 a[228:229], v10 offset:28672\n"
      "ds_read_b64_tr_b16 a[230:231], v10 offset:29184\n"
      "v_mfma_f32_16x16x32_bf16 v[104:107], v[156:159], a[240:243], v[104:107]\n"
      "buffer_load_dwordx4 v18, s[24:27], s64 offen offset:3072 lds\n"
      "v_cvt_pk_bf16_f32 v172, v136, v137\n"
      "v_cvt_pk_bf16_f32 v173, v138, v139\n"
      "v_mfma_f32_16x16x32_bf16 v[100:103], v[152:155], a[244:247], v[100:103]\n"
      "ds_read_b64_tr_b16 a[232:233], v11 offset:28672\n"
      "ds_read_b64_tr_b16 a[234:235], v11 offset:29184\n"
      "v_mfma_f32_16x16x32_bf16 v[108:111], v[156:159], a[244:247], v[108:111]\n"
      "v_cvt_pk_bf16_f32 v174, v140, v141\n"
      "v_cvt_pk_bf16_f32 v175, v142, v143\n"
      "v_mul_f32 v34, v34, -v34\n"
      "v_permlane16_swap_b32 v56, v60\n"
      "v_mfma_f32_4x4x4_16b_bf16 v[112:115], v[28:29], a[32:33], v[24:27]\n"
      "v_mfma_f32_4x4x4_16b_bf16 v[116:119], v[28:29], a[34:35], v[24:27]\n"
      "v_mfma_f32_4x4x4_16b_bf16 v[120:123], v[28:29], a[36:37], v[40:43]\n"
      "v_mfma_f32_4x4x4_16b_bf16 v[124:127], v[28:29], a[38:39], v[40:43]\n"
      "s_waitcnt vmcnt(4) lgkmcnt(6)\n"
      "s_barrier\n"
      "v_mfma_f32_16x16x32_bf16 v[80:83], v[160:163], a[248:251], v[80:83]\n"
      "ds_read_b64_tr_b16 a[236:237], v12 offset:28672\n"
      "ds_read_b64_tr_b16 a[238:239], v12 offset:29184\n"
      "v_mfma_f32_16x16x32_bf16 v[88:91], v[164:167], a[248:251], v[88:91]\n"
      "s_lshr_b32 s35, s95, 1\n"
      "s_add_u32 m0, s35, 98304\n"
      "v_add_u32 v19, s99, v3\n"
      "v_xor_b32 v18, 64, v19\n"
      "buffer_load_dwordx4 v19, s[24:27], 0 offen offset:0 lds\n"
      "v_cvt_pk_bf16_f32 v176, v112, v113\n"
      "v_cvt_pk_bf16_f32 v177, v114, v115\n"
      "v_mfma_f32_16x16x32_bf16 v[84:87], v[160:163], a[252:255], v[84:87]\n"
      "ds_read_b64_tr_b16 a[240:241], v9 offset:32768\n"
      "ds_read_b64_tr_b16 a[242:243], v9 offset:33280\n"
      "v_mfma_f32_16x16x32_bf16 v[92:95], v[164:167], a[252:255], v[92:95]\n"
      "v_cvt_pk_bf16_f32 v178, v116, v117\n"
      "v_cvt_pk_bf16_f32 v179, v118, v119\n"
      "v_add_f32 v35, v56, v60\n"
      "v_add_f32 v36, v56, v60\n"
      "s_nop 1\n"
      "v_permlane32_swap_b32 v35, v36\n"
      "v_mfma_f32_16x16x32_bf16 v[96:99], v[160:163], a[216:219], v[96:99]\n"
      "ds_read_b64_tr_b16 a[244:245], v10 offset:32768\n"
      "ds_read_b64_tr_b16 a[246:247], v10 offset:33280\n"
      "v_mfma_f32_16x16x32_bf16 v[104:107], v[164:167], a[216:219], v[104:107]\n"
      "buffer_load_dwordx4 v18, s[24:27], s62 offen offset:1024 lds\n"
      "v_cvt_pk_bf16_f32 v180, v120, v121\n"
      "v_cvt_pk_bf16_f32 v181, v122, v123\n"
      "v_mfma_f32_16x16x32_bf16 v[100:103], v[160:163], a[220:223], v[100:103]\n"
      "ds_read_b64_tr_b16 a[248:249], v11 offset:32768\n"
      "ds_read_b64_tr_b16 a[250:251], v11 offset:33280\n"
      "v_mfma_f32_16x16x32_bf16 v[108:111], v[164:167], a[220:223], v[108:111]\n"
      "v_cvt_pk_bf16_f32 v182, v124, v125\n"
      "v_cvt_pk_bf16_f32 v183, v126, v127\n"
      "v_add_f32 v35, v35, v36\n"
      "v_fmamk_f32 v35, v35, 0x3b124925, v34\n"
      "v_max_f32 v35, 0, v35\n"
      "v_mfma_f32_4x4x4_16b_bf16 v[128:131], v[28:29], a[40:41], v[24:27]\n"
      "v_mfma_f32_4x4x4_16b_bf16 v[132:135], v[28:29], a[42:43], v[24:27]\n"
      "v_mfma_f32_4x4x4_16b_bf16 v[136:139], v[28:29], a[44:45], v[40:43]\n"
      "v_mfma_f32_4x4x4_16b_bf16 v[140:143], v[28:29], a[46:47], v[40:43]\n"
      "s_waitcnt lgkmcnt(6)\n"
      "v_mfma_f32_16x16x32_bf16 v[80:83], v[168:171], a[224:227], v[80:83]\n"
      "ds_read_b64_tr_b16 a[252:253], v12 offset:32768\n"
      "ds_read_b64_tr_b16 a[254:255], v12 offset:33280\n"
      "v_mfma_f32_16x16x32_bf16 v[88:91], v[172:175], a[224:227], v[88:91]\n"
      "s_add_u32 m0, s95, 0\n"
      "s_nop 0\n"
      "buffer_load_dwordx4 v4, s[28:31], 0 offen offset:0 lds\n"
      "v_cvt_pk_bf16_f32 v184, v128, v129\n"
      "v_cvt_pk_bf16_f32 v185, v130, v131\n"
      "v_mfma_f32_16x16x32_bf16 v[84:87], v[168:171], a[228:231], v[84:87]\n"
      "ds_read_b64_tr_b16 a[216:217], v9 offset:36864\n"
      "ds_read_b64_tr_b16 a[218:219], v9 offset:37376\n"
      "v_mfma_f32_16x16x32_bf16 v[92:95], v[172:175], a[228:231], v[92:95]\n"
      "v_cvt_pk_bf16_f32 v186, v132, v133\n"
      "v_cvt_pk_bf16_f32 v187, v134, v135\n"
      "v_cvt_pk_bf16_f32 v35, 0, v35\n"
      "v_add_f32 v35, 0x3727c5ac, v35\n"
      "v_mfma_f32_16x16x32_bf16 v[96:99], v[168:171], a[232:235], v[96:99]\n"
      "ds_read_b64_tr_b16 a[220:221], v10 offset:36864\n"
      "ds_read_b64_tr_b16 a[222:223], v10 offset:37376\n"
      "v_mfma_f32_16x16x32_bf16 v[104:107], v[172:175], a[232:235], v[104:107]\n"
      "buffer_load_dwordx4 v4, s[28:31], s65 offen offset:1024 lds\n"
      "v_cvt_pk_bf16_f32 v188, v136, v137\n"
      "v_cvt_pk_bf16_f32 v189, v138, v139\n"
      "v_cvt_pk_bf16_f32 v35, 0, v35\n"
      "v_mfma_f32_16x16x32_bf16 v[100:103], v[168:171], a[236:239], v[100:103]\n"
      "ds_read_b64_tr_b16 a[224:225], v11 offset:36864\n"
      "ds_read_b64_tr_b16 a[226:227], v11 offset:37376\n"
      "v_mfma_f32_16x16x32_bf16 v[108:111], v[172:175], a[236:239], v[108:111]\n"
      "v_cvt_pk_bf16_f32 v190, v140, v141\n"
      "v_cvt_pk_bf16_f32 v191, v142, v143\n"
      "v_sqrt_f32 v35, v35\n"
      "v_mfma_f32_4x4x4_16b_bf16 v[112:115], v[28:29], a[48:49], v[24:27]\n"
      "v_mfma_f32_4x4x4_16b_bf16 v[116:119], v[28:29], a[50:51], v[24:27]\n"
      "v_mfma_f32_4x4x4_16b_bf16 v[120:123], v[28:29], a[52:53], v[40:43]\n"
      "v_mfma_f32_4x4x4_16b_bf16 v[124:127], v[28:29], a[54:55], v[40:43]\n"
      "s_waitcnt lgkmcnt(6)\n"
      "v_mfma_f32_16x16x32_bf16 v[80:83], v[176:179], a[240:243], v[80:83]\n"
      "ds_read_b64_tr_b16 a[228:229], v12 offset:36864\n"
      "ds_read_b64_tr_b16 a[230:231], v12 offset:37376\n"
      "v_mfma_f32_16x16x32_bf16 v[88:91], v[180:183], a[240:243], v[88:91]\n"
      "buffer_load_dwordx4 v4, s[28:31], s66 offen offset:2048 lds\n"
      "v_cvt_pk_bf16_f32 v192, v112, v113\n"
      "v_cvt_pk_bf16_f32 v193, v114, v115\n"
      "v_mfma_f32_16x16x32_bf16 v[84:87], v[176:179], a[244:247], v[84:87]\n"
      "ds_read_b64_tr_b16 a[232:233], v9 offset:40960\n"
      "ds_read_b64_tr_b16 a[234:235], v9 offset:41472\n"
      "v_mfma_f32_16x16x32_bf16 v[92:95], v[180:183], a[244:247], v[92:95]\n"
      "v_cvt_pk_bf16_f32 v194, v116, v117\n"
      "v_cvt_pk_bf16_f32 v195, v118, v119\n"
      "v_cvt_pk_bf16_f32 v35, 0, v35\n"
      "v_rcp_f32 v23, v35\n"
      "v_mfma_f32_16x16x32_bf16 v[96:99], v[176:179], a[248:251], v[96:99]\n"
      "ds_read_b64_tr_b16 a[236:237], v10 offset:40960\n"
      "ds_read_b64_tr_b16 a[238:239], v10 offset:41472\n"
      "v_mfma_f32_16x16x32_bf16 v[104:107], v[180:183], a[248:251], v[104:107]\n"
      "buffer_load_dwordx4 v4, s[28:31], s67 offen offset:3072 lds\n"
      "v_cvt_pk_bf16_f32 v196, v120, v121\n"
      "v_cvt_pk_bf16_f32 v197, v122, v123\n"
      "v_mfma_f32_16x16x32_bf16 v[100:103], v[176:179], a[252:255], v[100:103]\n"
      "ds_read_b64_tr_b16 a[240:241], v11 offset:40960\n"
      "ds_read_b64_tr_b16 a[242:243], v11 offset:41472\n"
      "v_mfma_f32_16x16x32_bf16 v[108:111], v[180:183], a[252:255], v[108:111]\n"
      "v_cvt_pk_bf16_f32 v198, v124, v125\n"
      "v_cvt_pk_bf16_f32 v199, v126, v127\n"
      "v_mul_f32 v22, v35, v35\n"
      "v_mul_f32 v20, 0xc0135761, v23\n"
      "v_mfma_f32_4x4x4_16b_bf16 v[128:131], v[28:29], a[56:57], v[24:27]\n"
      "v_mfma_f32_4x4x4_16b_bf16 v[132:135], v[28:29], a[58:59], v[24:27]\n"
      "v_mfma_f32_4x4x4_16b_bf16 v[136:139], v[28:29], a[60:61], v[40:43]\n"
      "v_mfma_f32_4x4x4_16b_bf16 v[140:143], v[28:29], a[62:63], v[40:43]\n"
      "s_waitcnt lgkmcnt(6)\n"
      "v_mfma_f32_16x16x32_bf16 v[80:83], v[184:187], a[216:219], v[80:83]\n"
      "ds_read_b64_tr_b16 a[244:245], v12 offset:40960\n"
      "ds_read_b64_tr_b16 a[246:247], v12 offset:41472\n"
      "v_mfma_f32_16x16x32_bf16 v[88:91], v[188:191], a[216:219], v[88:91]\n"
      "s_add_u32 m0, s95, 16384\n"
      "v_add_u32 v4, s16, v4\n"
      "buffer_load_dwordx4 v4, s[28:31], 0 offen offset:0 lds\n"
      "v_cvt_pk_bf16_f32 v200, v128, v129\n"
      "v_cvt_pk_bf16_f32 v201, v130, v131\n"
      "v_mfma_f32_16x16x32_bf16 v[84:87], v[184:187], a[220:223], v[84:87]\n"
      "ds_read_b64_tr_b16 a[248:249], v9 offset:45056\n"
      "ds_read_b64_tr_b16 a[250:251], v9 offset:45568\n"
      "v_mfma_f32_16x16x32_bf16 v[92:95], v[188:191], a[220:223], v[92:95]\n"
      "v_cvt_pk_bf16_f32 v202, v132, v133\n"
      "v_cvt_pk_bf16_f32 v203, v134, v135\n"
      "v_mul_f32 v36, v23, v23\n"
      "v_mul_f32 v36, 0x3d372713, v36\n"
      "v_mul_f32 v21, v20, v36\n"
      "v_mfma_f32_16x16x32_bf16 v[96:99], v[184:187], a[224:227], v[96:99]\n"
      "ds_read_b64_tr_b16 a[252:253], v10 offset:45056\n"
      "ds_read_b64_tr_b16 a[254:255], v10 offset:45568\n"
      "v_mfma_f32_16x16x32_bf16 v[104:107], v[188:191], a[224:227], v[104:107]\n"
      "buffer_load_dwordx4 v4, s[28:31], s65 offen offset:1024 lds\n"
      "v_cvt_pk_bf16_f32 v204, v136, v137\n"
      "v_cvt_pk_bf16_f32 v205, v138, v139\n"
      "v_mfma_f32_16x16x32_bf16 v[100:103], v[184:187], a[228:231], v[100:103]\n"
      "ds_read_b64_tr_b16 a[216:217], v11 offset:45056\n"
      "ds_read_b64_tr_b16 a[218:219], v11 offset:45568\n"
      "v_mfma_f32_16x16x32_bf16 v[108:111], v[188:191], a[228:231], v[108:111]\n"
      "v_cvt_pk_bf16_f32 v206, v140, v141\n"
      "v_cvt_pk_bf16_f32 v207, v142, v143\n"
      "s_lshr_b32 s35, s95, 2\n"
      "s_add_u32 s35, s35, 122880\n"
      "v_and_b32 v35, 31, v0\n"
      "v_lshl_add_u32 v35, v35, 2, s35\n"
      "v_and_b32 v36, 48, v0\n"
      "v_add_u32 v36, s35, v36\n"
      "v_mfma_f32_4x4x4_16b_bf16 v[112:115], v[28:29], a[64:65], v[24:27]\n"
      "v_mfma_f32_4x4x4_16b_bf16 v[116:119], v[28:29], a[66:67], v[24:27]\n"
      "v_mfma_f32_4x4x4_16b_bf16 v[120:123], v[28:29], a[68:69], v[40:43]\n"
      "v_mfma_f32_4x4x4_16b_bf16 v[124:127], v[28:29], a[70:71], v[40:43]\n"
      "s_waitcnt vmcnt(6) lgkmcnt(6)\n"
      "s_barrier\n"
      "v_mfma_f32_16x16x32_bf16 v[80:83], v[192:195], a[232:235], v[80:83]\n"
      "ds_read_b64_tr_b16 a[220:221], v12 offset:45056\n"
      "ds_read_b64_tr_b16 a[222:223], v12 offset:45568\n"
      "v_mfma_f32_16x16x32_bf16 v[88:91], v[196:199], a[232:235], v[88:91]\n"
      "ds_write_b32 v35, v23\n"
      "buffer_load_dwordx4 v4, s[28:31], s66 offen offset:2048 lds\n"
      "v_cvt_pk_bf16_f32 v208, v112, v113\n"
      "v_cvt_pk_bf16_f32 v209, v114, v115\n"
      "v_mfma_f32_16x16x32_bf16 v[84:87], v[192:195], a[236:239], v[84:87]\n"
      "ds_read_b64_tr_b16 a[224:225], v9 offset:49152\n"
      "ds_read_b64_tr_b16 a[226:227], v9 offset:49664\n"
      "v_mfma_f32_16x16x32_bf16 v[92:95], v[196:199], a[236:239], v[92:95]\n"
      "ds_read_b128 v[64:67], v36 offset:0\n"
      "v_cvt_pk_bf16_f32 v210, v116, v117\n"
      "v_cvt_pk_bf16_f32 v211, v118, v119\n"
      "s_waitcnt lgkmcnt(8)\n"
      "v_mfma_f32_16x16x32_bf16 v[96:99], v[192:195], a[240:243], v[96:99]\n"
      "ds_read_b64_tr_b16 a[228:229], v10 offset:49152\n"
      "ds_read_b64_tr_b16 a[230:231], v10 offset:49664\n"
      "v_mfma_f32_16x16x32_bf16 v[104:107], v[196:199], a[240:243], v[104:107]\n"
      "ds_read_b128 v[68:71], v36 offset:64\n"
      "buffer_load_dwordx4 v4, s[28:31], s67 offen offset:3072 lds\n"
      "v_cvt_pk_bf16_f32 v212, v120, v121\n"
      "v_cvt_pk_bf16_f32 v213, v122, v123\n"
      "v_mfma_f32_16x16x32_bf16 v[100:103], v[192:195], a[244:247], v[100:103]\n"
      "ds_read_b64_tr_b16 a[232:233], v11 offset:49152\n"
      "ds_read_b64_tr_b16 a[234:235], v11 offset:49664\n"
      "v_mfma_f32_16x16x32_bf16 v[108:111], v[196:199], a[244:247], v[108:111]\n"
      "v_cvt_pk_bf16_f32 v214, v124, v125\n"
      "v_cvt_pk_bf16_f32 v215, v126, v127\n"
      "v_mfma_f32_4x4x4_16b_bf16 v[128:131], v[28:29], a[72:73], v[24:27]\n"
      "v_mfma_f32_4x4x4_16b_bf16 v[132:135], v[28:29], a[74:75], v[24:27]\n"
      "v_mfma_f32_4x4x4_16b_bf16 v[136:139], v[28:29], a[76:77], v[40:43]\n"
      "v_mfma_f32_4x4x4_16b_bf16 v[140:143], v[28:29], a[78:79], v[40:43]\n"
      "s_waitcnt lgkmcnt(9)\n"
      "v_mfma_f32_16x16x32_bf16 v[80:83], v[200:203], a[248:251], v[80:83]\n"
      "ds_read_b64_tr_b16 a[236:237], v12 offset:49152\n"
      "ds_read_b64_tr_b16 a[238:239], v12 offset:49664\n"
      "v_mfma_f32_16x16x32_bf16 v[88:91], v[204:207], a[248:251], v[88:91]\n"
      "s_add_u32 m0, s95, 32768\n"
      "v_add_u32 v4, s17, v4\n"
      "buffer_load_dwordx4 v4, s[28:31], 0 offen offset:0 lds\n"
      "v_cvt_pk_bf16_f32 v216, v128, v129\n"
      "v_cvt_pk_bf16_f32 v217, v130, v131\n"
      "v_mfma_f32_16x16x32_bf16 v[84:87], v[200:203], a[252:255], v[84:87]\n"
      "ds_read_b64_tr_b16 a[240:241], v9 offset:53248\n"
      "ds_read_b64_tr_b16 a[242:243], v9 offset:53760\n"
      "v_mfma_f32_16x16x32_bf16 v[92:95], v[204:207], a[252:255], v[92:95]\n"
      "v_cvt_pk_bf16_f32 v218, v132, v133\n"
      "v_cvt_pk_bf16_f32 v219, v134, v135\n"
      "s_waitcnt lgkmcnt(7)\n"
      "v_mfma_f32_16x16x32_bf16 v[96:99], v[200:203], a[216:219], v[96:99]\n"
      "ds_read_b64_tr_b16 a[244:245], v10 offset:53248\n"
      "ds_read_b64_tr_b16 a[246:247], v10 offset:53760\n"
      "v_mfma_f32_16x16x32_bf16 v[104:107], v[204:207], a[216:219], v[104:107]\n"
      "v_cvt_pk_bf16_f32 v220, v136, v137\n"
      "v_cvt_pk_bf16_f32 v221, v138, v139\n"
      "v_mfma_f32_16x16x32_bf16 v[100:103], v[200:203], a[220:223], v[100:103]\n"
      "ds_read_b64_tr_b16 a[248:249], v11 offset:53248\n"
      "ds_read_b64_tr_b16 a[250:251], v11 offset:53760\n"
      "v_mfma_f32_16x16x32_bf16 v[108:111], v[204:207], a[220:223], v[108:111]\n"
      "buffer_load_dwordx4 v4, s[28:31], s65 offen offset:1024 lds\n"
      "v_cvt_pk_bf16_f32 v222, v140, v141\n"
      "v_cvt_pk_bf16_f32 v223, v142, v143\n"
      "v_mfma_f32_4x4x4_16b_bf16 v[112:115], v[28:29], a[80:81], v[24:27]\n"
      "v_mfma_f32_4x4x4_16b_bf16 v[116:119], v[28:29], a[82:83], v[24:27]\n"
      "v_mfma_f32_4x4x4_16b_bf16 v[120:123], v[28:29], a[84:85], v[40:43]\n"
      "v_mfma_f32_4x4x4_16b_bf16 v[124:127], v[28:29], a[86:87], v[40:43]\n"
      "s_waitcnt lgkmcnt(6)\n"
      "v_mfma_f32_16x16x32_bf16 v[80:83], v[208:211], a[224:227], v[80:83]\n"
      "ds_read_b64_tr_b16 a[252:253], v12 offset:53248\n"
      "ds_read_b64_tr_b16 a[254:255], v12 offset:53760\n"
      "v_mfma_f32_16x16x32_bf16 v[88:91], v[212:215], a[224:227], v[88:91]\n"
      "v_cvt_pk_bf16_f32 v224, v112, v113\n"
      "v_cvt_pk_bf16_f32 v225, v114, v115\n"
      "v_mfma_f32_16x16x32_bf16 v[84:87], v[208:211], a[228:231], v[84:87]\n"
      "ds_read_b64_tr_b16 a[216:217], v9 offset:57344\n"
      "ds_read_b64_tr_b16 a[218:219], v9 offset:57856\n"
      "v_mfma_f32_16x16x32_bf16 v[92:95], v[212:215], a[228:231], v[92:95]\n"
      "buffer_load_dwordx4 v4, s[28:31], s66 offen offset:2048 lds\n"
      "v_cvt_pk_bf16_f32 v226, v116, v117\n"
      "v_cvt_pk_bf16_f32 v227, v118, v119\n"
      "s_waitcnt lgkmcnt(6)\n"
      "v_mfma_f32_16x16x32_bf16 v[96:99], v[208:211], a[232:235], v[96:99]\n"
      "ds_read_b64_tr_b16 a[220:221], v10 offset:57344\n"
      "ds_read_b64_tr_b16 a[222:223], v10 offset:57856\n"
      "v_mfma_f32_16x16x32_bf16 v[104:107], v[212:215], a[232:235], v[104:107]\n"
      "v_cvt_pk_bf16_f32 v228, v120, v121\n"
      "v_cvt_pk_bf16_f32 v229, v122, v123\n"
      "v_mfma_f32_16x16x32_bf16 v[100:103], v[208:211], a[236:239], v[100:103]\n"
      "ds_read_b64_tr_b16 a[224:225], v11 offset:57344\n"
      "ds_read_b64_tr_b16 a[226:227], v11 offset:57856\n"
      "v_mfma_f32_16x16x32_bf16 v[108:111], v[212:215], a[236:239], v[108:111]\n"
      "buffer_load_dwordx4 v4, s[28:31], s67 offen offset:3072 lds\n"
      "v_cvt_pk_bf16_f32 v230, v124, v125\n"
      "v_cvt_pk_bf16_f32 v231, v126, v127\n"
      "v_mfma_f32_4x4x4_16b_bf16 v[128:131], v[28:29], a[88:89], v[24:27]\n"
      "v_mfma_f32_4x4x4_16b_bf16 v[132:135], v[28:29], a[90:91], v[24:27]\n"
      "v_mfma_f32_4x4x4_16b_bf16 v[136:139], v[28:29], a[92:93], v[40:43]\n"
      "v_mfma_f32_4x4x4_16b_bf16 v[140:143], v[28:29], a[94:95], v[40:43]\n"
      "s_waitcnt lgkmcnt(6)\n"
      "v_mfma_f32_16x16x32_bf16 v[80:83], v[216:219], a[240:243], v[80:83]\n"
      "ds_read_b64_tr_b16 a[228:229], v12 offset:57344\n"
      "ds_read_b64_tr_b16 a[230:231], v12 offset:57856\n"
      "v_mfma_f32_16x16x32_bf16 v[88:91], v[220:223], a[240:243], v[88:91]\n"
      "v_cvt_pk_bf16_f32 v232, v128, v129\n"
      "v_cvt_pk_bf16_f32 v233, v130, v131\n"
      "v_mfma_f32_16x16x32_bf16 v[84:87], v[216:219], a[244:247], v[84:87]\n"
      "ds_read_b64_tr_b16 a[232:233], v9 offset:61440\n"
      "ds_read_b64_tr_b16 a[234:235], v9 offset:61952\n"
      "v_mfma_f32_16x16x32_bf16 v[92:95], v[220:223], a[244:247], v[92:95]\n"
      "s_add_u32 m0, s95, 49152\n"
      "v_add_u32 v4, s18, v4\n"
      "buffer_load_dwordx4 v4, s[28:31], 0 offen offset:0 lds\n"
      "v_cvt_pk_bf16_f32 v234, v132, v133\n"
      "v_cvt_pk_bf16_f32 v235, v134, v135\n"
      "v_mfma_f32_16x16x32_bf16 v[96:99], v[216:219], a[248:251], v[96:99]\n"
      "ds_read_b64_tr_b16 a[236:237], v10 offset:61440\n"
      "ds_read_b64_tr_b16 a[238:239], v10 offset:61952\n"
      "v_mfma_f32_16x16x32_bf16 v[104:107], v[220:223], a[248:251], v[104:107]\n"
      "v_cvt_pk_bf16_f32 v236, v136, v137\n"
      "v_cvt_pk_bf16_f32 v237, v138, v139\n"
      "v_mfma_f32_16x16x32_bf16 v[100:103], v[216:219], a[252:255], v[100:103]\n"
      "ds_read_b64_tr_b16 a[240:241], v11 offset:61440\n"
      "ds_read_b64_tr_b16 a[242:243], v11 offset:61952\n"
      "v_mfma_f32_16x16x32_bf16 v[108:111], v[220:223], a[252:255], v[108:111]\n"
      "buffer_load_dwordx4 v4, s[28:31], s65 offen offset:1024 lds\n"
      "v_cvt_pk_bf16_f32 v238, v140, v141\n"
      "v_cvt_pk_bf16_f32 v239, v142, v143\n"
      "v_mfma_f32_4x4x4_16b_bf16 v[112:115], v[28:29], a[96:97], v[24:27]\n"
      "v_mfma_f32_4x4x4_16b_bf16 v[116:119], v[28:29], a[98:99], v[24:27]\n"
      "v_mfma_f32_4x4x4_16b_bf16 v[120:123], v[28:29], a[100:101], v[40:43]\n"
      "v_mfma_f32_4x4x4_16b_bf16 v[124:127], v[28:29], a[102:103], v[40:43]\n"
      "s_waitcnt lgkmcnt(6)\n"
      "v_mfma_f32_16x16x32_bf16 v[80:83], v[224:227], a[216:219], v[80:83]\n"
      "ds_read_b64_tr_b16 a[244:245], v12 offset:61440\n"
      "ds_read_b64_tr_b16 a[246:247], v12 offset:61952\n"
      "v_mfma_f32_16x16x32_bf16 v[88:91], v[228:231], a[216:219], v[88:91]\n"
      "v_cvt_pk_bf16_f32 v240, v112, v113\n"
      "v_cvt_pk_bf16_f32 v241, v114, v115\n"
      "v_mfma_f32_16x16x32_bf16 v[84:87], v[224:227], a[220:223], v[84:87]\n"
      "ds_read_b64_tr_b16 a[248:249], v9 offset:0\n"
      "ds_read_b64_tr_b16 a[250:251], v9 offset:512\n"
      "v_mfma_f32_16x16x32_bf16 v[92:95], v[228:231], a[220:223], v[92:95]\n"
      "buffer_load_dwordx4 v4, s[28:31], s66 offen offset:2048 lds\n"
      "v_cvt_pk_bf16_f32 v242, v116, v117\n"
      "v_cvt_pk_bf16_f32 v243, v118, v119\n"
      "v_mfma_f32_16x16x32_bf16 v[96:99], v[224:227], a[224:227], v[96:99]\n"
      "ds_read_b64_tr_b16 a[252:253], v10 offset:0\n"
      "ds_read_b64_tr_b16 a[254:255], v10 offset:512\n"
      "v_mfma_f32_16x16x32_bf16 v[104:107], v[228:231], a[224:227], v[104:107]\n"
      "v_cvt_pk_bf16_f32 v244, v120, v121\n"
      "v_cvt_pk_bf16_f32 v245, v122, v123\n"
      "v_mfma_f32_16x16x32_bf16 v[100:103], v[224:227], a[228:231], v[100:103]\n"
      "ds_read_b64_tr_b16 a[216:217], v11 offset:0\n"
      "ds_read_b64_tr_b16 a[218:219], v11 offset:512\n"
      "v_mfma_f32_16x16x32_bf16 v[108:111], v[228:231], a[228:231], v[108:111]\n"
      "buffer_load_dwordx4 v4, s[28:31], s67 offen offset:3072 lds\n"
      "v_cvt_pk_bf16_f32 v246, v124, v125\n"
      "v_cvt_pk_bf16_f32 v247, v126, v127\n"
      "v_mfma_f32_4x4x4_16b_bf16 v[128:131], v[28:29], a[104:105], v[24:27]\n"
      "v_mfma_f32_4x4x4_16b_bf16 v[132:135], v[28:29], a[106:107], v[24:27]\n"
      "v_mfma_f32_4x4x4_16b_bf16 v[136:139], v[28:29], a[108:109], v[40:43]\n"
      "v_mfma_f32_4x4x4_16b_bf16 v[140:143], v[28:29], a[110:111], v[40:43]\n"
      "s_waitcnt lgkmcnt(6)\n"
      "v_mfma_f32_16x16x32_bf16 v[80:83], v[232:235], a[232:235], v[80:83]\n"
      "ds_read_b64_tr_b16 a[220:221], v12 offset:0\n"
      "ds_read_b64_tr_b16 a[222:223], v12 offset:512\n"
      "v_mfma_f32_16x16x32_bf16 v[88:91], v[236:239], a[232:235], v[88:91]\n"
      "v_cvt_pk_bf16_f32 v248, v128, v129\n"
      "v_cvt_pk_bf16_f32 v249, v130, v131\n"
      "v_mfma_f32_16x16x32_bf16 v[84:87], v[232:235], a[236:239], v[84:87]\n"
      "ds_read_b64_tr_b16 a[224:225], v9 offset:4096\n"
      "ds_read_b64_tr_b16 a[226:227], v9 offset:4608\n"
      "v_mfma_f32_16x16x32_bf16 v[92:95], v[236:239], a[236:239], v[92:95]\n"
      "s_add_u32 m0, s95, 65536\n"
      "v_add_u32 v4, s19, v4\n"
      "buffer_load_dwordx4 v4, s[28:31], 0 offen offset:0 lds\n"
      "v_cvt_pk_bf16_f32 v250, v132, v133\n"
      "v_cvt_pk_bf16_f32 v251, v134, v135\n"
      "v_mfma_f32_16x16x32_bf16 v[96:99], v[232:235], a[240:243], v[96:99]\n"
      "ds_read_b64_tr_b16 a[228:229], v10 offset:4096\n"
      "ds_read_b64_tr_b16 a[230:231], v10 offset:4608\n"
      "v_mfma_f32_16x16x32_bf16 v[104:107], v[236:239], a[240:243], v[104:107]\n"
      "v_cvt_pk_bf16_f32 v252, v136, v137\n"
      "v_cvt_pk_bf16_f32 v253, v138, v139\n"
      "s_sleep 2\n"
      "v_mfma_f32_16x16x32_bf16 v[100:103], v[232:235], a[244:247], v[100:103]\n"
      "ds_read_b64_tr_b16 a[232:233], v11 offset:4096\n"
      "ds_read_b64_tr_b16 a[234:235], v11 offset:4608\n"
      "v_mfma_f32_16x16x32_bf16 v[108:111], v[236:239], a[244:247], v[108:111]\n"
      "buffer_load_dwordx4 v4, s[28:31], s65 offen offset:1024 lds\n"
      "v_cvt_pk_bf16_f32 v254, v140, v141\n"
      "v_cvt_pk_bf16_f32 v255, v142, v143\n"
      "s_waitcnt vmcnt(10) lgkmcnt(6)\n"
      "s_barrier\n"
      "v_mfma_f32_16x16x32_bf16 v[80:83], v[240:243], a[248:251], v[80:83]\n"
      "ds_read_b64_tr_b16 a[236:237], v12 offset:4096\n"
      "ds_read_b64_tr_b16 a[238:239], v12 offset:4608\n"
      "v_mfma_f32_16x16x32_bf16 v[88:91], v[244:247], a[248:251], v[88:91]\n"
      "v_xor_b32 v9, 0x0, v7\n"
      "v_mfma_f32_16x16x32_bf16 v[84:87], v[240:243], a[252:255], v[84:87]\n"
      "ds_read_b64_tr_b16 a[240:241], v9 offset:0\n"
      "ds_read_b64_tr_b16 a[242:243], v9 offset:512\n"
      "v_mfma_f32_16x16x32_bf16 v[92:95], v[244:247], a[252:255], v[92:95]\n"
      "buffer_load_dwordx4 v4, s[28:31], s66 offen offset:2048 lds\n"
      "v_mfma_f32_16x16x32_bf16 v[96:99], v[240:243], a[216:219], v[96:99]\n"
      "ds_read_b64_tr_b16 a[244:245], v9 offset:8192\n"
      "ds_read_b64_tr_b16 a[246:247], v9 offset:8704\n"
      "v_mfma_f32_16x16x32_bf16 v[104:107], v[244:247], a[216:219], v[104:107]\n"
      "v_xor_b32 v10, 0x20, v7\n"
      "v_mfma_f32_16x16x32_bf16 v[100:103], v[240:243], a[220:223], v[100:103]\n"
      "ds_read_b64_tr_b16 a[248:249], v10 offset:0\n"
      "ds_read_b64_tr_b16 a[250:251], v10 offset:512\n"
      "v_mfma_f32_16x16x32_bf16 v[108:111], v[244:247], a[220:223], v[108:111]\n"
      "buffer_load_dwordx4 v4, s[28:31], s67 offen offset:3072 lds\n"
      "s_waitcnt lgkmcnt(6)\n"
      "v_mfma_f32_16x16x32_bf16 v[80:83], v[248:251], a[224:227], v[80:83]\n"
      "ds_read_b64_tr_b16 a[252:253], v10 offset:8192\n"
      "ds_read_b64_tr_b16 a[254:255], v10 offset:8704\n"
      "v_mfma_f32_16x16x32_bf16 v[88:91], v[252:255], a[224:227], v[88:91]\n"
      "v_mfma_f32_16x16x32_bf16 v[84:87], v[248:251], a[228:231], v[84:87]\n"
      "ds_read_b64_tr_b16 a[216:217], v9 offset:2048\n"
      "ds_read_b64_tr_b16 a[218:219], v9 offset:2560\n"
      "v_mfma_f32_16x16x32_bf16 v[92:95], v[252:255], a[228:231], v[92:95]\n"
      "s_add_u32 m0, s95, 81920\n"
      "v_add_u32 v4, s8, v4\n"
      "buffer_load_dwordx4 v4, s[28:31], 0 offen offset:0 lds\n"
      "v_mfma_f32_16x16x32_bf16 v[96:99], v[248:251], a[232:235], v[96:99]\n"
      "ds_read_b64_tr_b16 a[220:221], v9 offset:10240\n"
      "ds_read_b64_tr_b16 a[222:223], v9 offset:10752\n"
      "v_mfma_f32_16x16x32_bf16 v[104:107], v[252:255], a[232:235], v[104:107]\n"
      "v_mfma_f32_16x16x32_bf16 v[100:103], v[248:251], a[236:239], v[100:103]\n"
      "ds_read_b64_tr_b16 a[224:225], v10 offset:2048\n"
      "ds_read_b64_tr_b16 a[226:227], v10 offset:2560\n"
      "v_mfma_f32_16x16x32_bf16 v[108:111], v[252:255], a[236:239], v[108:111]\n"
      "buffer_load_dwordx4 v4, s[28:31], s65 offen offset:1024 lds\n"
      "s_waitcnt lgkmcnt(6)\n"
      "v_mfma_f32_16x16x32_bf16 v[112:115], a[240:243], v[144:147], 0\n"
      "ds_read_b64_tr_b16 a[228:229], v10 offset:10240\n"
      "ds_read_b64_tr_b16 a[230:231], v10 offset:10752\n"
      "v_mfma_f32_16x16x32_bf16 v[128:131], a[240:243], v[148:151], 0\n"
      "v_mfma_f32_16x16x32_bf16 v[120:123], a[244:247], v[144:147], 0\n"
      "ds_read_b64_tr_b16 a[232:233], v9 offset:16384\n"
      "ds_read_b64_tr_b16 a[234:235], v9 offset:16896\n"
      "v_mfma_f32_16x16x32_bf16 v[136:139], a[244:247], v[148:151], 0\n"
      "s_mov_b64 exec, 1\n"
      "buffer_load_dwordx2 v[16:17], v0, s[48:51], 0 offen sc1\n"
      "s_mov_b64 exec, -1\n"
      "buffer_load_dwordx4 v4, s[28:31], s66 offen offset:2048 lds\n"
      "v_mfma_f32_16x16x32_bf16 v[116:119], a[248:251], v[144:147], 0\n"
      "ds_read_b64_tr_b16 a[236:237], v9 offset:24576\n"
      "ds_read_b64_tr_b16 a[238:239], v9 offset:25088\n"
      "v_mfma_f32_16x16x32_bf16 v[132:135], a[248:251], v[148:151], 0\n"
      "v_mfma_f32_16x16x32_bf16 v[124:127], a[252:255], v[144:147], 0\n"
      "ds_read_b64_tr_b16 a[240:241], v10 offset:16384\n"
      "ds_read_b64_tr_b16 a[242:243], v10 offset:16896\n"
      "v_mfma_f32_16x16x32_bf16 v[140:143], a[252:255], v[148:151], 0\n"
      "buffer_load_dwordx4 v4, s[28:31], s67 offen offset:3072 lds\n"
      "v_mul_f32 v80, v80, v64\n"
      "s_waitcnt lgkmcnt(6)\n"
      "v_mfma_f32_16x16x32_bf16 v[112:115], a[216:219], v[152:155], v[112:115]\n"
      "ds_read_b64_tr_b16 a[244:245], v10 offset:24576\n"
      "ds_read_b64_tr_b16 a[246:247], v10 offset:25088\n"
      "v_mfma_f32_16x16x32_bf16 v[128:131], a[216:219], v[156:159], v[128:131]\n"
      "v_mul_f32 v81, v81, v65\n"
      "v_mul_f32 v82, v82, v66\n"
      "v_mul_f32 v83, v83, v67\n"
      "v_mfma_f32_16x16x32_bf16 v[120:123], a[220:223], v[152:155], v[120:123]\n"
      "ds_read_b64_tr_b16 a[248:249], v9 offset:18432\n"
      "ds_read_b64_tr_b16 a[250:251], v9 offset:18944\n"
      "v_mfma_f32_16x16x32_bf16 v[136:139], a[220:223], v[156:159], v[136:139]\n"
      "v_mul_f32 v84, v84, v64\n"
      "v_mul_f32 v85, v85, v65\n"
      "v_mul_f32 v86, v86, v66\n"
      "v_mfma_f32_16x16x32_bf16 v[116:119], a[224:227], v[152:155], v[116:119]\n"
      "ds_read_b64_tr_b16 a[252:253], v9 offset:26624\n"
      "ds_read_b64_tr_b16 a[254:255], v9 offset:27136\n"
      "v_mfma_f32_16x16x32_bf16 v[132:135], a[224:227], v[156:159], v[132:135]\n"
      "v_mul_f32 v87, v87, v67\n"
      "v_cvt_pk_bf16_f32 v48, v80, v81\n"
      "v_cvt_pk_bf16_f32 v49, v82, v83\n"
      "v_cvt_pk_bf16_f32 v50, v84, v85\n"
      ".LPAD8:\n"
      "s_cbranch_execz .LPAD4\n"
      "v_mfma_f32_16x16x32_bf16 v[124:127], a[228:231], v[152:155], v[124:127]\n"
      "ds_read_b64_tr_b16 a[216:217], v10 offset:18432\n"
      "ds_read_b64_tr_b16 a[218:219], v10 offset:18944\n"
      "v_mfma_f32_16x16x32_bf16 v[140:143], a[228:231], v[156:159], v[140:143]\n"
      "v_cvt_pk_bf16_f32 v51, v86, v87\n"
      "v_permlane32_swap_b32 v48, v50\n"
      "s_nop 0\n"
      "v_permlane32_swap_b32 v49, v51\n"
      "s_waitcnt vmcnt(9) lgkmcnt(6)\n"
      "s_barrier\n"
      "v_mfma_f32_16x16x32_bf16 v[112:115], a[232:235], v[160:163], v[112:115]\n"
      "ds_read_b64_tr_b16 a[220:221], v10 offset:26624\n"
      "ds_read_b64_tr_b16 a[222:223], v10 offset:27136\n"
      "v_mfma_f32_16x16x32_bf16 v[128:131], a[232:235], v[164:167], v[128:131]\n"
      "s_add_u32 m0, s95, 98304\n"
      "v_add_u32 v4, s9, v4\n"
      "buffer_load_dwordx4 v4, s[28:31], 0 offen offset:0 lds\n"
      "buffer_store_dwordx4 v[48:51], v15, s[48:51], s84 offen offset:0 sc1\n"
      "v_mfma_f32_16x16x32_bf16 v[120:123], a[236:239], v[160:163], v[120:123]\n"
      "ds_read_b64_tr_b16 a[224:225], v9 offset:32768\n"
      "ds_read_b64_tr_b16 a[226:227], v9 offset:33280\n"
      "v_mfma_f32_16x16x32_bf16 v[136:139], a[236:239], v[164:167], v[136:139]\n"
      "v_mul_f32 v88, v88, v68\n"
      "v_mul_f32 v89, v89, v69\n"
      "v_mul_f32 v90, v90, v70\n"
      "v_mul_f32 v91, v91, v71\n"
      "v_mul_f32 v92, v92, v68\n"
      "v_mfma_f32_16x16x32_bf16 v[116:119], a[240:243], v[160:163], v[116:119]\n"
      "ds_read_b64_tr_b16 a[228:229], v9 offset:40960\n"
      "ds_read_b64_tr_b16 a[230:231], v9 offset:41472\n"
      "v_mfma_f32_16x16x32_bf16 v[132:135], a[240:243], v[164:167], v[132:135]\n"
      "buffer_load_dwordx4 v4, s[28:31], s65 offen offset:1024 lds\n"
      "v_mul_f32 v93, v93, v69\n"
      "v_mul_f32 v94, v94, v70\n"
      "v_mul_f32 v95, v95, v71\n"
      "v_cvt_pk_bf16_f32 v52, v88, v89\n"
      "v_mfma_f32_16x16x32_bf16 v[124:127], a[244:247], v[160:163], v[124:127]\n"
      "ds_read_b64_tr_b16 a[232:233], v10 offset:32768\n"
      "ds_read_b64_tr_b16 a[234:235], v10 offset:33280\n"
      "v_mfma_f32_16x16x32_bf16 v[140:143], a[244:247], v[164:167], v[140:143]\n"
      "v_cvt_pk_bf16_f32 v53, v90, v91\n"
      "v_cvt_pk_bf16_f32 v54, v92, v93\n"
      "v_cvt_pk_bf16_f32 v55, v94, v95\n"
      "s_nop 0\n"
      "v_permlane32_swap_b32 v52, v54\n"
      "v_permlane32_swap_b32 v53, v55\n"
      "s_waitcnt lgkmcnt(6)\n"
      "v_mfma_f32_16x16x32_bf16 v[112:115], a[248:251], v[168:171], v[112:115]\n"
      "ds_read_b64_tr_b16 a[236:237], v10 offset:40960\n"
      "ds_read_b64_tr_b16 a[238:239], v10 offset:41472\n"
      "v_mfma_f32_16x16x32_bf16 v[128:131], a[248:251], v[172:175], v[128:131]\n"
      "buffer_load_dwordx4 v4, s[28:31], s66 offen offset:2048 lds\n"
      "buffer_store_dwordx4 v[52:55], v15, s[48:51], s85 offen offset:0 sc1\n"
      "v_mfma_f32_16x16x32_bf16 v[120:123], a[252:255], v[168:171], v[120:123]\n"
      "ds_read_b64_tr_b16 a[240:241], v9 offset:34816\n"
      "ds_read_b64_tr_b16 a[242:243], v9 offset:35328\n"
      "v_mfma_f32_16x16x32_bf16 v[136:139], a[252:255], v[172:175], v[136:139]\n"
      "v_mul_f32 v96, v96, v64\n"
      "v_mul_f32 v97, v97, v65\n"
      "v_mul_f32 v98, v98, v66\n"
      "v_mul_f32 v99, v99, v67\n"
      "v_mul_f32 v100, v100, v64\n"
      "v_mul_f32 v101, v101, v65\n"
      "v_mfma_f32_16x16x32_bf16 v[116:119], a[216:219], v[168:171], v[116:119]\n"
      "ds_read_b64_tr_b16 a[244:245], v9 offset:43008\n"
      "ds_read_b64_tr_b16 a[246:247], v9 offset:43520\n"
      "v_mfma_f32_16x16x32_bf16 v[132:135], a[216:219], v[172:175], v[132:135]\n"
      "buffer_load_dwordx4 v4, s[28:31], s67 offen offset:3072 lds\n"
      "v_mul_f32 v102, v102, v66\n"
      "v_mul_f32 v103, v103, v67\n"
      "v_cvt_pk_bf16_f32 v56, v96, v97\n"
      "v_mfma_f32_16x16x32_bf16 v[124:127], a[220:223], v[168:171], v[124:127]\n"
      "ds_read_b64_tr_b16 a[248:249], v10 offset:34816\n"
      "ds_read_b64_tr_b16 a[250:251], v10 offset:35328\n"
      "v_mfma_f32_16x16x32_bf16 v[140:143], a[220:223], v[172:175], v[140:143]\n"
      "v_cvt_pk_bf16_f32 v57, v98, v99\n"
      "v_cvt_pk_bf16_f32 v58, v100, v101\n"
      "v_cvt_pk_bf16_f32 v59, v102, v103\n"
      "s_nop 0\n"
      "v_permlane32_swap_b32 v56, v58\n"
      "v_permlane32_swap_b32 v57, v59\n"
      "s_waitcnt lgkmcnt(6)\n"
      "v_mfma_f32_16x16x32_bf16 v[112:115], a[224:227], v[176:179], v[112:115]\n"
      "ds_read_b64_tr_b16 a[252:253], v10 offset:43008\n"
      "ds_read_b64_tr_b16 a[254:255], v10 offset:43520\n"
      "v_mfma_f32_16x16x32_bf16 v[128:131], a[224:227], v[180:183], v[128:131]\n"
      "buffer_store_dwordx4 v[56:59], v15, s[48:51], s84 offen offset:512 sc1\n"
      "v_mul_f32 v104, v104, v68\n"
      "v_mfma_f32_16x16x32_bf16 v[120:123], a[228:231], v[176:179], v[120:123]\n"
      "ds_read_b64_tr_b16 a[216:217], v9 offset:49152\n"
      "ds_read_b64_tr_b16 a[218:219], v9 offset:49664\n"
      "v_mfma_f32_16x16x32_bf16 v[136:139], a[228:231], v[180:183], v[136:139]\n"
      "v_mul_f32 v105, v105, v69\n"
      "v_mul_f32 v106, v106, v70\n"
      "v_mul_f32 v107, v107, v71\n"
      "v_mul_f32 v108, v108, v68\n"
      "v_mul_f32 v109, v109, v69\n"
      "v_mfma_f32_16x16x32_bf16 v[116:119], a[232:235], v[176:179], v[116:119]\n"
      "ds_read_b64_tr_b16 a[220:221], v9 offset:57344\n"
      "ds_read_b64_tr_b16 a[222:223], v9 offset:57856\n"
      "v_mfma_f32_16x16x32_bf16 v[132:135], a[232:235], v[180:183], v[132:135]\n"
      "v_mul_f32 v110, v110, v70\n"
      "v_mul_f32 v111, v111, v71\n"
      "v_cvt_pk_bf16_f32 v60, v104, v105\n"
      "v_cvt_pk_bf16_f32 v61, v106, v107\n"
      "v_mfma_f32_16x16x32_bf16 v[124:127], a[236:239], v[176:179], v[124:127]\n"
      "ds_read_b64_tr_b16 a[224:225], v10 offset:49152\n"
      "ds_read_b64_tr_b16 a[226:227], v10 offset:49664\n"
      "v_mfma_f32_16x16x32_bf16 v[140:143], a[236:239], v[180:183], v[140:143]\n"
      "v_cvt_pk_bf16_f32 v62, v108, v109\n"
      "v_cvt_pk_bf16_f32 v63, v110, v111\n"
      "s_nop 0\n"
      "v_permlane32_swap_b32 v60, v62\n"
      "v_permlane32_swap_b32 v61, v63\n"
      "s_waitcnt lgkmcnt(6)\n"
      "v_mfma_f32_16x16x32_bf16 v[112:115], a[240:243], v[184:187], v[112:115]\n"
      "ds_read_b64_tr_b16 a[228:229], v10 offset:57344\n"
      "ds_read_b64_tr_b16 a[230:231], v10 offset:57856\n"
      "v_mfma_f32_16x16x32_bf16 v[128:131], a[240:243], v[188:191], v[128:131]\n"
      "buffer_store_dwordx4 v[60:63], v15, s[48:51], s85 offen offset:512 sc1\n"
      "s_lshl_b32 s13, s55, 7\n"
      "v_mfma_f32_16x16x32_bf16 v[120:123], a[244:247], v[184:187], v[120:123]\n"
      "ds_read_b64_tr_b16 a[232:233], v9 offset:51200\n"
      "ds_read_b64_tr_b16 a[234:235], v9 offset:51712\n"
      "v_mfma_f32_16x16x32_bf16 v[136:139], a[244:247], v[188:191], v[136:139]\n"
      "s_lshl_b32 s88, s12, 11\n"
      "s_lshl3_add_u32 s88, s55, s88\n"
      "s_add_u32 s88, s88, 131128\n"
      "v_mfma_f32_16x16x32_bf16 v[116:119], a[248:251], v[184:187], v[116:119]\n"
      "ds_read_b64_tr_b16 a[236:237], v9 offset:59392\n"
      "ds_read_b64_tr_b16 a[238:239], v9 offset:59904\n"
      "v_mfma_f32_16x16x32_bf16 v[132:135], a[248:251], v[188:191], v[132:135]\n"
      "s_lshl3_add_u32 s60, s53, s52\n"
      "s_lshl_b32 s89, s60, 12\n"
      "s_add_u32 s89, s89, 278528\n"
      "s_mul_i32 s60, s60, 71680\n"
      "v_and_b32 v35, 15, v0\n"
      "v_mfma_f32_16x16x32_bf16 v[124:127], a[252:255], v[184:187], v[124:127]\n"
      "ds_read_b64_tr_b16 a[240:241], v10 offset:51200\n"
      "ds_read_b64_tr_b16 a[242:243], v10 offset:51712\n"
      "v_mfma_f32_16x16x32_bf16 v[140:143], a[252:255], v[188:191], v[140:143]\n"
      "s_add_u32 s82, s60, 5586944\n"
      "s_lshl3_add_u32 s60, s53, s55\n"
      "s_lshl3_add_u32 s60, s60, s52\n"
      "v_mul_u32_u24 v35, 1792, v35\n"
      "v_lshrrev_b32 v36, 5, v0\n"
      "s_waitcnt vmcnt(8) lgkmcnt(6)\n"
      "s_barrier\n"
      "v_mfma_f32_16x16x32_bf16 v[112:115], a[216:219], v[192:195], v[112:115]\n"
      "ds_read_b64_tr_b16 a[244:245], v10 offset:59392\n"
      "ds_read_b64_tr_b16 a[246:247], v10 offset:59904\n"
      "v_mfma_f32_16x16x32_bf16 v[128:131], a[216:219], v[196:199], v[128:131]\n"
      "s_add_u32 m0, s95, 114688\n"
      "v_add_u32 v4, s10, v4\n"
      "buffer_load_dwordx4 v4, s[28:31], 0 offen offset:0 lds\n"
      "v_xor_b32 v11, 0xc000, v7\n"
      "v_mfma_f32_16x16x32_bf16 v[120:123], a[220:223], v[192:195], v[120:123]\n"
      "ds_read_b64_tr_b16 a[248:249], v11 offset:16384\n"
      "ds_read_b64_tr_b16 a[250:251], v11 offset:16896\n"
      "v_mfma_f32_16x16x32_bf16 v[136:139], a[220:223], v[196:199], v[136:139]\n"
      "v_mul_u32_u24 v39, 896, v0\n"
      "s_lshl_b32 s60, s60, 11\n"
      "s_add_u32 s92, s60, 14688256\n"
      "s_xor_b32 s91, s92, 8192\n"
      "v_mfma_f32_16x16x32_bf16 v[116:119], a[224:227], v[192:195], v[116:119]\n"
      "ds_read_b64_tr_b16 a[252:253], v11 offset:24576\n"
      "ds_read_b64_tr_b16 a[254:255], v11 offset:25088\n"
      "v_mfma_f32_16x16x32_bf16 v[132:135], a[224:227], v[196:199], v[132:135]\n"
      "v_xor_b32 v12, 0xc020, v7\n"
      "v_readfirstlane_b32 s96, v16\n"
      "v_readfirstlane_b32 s97, v17\n"
      "v_lshl_add_u32 v15, v36, 4, v35\n"
      "v_and_b32 v36, 16, v0\n"
      "v_mfma_f32_16x16x32_bf16 v[124:127], a[228:231], v[192:195], v[124:127]\n"
      "ds_read_b64_tr_b16 a[216:217], v12 offset:16384\n"
      "ds_read_b64_tr_b16 a[218:219], v12 offset:16896\n"
      "v_mfma_f32_16x16x32_bf16 v[140:143], a[228:231], v[196:199], v[140:143]\n"
      "buffer_load_dwordx4 v4, s[28:31], s65 offen offset:1024 lds\n"
      "s_add_u32 s96, s96, 0x7f4a7c15\n"
      "s_addc_u32 s97, s97, 0x9e3779b9\n"
      "v_lshl_add_u32 v15, v36, 1, v15\n"
      "s_waitcnt lgkmcnt(6)\n"
      "v_mfma_f32_16x16x32_bf16 v[112:115], a[232:235], v[200:203], v[112:115]\n"
      "ds_read_b64_tr_b16 a[220:221], v12 offset:24576\n"
      "ds_read_b64_tr_b16 a[222:223], v12 offset:25088\n"
      "v_mfma_f32_16x16x32_bf16 v[128:131], a[232:235], v[204:207], v[128:131]\n"
      "v_mov_b32 v16, s96\n"
      "v_mov_b32 v17, s97\n"
      "s_lshl3_add_u32 s90, s55, s89\n"
      "v_mfma_f32_16x16x32_bf16 v[120:123], a[236:239], v[200:203], v[120:123]\n"
      "ds_read_b64_tr_b16 a[224:225], v11 offset:18432\n"
      "ds_read_b64_tr_b16 a[226:227], v11 offset:18944\n"
      "v_mfma_f32_16x16x32_bf16 v[136:139], a[236:239], v[204:207], v[136:139]\n"
      "s_lshl_b32 s61, s55, 7\n"
      "s_mul_i32 s57, s12, 12544\n"
      "s_sub_u32 s93, s89, 16384\n"
      "v_mfma_f32_16x16x32_bf16 v[116:119], a[240:243], v[200:203], v[116:119]\n"
      "ds_read_b64_tr_b16 a[228:229], v11 offset:26624\n"
      "ds_read_b64_tr_b16 a[230:231], v11 offset:27136\n"
      "v_mfma_f32_16x16x32_bf16 v[132:135], a[240:243], v[204:207], v[132:135]\n"
      "v_mov_b32 v72, -1\n"
      "s_lshl_b32 s60, s55, 13\n"
      "s_sub_u32 s59, s82, 57344\n"
      "s_add_u32 s75, s59, s60\n"
      "v_mfma_f32_16x16x32_bf16 v[124:127], a[244:247], v[200:203], v[124:127]\n"
      "ds_read_b64_tr_b16 a[232:233], v12 offset:18432\n"
      "ds_read_b64_tr_b16 a[234:235], v12 offset:18944\n"
      "v_mfma_f32_16x16x32_bf16 v[140:143], a[244:247], v[204:207], v[140:143]\n"
      "buffer_load_dwordx4 v4, s[28:31], s66 offen offset:2048 lds\n"
      "v_mov_b32 v73, -1\n"
      "v_mov_b32 v74, -1\n"
      "s_waitcnt lgkmcnt(6)\n"
      "v_mfma_f32_16x16x32_bf16 v[112:115], a[248:251], v[208:211], v[112:115]\n"
      "ds_read_b64_tr_b16 a[236:237], v12 offset:26624\n"
      "ds_read_b64_tr_b16 a[238:239], v12 offset:27136\n"
      "v_mfma_f32_16x16x32_bf16 v[128:131], a[248:251], v[212:215], v[128:131]\n"
      "s_mul_i32 s68, s60, 14\n"
      "s_add_u32 s83, s82, 8192\n"
      "v_mfma_f32_16x16x32_bf16 v[120:123], a[252:255], v[208:211], v[120:123]\n"
      "ds_read_b64_tr_b16 a[240:241], v11 offset:32768\n"
      "ds_read_b64_tr_b16 a[242:243], v11 offset:33280\n"
      "v_mfma_f32_16x16x32_bf16 v[136:139], a[252:255], v[212:215], v[136:139]\n"
      "v_mov_b32 v75, -1\n"
      "s_addk_i32 s60, 8192\n"
      "s_cmp_eq_u32 s60, 57344\n"
      "s_cselect_b32 s60, 0, s60\n"
      "v_mfma_f32_16x16x32_bf16 v[116:119], a[216:219], v[208:211], v[116:119]\n"
      "ds_read_b64_tr_b16 a[244:245], v11 offset:40960\n"
      "ds_read_b64_tr_b16 a[246:247], v11 offset:41472\n"
      "v_mfma_f32_16x16x32_bf16 v[132:135], a[216:219], v[212:215], v[132:135]\n"
      "s_add_u32 s57, s57, s61\n"
      "s_mul_i32 s58, s12, 3584\n"
      "s_mul_i32 s69, s60, 14\n"
      "v_mfma_f32_16x16x32_bf16 v[124:127], a[220:223], v[208:211], v[124:127]\n"
      "ds_read_b64_tr_b16 a[248:249], v12 offset:32768\n"
      "ds_read_b64_tr_b16 a[250:251], v12 offset:33280\n"
      "v_mfma_f32_16x16x32_bf16 v[140:143], a[220:223], v[212:215], v[140:143]\n"
      "buffer_load_dwordx4 v4, s[28:31], s67 offen offset:3072 lds\n"
      "s_add_u32 s58, s58, s61\n"
      "s_add_u32 s76, s59, s60\n"
      "s_waitcnt lgkmcnt(6)\n"
      "v_mfma_f32_16x16x32_bf16 v[112:115], a[224:227], v[216:219], v[112:115]\n"
      "ds_read_b64_tr_b16 a[252:253], v12 offset:40960\n"
      "ds_read_b64_tr_b16 a[254:255], v12 offset:41472\n"
      "v_mfma_f32_16x16x32_bf16 v[128:131], a[224:227], v[220:223], v[128:131]\n"
      "s_mov_b64 exec, 0x3fff\n"
      "buffer_load_dword v45, v39, s[36:39], s57 offen\n"
      "s_mov_b64 exec, -1\n"
      "v_mfma_f32_16x16x32_bf16 v[120:123], a[228:231], v[216:219], v[120:123]\n"
      "ds_read_b64_tr_b16 a[216:217], v11 offset:34816\n"
      "ds_read_b64_tr_b16 a[218:219], v11 offset:35328\n"
      "v_mfma_f32_16x16x32_bf16 v[136:139], a[228:231], v[220:223], v[136:139]\n"
      "v_mov_b32 v25, v20\n"
      "s_addk_i32 s60, 8192\n"
      "s_cmp_eq_u32 s60, 57344\n"
      "s_cselect_b32 s60, 0, s60\n"
      "v_mfma_f32_16x16x32_bf16 v[116:119], a[232:235], v[216:219], v[116:119]\n"
      "ds_read_b64_tr_b16 a[220:221], v11 offset:43008\n"
      "ds_read_b64_tr_b16 a[222:223], v11 offset:43520\n"
      "v_mfma_f32_16x16x32_bf16 v[132:135], a[232:235], v[220:223], v[132:135]\n"
      "s_mov_b64 exec, 15\n"
      "buffer_load_dword v45, v39, s[40:43], s58 offen\n"
      "s_mov_b64 exec, -1\n"
      "s_add_u32 m0, s95, 131072\n"
      "v_add_u32 v4, s16, v4\n"
      "buffer_load_dwordx4 v4, s[28:31], 0 offen offset:0 lds\n"
      "v_mov_b32 v26, v21\n"
      "v_mfma_f32_16x16x32_bf16 v[124:127], a[236:239], v[216:219], v[124:127]\n"
      "ds_read_b64_tr_b16 a[224:225], v12 offset:34816\n"
      "ds_read_b64_tr_b16 a[226:227], v12 offset:35328\n"
      "v_mfma_f32_16x16x32_bf16 v[140:143], a[236:239], v[220:223], v[140:143]\n"
      "v_mov_b32 v27, v22\n"
      "s_mul_i32 s70, s60, 14\n"
      "s_add_u32 s77, s59, s60\n"
      "s_waitcnt vmcnt(9) lgkmcnt(6)\n"
      "s_barrier\n"
      "v_mfma_f32_16x16x32_bf16 v[112:115], a[240:243], v[224:227], v[112:115]\n"
      "ds_read_b64_tr_b16 a[228:229], v12 offset:43008\n"
      "ds_read_b64_tr_b16 a[230:231], v12 offset:43520\n"
      "v_mfma_f32_16x16x32_bf16 v[128:131], a[240:243], v[228:231], v[128:131]\n"
      "s_addk_i32 s60, 8192\n"
      "v_mfma_f32_16x16x32_bf16 v[120:123], a[244:247], v[224:227], v[120:123]\n"
      "ds_read_b64_tr_b16 a[232:233], v11 offset:49152\n"
      "ds_read_b64_tr_b16 a[234:235], v11 offset:49664\n"
      "v_mfma_f32_16x16x32_bf16 v[136:139], a[244:247], v[228:231], v[136:139]\n"
      "v_permlane16_swap_b32 v20, v25\n"
      "s_cmp_eq_u32 s60, 57344\n"
      "s_cselect_b32 s60, 0, s60\n"
      "s_mul_i32 s71, s60, 14\n"
      "v_mfma_f32_16x16x32_bf16 v[116:119], a[248:251], v[224:227], v[116:119]\n"
      "ds_read_b64_tr_b16 a[236:237], v11 offset:57344\n"
      "ds_read_b64_tr_b16 a[238:239], v11 offset:57856\n"
      "v_mfma_f32_16x16x32_bf16 v[132:135], a[248:251], v[228:231], v[132:135]\n"
      "buffer_load_dwordx4 v4, s[28:31], s65 offen offset:1024 lds\n"
      "v_permlane16_swap_b32 v21, v26\n"
      "s_add_u32 s78, s59, s60\n"
      "v_mfma_f32_16x16x32_bf16 v[124:127], a[252:255], v[224:227], v[124:127]\n"
      "ds_read_b64_tr_b16 a[240:241], v12 offset:49152\n"
      "ds_read_b64_tr_b16 a[242:243], v12 offset:49664\n"
      "v_mfma_f32_16x16x32_bf16 v[140:143], a[252:255], v[228:231], v[140:143]\n"
      "s_waitcnt vmcnt(8)\n"
      "buffer_store_dwordx2 v[16:17], v13, s[48:51], s88 offen offset:0 sc1\n"
      "v_permlane16_swap_b32 v22, v27\n"
      "s_addk_i32 s60, 8192\n"
      "s_cmp_eq_u32 s60, 57344\n"
      "s_cselect_b32 s60, 0, s60\n"
      "s_waitcnt lgkmcnt(6)\n"
      "v_mfma_f32_16x16x32_bf16 v[112:115], a[216:219], v[232:235], v[112:115]\n"
      "ds_read_b64_tr_b16 a[244:245], v12 offset:57344\n"
      "ds_read_b64_tr_b16 a[246:247], v12 offset:57856\n"
      "v_mfma_f32_16x16x32_bf16 v[128:131], a[216:219], v[236:239], v[128:131]\n"
      "s_mul_i32 s72, s60, 14\n"
      "s_add_u32 s79, s59, s60\n"
      "v_mfma_f32_16x16x32_bf16 v[120:123], a[220:223], v[232:235], v[120:123]\n"
      "ds_read_b64_tr_b16 a[248:249], v11 offset:51200\n"
      "ds_read_b64_tr_b16 a[250:251], v11 offset:51712\n"
      "v_mfma_f32_16x16x32_bf16 v[136:139], a[220:223], v[236:239], v[136:139]\n"
      "s_addk_i32 s60, 8192\n"
      "s_cmp_eq_u32 s60, 57344\n"
      "s_cselect_b32 s60, 0, s60\n"
      "v_mfma_f32_16x16x32_bf16 v[116:119], a[224:227], v[232:235], v[116:119]\n"
      "ds_read_b64_tr_b16 a[252:253], v11 offset:59392\n"
      "ds_read_b64_tr_b16 a[254:255], v11 offset:59904\n"
      "v_mfma_f32_16x16x32_bf16 v[132:135], a[224:227], v[236:239], v[132:135]\n"
      "buffer_load_dwordx4 v4, s[28:31], s66 offen offset:2048 lds\n"
      "s_mul_i32 s73, s60, 14\n"
      "s_add_u32 s80, s59, s60\n"
      "v_mfma_f32_16x16x32_bf16 v[124:127], a[228:231], v[232:235], v[124:127]\n"
      "ds_read_b64_tr_b16 a[216:217], v12 offset:51200\n"
      "ds_read_b64_tr_b16 a[218:219], v12 offset:51712\n"
      "v_mfma_f32_16x16x32_bf16 v[140:143], a[228:231], v[236:239], v[140:143]\n"
      "s_addk_i32 s60, 8192\n"
      "s_cmp_eq_u32 s60, 57344\n"
      "s_cselect_b32 s60, 0, s60\n"
      "s_waitcnt lgkmcnt(6)\n"
      "v_mfma_f32_16x16x32_bf16 v[112:115], a[232:235], v[240:243], v[112:115]\n"
      "ds_read_b64_tr_b16 a[220:221], v12 offset:59392\n"
      "ds_read_b64_tr_b16 a[222:223], v12 offset:59904\n"
      "v_mfma_f32_16x16x32_bf16 v[128:131], a[232:235], v[244:247], v[128:131]\n"
      "v_xor_b32 v9, 0x40, v7\n"
      "s_mul_i32 s74, s60, 14\n"
      "s_add_u32 s81, s59, s60\n"
      "v_mfma_f32_16x16x32_bf16 v[120:123], a[236:239], v[240:243], v[120:123]\n"
      "ds_read_b64_tr_b16 a[224:225], v9 offset:0\n"
      "ds_read_b64_tr_b16 a[226:227], v9 offset:512\n"
      "v_mfma_f32_16x16x32_bf16 v[136:139], a[236:239], v[244:247], v[136:139]\n"
      "buffer_load_dwordx4 v4, s[28:31], s67 offen offset:3072 lds\n"
      "s_add_u32 s98, s98, s13\n"
      "v_mfma_f32_16x16x32_bf16 v[116:119], a[240:243], v[240:243], v[116:119]\n"
      "ds_read_b64_tr_b16 a[228:229], v9 offset:8192\n"
      "ds_read_b64_tr_b16 a[230:231], v9 offset:8704\n"
      "v_mfma_f32_16x16x32_bf16 v[132:135], a[240:243], v[244:247], v[132:135]\n"
      "v_xor_b32 v10, 0x60, v7\n"
      "s_lshl3_add_u32 s13, s55, 1024\n"
      "s_movk_i32 s60, 17280\n"
      "v_bfrev_b32 v24, 1\n"
      "v_mfma_f32_16x16x32_bf16 v[124:127], a[244:247], v[240:243], v[124:127]\n"
      "ds_read_b64_tr_b16 a[232:233], v10 offset:0\n"
      "ds_read_b64_tr_b16 a[234:235], v10 offset:512\n"
      "v_mfma_f32_16x16x32_bf16 v[140:143], a[244:247], v[244:247], v[140:143]\n"
      "v_writelane_b32 v24, s13, 1\n"
      "v_writelane_b32 v24, s60, 0\n"
      "s_addk_i32 s98, 64\n"
      "v_add_u32 v36, s95, v1\n"
      "s_waitcnt lgkmcnt(6)\n"
      "v_mfma_f32_16x16x32_bf16 v[112:115], a[248:251], v[248:251], v[112:115]\n"
      "ds_read_b64_tr_b16 a[236:237], v10 offset:8192\n"
      "ds_read_b64_tr_b16 a[238:239], v10 offset:8704\n"
      "v_mfma_f32_16x16x32_bf16 v[128:131], a[248:251], v[252:255], v[128:131]\n"
      "s_and_b32 s60, s52, 1\n"
      "s_mul_i32 s60, s60, 12544\n"
      "s_mul_i32 s59, s52, 5376\n"
      "v_mfma_f32_16x16x32_bf16 v[120:123], a[252:255], v[248:251], v[120:123]\n"
      "ds_read_b64_tr_b16 a[240:241], v9 offset:2048\n"
      "ds_read_b64_tr_b16 a[242:243], v9 offset:2560\n"
      "v_mfma_f32_16x16x32_bf16 v[136:139], a[252:255], v[252:255], v[136:139]\n"
      "s_add_u32 m0, s95, 147456\n"
      "v_add_u32 v4, s17, v4\n"
      "buffer_load_dwordx4 v4, s[28:31], 0 offen offset:0 lds\n"
      "v_mfma_f32_16x16x32_bf16 v[116:119], a[216:219], v[248:251], v[116:119]\n"
      "ds_read_b64_tr_b16 a[244:245], v9 offset:10240\n"
      "ds_read_b64_tr_b16 a[246:247], v9 offset:10752\n"
      "v_mfma_f32_16x16x32_bf16 v[132:135], a[216:219], v[252:255], v[132:135]\n"
      "s_sub_u32 s60, s60, s59\n"
      "v_lshrrev_b32 v40, 5, v0\n"
      "v_mul_u32_u24 v40, 3584, v40\n"
      "v_mfma_f32_16x16x32_bf16 v[124:127], a[220:223], v[248:251], v[124:127]\n"
      "ds_read_b64_tr_b16 a[248:249], v10 offset:2048\n"
      "ds_read_b64_tr_b16 a[250:251], v10 offset:2560\n"
      "v_mfma_f32_16x16x32_bf16 v[140:143], a[220:223], v[252:255], v[140:143]\n"
      "v_add3_u32 v40, v40, v5, s60\n"
      "v_lshrrev_b32 v41, 4, v0\n"
      "v_lshl_add_u32 v41, v41, 8, v1\n"
      "v_add_u32 v42, 4096, v41\n"
      "s_branch .LJOIN\n"
      ".LAUX:\n"
      "s_lshl1_add_u32 s93, s12, s54\n"
      "v_lshlrev_b32 v77, 7, v0\n"
      "v_lshlrev_b32 v78, 8, v0\n"
      "s_mul_i32 s35, s93, 8192\n"
      "buffer_load_dword a215, v77, s[24:27], s35 offen\n"
      "s_mul_i32 s35, s93, 1280\n"
      "s_add_u32 s35, s35, 1048576\n"
      "s_bfm_b64 exec, 10, 0\n"
      "buffer_load_dword a215, v77, s[24:27], s35 offen\n"
      "s_mov_b64 exec, -1\n"
      "buffer_load_dwordx4 v40, s[20:23], s61 offen offset:0 lds\n"
      "buffer_load_dwordx4 v41, s[20:23], s61 offen offset:1024 lds\n"
      "buffer_load_dwordx4 v42, s[20:23], s61 offen offset:2048 lds\n"
      "buffer_load_dwordx4 v43, s[20:23], s61 offen offset:3072 lds\n"
      "s_addk_i32 s61, 128\n"
      "s_addk_i32 m0, 0x4000\n"
      "s_cmp_eq_u32 s61, s11\n"
      "s_cselect_b32 s61, s98, s61\n"
      "buffer_load_dwordx4 v40, s[20:23], s61 offen offset:0 lds\n"
      "buffer_load_dwordx4 v41, s[20:23], s61 offen offset:1024 lds\n"
      "buffer_load_dwordx4 v42, s[20:23], s61 offen offset:2048 lds\n"
      "buffer_load_dwordx4 v43, s[20:23], s61 offen offset:3072 lds\n"
      "s_addk_i32 s61, 128\n"
      "s_addk_i32 m0, 0x4000\n"
      "s_cmp_eq_u32 s61, s11\n"
      "s_cselect_b32 s61, s98, s61\n"
      "s_bitcmp1_b32 s2, 0\n"
      "s_cbranch_scc1 .LMB\n"
      ".LMA:\n"
      "v_bfe_u32 v36, v0, 1, 3\n"
      "v_lshrrev_b32 v74, 4, v0\n"
      "v_xor_b32 v75, v36, v74\n"
      "v_and_b32 v76, 15, v0\n"
      "v_lshl_add_u32 v75, v75, 4, s95\n"
      "v_lshl_add_u32 v32, v76, 7, v75\n"
      "v_xor_b32 v33, 64, v32\n"
      "v_xor_b32 v34, 0x10000, v32\n"
      "v_xor_b32 v35, 64, v34\n"
      "v_and_b32 v66, 1, v74\n"
      "v_lshrrev_b32 v67, 5, v0\n"
      "v_and_b32 v68, 31, v0\n"
      "v_mov_b32 v30, 0x3f80\n"
      "v_mov_b32 v31, 0\n"
      "v_lshlrev_b32 v75, 4, v69\n"
      "v_lshlrev_b64 v[28:29], v75, v[30:31]\n"
      "v_mov_b32 v30, 0x3f803f80\n"
      "v_mov_b32 v31, v30\n"
      "s_movk_i32 s62, 20480\n"
      "s_lshl_b32 s63, s62, 1\n"
      "s_add_u32 s64, s62, s63\n"
      "s_lshl_b32 s58, s55, 7\n"
      "s_add_u32 s56, s3, s59\n"
      "s_lshl1_add_u32 s56, s56, s60\n"
      "s_sub_u32 s57, s56, 14\n"
      "s_min_u32 s56, s56, s57\n"
      "s_mul_i32 s34, s56, 86016\n"
      "s_add_u32 s56, s56, 4\n"
      "s_sub_u32 s57, s56, 14\n"
      "s_min_u32 s56, s56, s57\n"
      "s_mul_i32 s86, s56, 86016\n"
      "s_add_u32 s56, s56, 4\n"
      "s_sub_u32 s57, s56, 14\n"
      "s_min_u32 s56, s56, s57\n"
      "s_mul_i32 s87, s56, 86016\n"
      "s_sub_u32 s56, s3, 1\n"
      "s_min_u32 s56, s56, 6\n"
      "s_lshl2_add_u32 s56, s56, s52\n"
      "s_mul_i32 s99, s56, 43008\n"
      "v_lshlrev_b32 v75, 1, v66\n"
      "v_xor_b32 v75, v75, v65\n"
      "v_lshlrev_b32 v75, 4, v75\n"
      "v_mul_u32_u24 v3, 2688, v64\n"
      "s_movk_i32 s56, 0\n"
      "v_add3_u32 v3, v3, v75, s56\n"
      "v_bfe_u32 v71, v0, 2, 2\n"
      "v_lshlrev_b32 v70, 7, v71\n"
      "v_lshrrev_b32 v71, 1, v71\n"
      "v_lshl_or_b32 v75, v66, 1, v71\n"
      "v_lshl_add_u32 v75, v75, 5, v70\n"
      "v_lshl_add_u32 v37, v69, 3, v75\n"
      "v_lshl_add_u32 v6, v74, 10, v37\n"
      "v_lshlrev_b32 v1, 4, v0\n"
      "v_cmp_gt_u32 vcc, 16, v0\n"
      "v_bfrev_b32 v73, 1\n"
      "v_lshlrev_b32 v75, 3, v0\n"
      "v_cndmask_b32 v13, v73, v75, vcc\n"
      "s_sub_u32 s58, s12, 2\n"
      "s_cselect_b32 s58, s12, s58\n"
      "s_cselect_b32 s74, 0, -1\n"
      "s_and_b32 s59, s74, 4096\n"
      "v_and_b32 v75, 47, v0\n"
      "v_cmp_gt_u32 vcc, 14, v75\n"
      "v_mul_u32_u24 v76, s59, v66\n"
      "v_lshl_add_u32 v75, v75, 3, v76\n"
      "v_cndmask_b32 v14, v73, v75, vcc\n"
      "v_bfe_u32 v75, v0, 2, 1\n"
      "v_and_b32 v76, 19, v0\n"
      "v_lshl_or_b32 v76, v75, 3, v76\n"
      "v_lshl_or_b32 v76, v71, 2, v76\n"
      "v_mul_u32_u24 v75, 7168, v67\n"
      "v_lshl_add_u32 v4, v76, 4, v75\n"
      "v_lshl_or_b32 v15, v67, 31, v1\n"
      "v_lshlrev_b32 v75, 2, v67\n"
      "v_sub_u32 v75, v68, v75\n"
      "v_add_u32 v21, 25, v75\n"
      "v_add_u32 v22, -1, v67\n"
      "v_and_b32 v75, 0x600, v22\n"
      "v_lshl_or_b32 v7, v68, 4, v75\n"
      "v_or_b32 v2, 0x180, v1\n"
      "buffer_load_dwordx4 v40, s[20:23], s61 offen offset:0 lds\n"
      "buffer_load_dwordx4 v41, s[20:23], s61 offen offset:1024 lds\n"
      "buffer_load_dwordx4 v42, s[20:23], s61 offen offset:2048 lds\n"
      "buffer_load_dwordx4 v43, s[20:23], s61 offen offset:3072 lds\n"
      "s_addk_i32 s61, 128\n"
      "s_addk_i32 m0, 0x4000\n"
      "s_cmp_eq_u32 s61, s11\n"
      "s_cselect_b32 s61, s98, s61\n"
      "s_getpc_b64 s[14:15]\n"
      ".LHGA:\n"
      "s_add_u32 s14, s14, .LHBA-.LHGA\n"
      "s_addc_u32 s15, s15, 0\n"
      "s_mov_b64 exec, 0\n"
      "s_cmp_eq_u32 s52, 0\n"
      "s_cbranch_scc1 .LPAD9\n"
      "s_cmp_eq_u32 s52, 1\n"
      "s_cbranch_scc1 .LPAD10\n"
      "s_cmp_eq_u32 s52, 2\n"
      "s_cbranch_scc1 .LPAD11\n"
      "s_cmp_eq_u32 s52, 3\n"
      "s_cbranch_scc1 .LPAD12\n"
      ".LHBA:\n"
      "s_mov_b64 exec, -1\n"
      "v_or_b32 v8, 0x180, v7\n"
      "s_waitcnt vmcnt(8)\n"
      "ds_read_b128 a[0:3], v32 offset:0\n"
      "ds_read_b128 a[4:7], v32 offset:2048\n"
      "ds_read_b128 a[8:11], v33 offset:0\n"
      "ds_read_b128 a[12:15], v33 offset:2048\n"
      "buffer_load_dwordx4 v40, s[20:23], s61 offen offset:0 lds\n"
      "buffer_load_dwordx4 v41, s[20:23], s61 offen offset:1024 lds\n"
      "buffer_load_dwordx4 v42, s[20:23], s61 offen offset:2048 lds\n"
      "buffer_load_dwordx4 v43, s[20:23], s61 offen offset:3072 lds\n"
      "s_addk_i32 s61, 128\n"
      "s_addk_i32 m0, 0x4000\n"
      "s_cmp_eq_u32 s61, s11\n"
      "s_cselect_b32 s61, s98, s61\n"
      "s_waitcnt vmcnt(8) lgkmcnt(0)\n"
      "ds_read_b128 a[16:19], v32 offset:16384\n"
      "ds_read_b128 a[20:23], v32 offset:18432\n"
      "ds_read_b128 a[24:27], v33 offset:16384\n"
      "ds_read_b128 a[28:31], v33 offset:18432\n"
      "buffer_load_dwordx4 v40, s[20:23], s61 offen offset:0 lds\n"
      "buffer_load_dwordx4 v41, s[20:23], s61 offen offset:1024 lds\n"
      "buffer_load_dwordx4 v42, s[20:23], s61 offen offset:2048 lds\n"
      "buffer_load_dwordx4 v43, s[20:23], s61 offen offset:3072 lds\n"
      "s_addk_i32 s61, 128\n"
      "s_addk_i32 m0, 0x4000\n"
      "s_cmp_eq_u32 s61, s11\n"
      "s_cselect_b32 s61, s98, s61\n"
      "v_mfma_f32_4x4x4_16b_bf16 v[48:51], v[30:31], a[0:1], 0\n"
      "v_mfma_f32_4x4x4_16b_bf16 v[56:59], a[0:1], a[0:1], 0\n"
      "v_mfma_f32_4x4x4_16b_bf16 v[52:55], v[30:31], a[4:5], 0\n"
      "v_mfma_f32_4x4x4_16b_bf16 v[60:63], a[4:5], a[4:5], 0\n"
      "v_mfma_f32_4x4x4_16b_bf16 v[48:51], v[30:31], a[2:3], v[48:51]\n"
      "v_mfma_f32_4x4x4_16b_bf16 v[56:59], a[2:3], a[2:3], v[56:59]\n"
      "v_mfma_f32_4x4x4_16b_bf16 v[52:55], v[30:31], a[6:7], v[52:55]\n"
      "v_mfma_f32_4x4x4_16b_bf16 v[60:63], a[6:7], a[6:7], v[60:63]\n"
      "v_mfma_f32_4x4x4_16b_bf16 v[48:51], v[30:31], a[8:9], v[48:51]\n"
      "v_mfma_f32_4x4x4_16b_bf16 v[56:59], a[8:9], a[8:9], v[56:59]\n"
      "v_mfma_f32_4x4x4_16b_bf16 v[52:55], v[30:31], a[12:13], v[52:55]\n"
      "v_mfma_f32_4x4x4_16b_bf16 v[60:63], a[12:13], a[12:13], v[60:63]\n"
      "v_mfma_f32_4x4x4_16b_bf16 v[48:51], v[30:31], a[10:11], v[48:51]\n"
      "v_mfma_f32_4x4x4_16b_bf16 v[56:59], a[10:11], a[10:11], v[56:59]\n"
      "v_mfma_f32_4x4x4_16b_bf16 v[52:55], v[30:31], a[14:15], v[52:55]\n"
      "v_mfma_f32_4x4x4_16b_bf16 v[60:63], a[14:15], a[14:15], v[60:63]\n"
      "s_waitcnt vmcnt(8) lgkmcnt(0)\n"
      "ds_read_b128 a[32:35], v32 offset:32768\n"
      "ds_read_b128 a[36:39], v32 offset:34816\n"
      "ds_read_b128 a[40:43], v33 offset:32768\n"
      "ds_read_b128 a[44:47], v33 offset:34816\n"
      "buffer_load_dwordx4 v40, s[20:23], s61 offen offset:0 lds\n"
      "buffer_load_dwordx4 v41, s[20:23], s61 offen offset:1024 lds\n"
      "buffer_load_dwordx4 v42, s[20:23], s61 offen offset:2048 lds\n"
      "buffer_load_dwordx4 v43, s[20:23], s61 offen offset:3072 lds\n"
      "s_addk_i32 s61, 128\n"
      "s_addk_i32 m0, 0x4000\n"
      "s_cmp_eq_u32 s61, s11\n"
      "s_cselect_b32 s61, s98, s61\n"
      "v_mfma_f32_4x4x4_16b_bf16 v[48:51], v[30:31], a[16:17], v[48:51]\n"
      "v_mfma_f32_4x4x4_16b_bf16 v[56:59], a[16:17], a[16:17], v[56:59]\n"
      "v_mfma_f32_4x4x4_16b_bf16 v[52:55], v[30:31], a[20:21], v[52:55]\n"
      "v_mfma_f32_4x4x4_16b_bf16 v[60:63], a[20:21], a[20:21], v[60:63]\n"
      "v_mfma_f32_4x4x4_16b_bf16 v[48:51], v[30:31], a[18:19], v[48:51]\n"
      "v_mfma_f32_4x4x4_16b_bf16 v[56:59], a[18:19], a[18:19], v[56:59]\n"
      "v_mfma_f32_4x4x4_16b_bf16 v[52:55], v[30:31], a[22:23], v[52:55]\n"
      "v_mfma_f32_4x4x4_16b_bf16 v[60:63], a[22:23], a[22:23], v[60:63]\n"
      "v_mfma_f32_4x4x4_16b_bf16 v[48:51], v[30:31], a[24:25], v[48:51]\n"
      "v_mfma_f32_4x4x4_16b_bf16 v[56:59], a[24:25], a[24:25], v[56:59]\n"
      "v_mfma_f32_4x4x4_16b_bf16 v[52:55], v[30:31], a[28:29], v[52:55]\n"
      "v_mfma_f32_4x4x4_16b_bf16 v[60:63], a[28:29], a[28:29], v[60:63]\n"
      "v_mfma_f32_4x4x4_16b_bf16 v[48:51], v[30:31], a[26:27], v[48:51]\n"
      "v_mfma_f32_4x4x4_16b_bf16 v[56:59], a[26:27], a[26:27], v[56:59]\n"
      "v_mfma_f32_4x4x4_16b_bf16 v[52:55], v[30:31], a[30:31], v[52:55]\n"
      "v_mfma_f32_4x4x4_16b_bf16 v[60:63], a[30:31], a[30:31], v[60:63]\n"
      "s_waitcnt vmcnt(8) lgkmcnt(0)\n"
      "ds_read_b128 a[48:51], v32 offset:49152\n"
      "ds_read_b128 a[52:55], v32 offset:51200\n"
      "ds_read_b128 a[56:59], v33 offset:49152\n"
      "ds_read_b128 a[60:63], v33 offset:51200\n"
      "buffer_load_dwordx4 v40, s[20:23], s61 offen offset:0 lds\n"
      "buffer_load_dwordx4 v41, s[20:23], s61 offen offset:1024 lds\n"
      "buffer_load_dwordx4 v42, s[20:23], s61 offen offset:2048 lds\n"
      "buffer_load_dwordx4 v43, s[20:23], s61 offen offset:3072 lds\n"
      "v_mfma_f32_4x4x4_16b_bf16 v[48:51], v[30:31], a[32:33], v[48:51]\n"
      "v_mfma_f32_4x4x4_16b_bf16 v[56:59], a[32:33], a[32:33], v[56:59]\n"
      "v_mfma_f32_4x4x4_16b_bf16 v[52:55], v[30:31], a[36:37], v[52:55]\n"
      "v_mfma_f32_4x4x4_16b_bf16 v[60:63], a[36:37], a[36:37], v[60:63]\n"
      "v_mfma_f32_4x4x4_16b_bf16 v[48:51], v[30:31], a[34:35], v[48:51]\n"
      "v_mfma_f32_4x4x4_16b_bf16 v[56:59], a[34:35], a[34:35], v[56:59]\n"
      "v_mfma_f32_4x4x4_16b_bf16 v[52:55], v[30:31], a[38:39], v[52:55]\n"
      "v_mfma_f32_4x4x4_16b_bf16 v[60:63], a[38:39], a[38:39], v[60:63]\n"
      "v_mfma_f32_4x4x4_16b_bf16 v[48:51], v[30:31], a[40:41], v[48:51]\n"
      "v_mfma_f32_4x4x4_16b_bf16 v[56:59], a[40:41], a[40:41], v[56:59]\n"
      "v_mfma_f32_4x4x4_16b_bf16 v[52:55], v[30:31], a[44:45], v[52:55]\n"
      "v_mfma_f32_4x4x4_16b_bf16 v[60:63], a[44:45], a[44:45], v[60:63]\n"
      "v_mfma_f32_4x4x4_16b_bf16 v[48:51], v[30:31], a[42:43], v[48:51]\n"
      "v_mfma_f32_4x4x4_16b_bf16 v[56:59], a[42:43], a[42:43], v[56:59]\n"
      "v_mfma_f32_4x4x4_16b_bf16 v[52:55], v[30:31], a[46:47], v[52:55]\n"
      "v_mfma_f32_4x4x4_16b_bf16 v[60:63], a[46:47], a[46:47], v[60:63]\n"
      "s_waitcnt vmcnt(8) lgkmcnt(0)\n"
      "ds_read_b128 a[64:67], v34 offset:0\n"
      "ds_read_b128 a[68:71], v34 offset:2048\n"
      ".LPAD9:\n"
      "s_cbranch_execz .LRET\n"
      "ds_read_b128 a[72:75], v35 offset:0\n"
      "ds_read_b128 a[76:79], v35 offset:2048\n"
      "s_add_u32 m0, s95, 114688\n"
      "v_add_u32 v19, s34, v3\n"
      "v_xor_b32 v18, 64, v19\n"
      "buffer_load_dwordx4 v19, s[24:27], 0 offen offset:0 lds\n"
      "buffer_load_dwordx4 v18, s[24:27], s62 offen offset:1024 lds\n"
      "buffer_load_dwordx4 v19, s[24:27], s63 offen offset:2048 lds\n"
      "buffer_load_dwordx4 v18, s[24:27], s64 offen offset:3072 lds\n"
      "v_mfma_f32_4x4x4_16b_bf16 v[48:51], v[30:31], a[48:49], v[48:51]\n"
      "v_mfma_f32_4x4x4_16b_bf16 v[56:59], a[48:49], a[48:49], v[56:59]\n"
      "v_mfma_f32_4x4x4_16b_bf16 v[52:55], v[30:31], a[52:53], v[52:55]\n"
      "v_mfma_f32_4x4x4_16b_bf16 v[60:63], a[52:53], a[52:53], v[60:63]\n"
      "v_mfma_f32_4x4x4_16b_bf16 v[48:51], v[30:31], a[50:51], v[48:51]\n"
      "v_mfma_f32_4x4x4_16b_bf16 v[56:59], a[50:51], a[50:51], v[56:59]\n"
      "v_mfma_f32_4x4x4_16b_bf16 v[52:55], v[30:31], a[54:55], v[52:55]\n"
      "v_mfma_f32_4x4x4_16b_bf16 v[60:63], a[54:55], a[54:55], v[60:63]\n"
      "v_mfma_f32_4x4x4_16b_bf16 v[48:51], v[30:31], a[56:57], v[48:51]\n"
      "v_mfma_f32_4x4x4_16b_bf16 v[56:59], a[56:57], a[56:57], v[56:59]\n"
      "v_mfma_f32_4x4x4_16b_bf16 v[52:55], v[30:31], a[60:61], v[52:55]\n"
      "v_mfma_f32_4x4x4_16b_bf16 v[60:63], a[60:61], a[60:61], v[60:63]\n"
      "v_mfma_f32_4x4x4_16b_bf16 v[48:51], v[30:31], a[58:59], v[48:51]\n"
      "v_mfma_f32_4x4x4_16b_bf16 v[56:59], a[58:59], a[58:59], v[56:59]\n"
      "v_mfma_f32_4x4x4_16b_bf16 v[52:55], v[30:31], a[62:63], v[52:55]\n"
      "v_mfma_f32_4x4x4_16b_bf16 v[60:63], a[62:63], a[62:63], v[60:63]\n"
      "s_waitcnt vmcnt(8) lgkmcnt(0)\n"
      "ds_read_b128 a[80:83], v34 offset:16384\n"
      "ds_read_b128 a[84:87], v34 offset:18432\n"
      "ds_read_b128 a[88:91], v35 offset:16384\n"
      "ds_read_b128 a[92:95], v35 offset:18432\n"
      "s_add_u32 m0, s95, 131072\n"
      "v_add_u32 v19, s86, v3\n"
      "v_xor_b32 v18, 64, v19\n"
      "buffer_load_dwordx4 v19, s[24:27], 0 offen offset:0 lds\n"
      "buffer_load_dwordx4 v18, s[24:27], s62 offen offset:1024 lds\n"
      "buffer_load_dwordx4 v19, s[24:27], s63 offen offset:2048 lds\n"
      "buffer_load_dwordx4 v18, s[24:27], s64 offen offset:3072 lds\n"
      "v_mfma_f32_4x4x4_16b_bf16 v[48:51], v[30:31], a[64:65], v[48:51]\n"
      "v_mfma_f32_4x4x4_16b_bf16 v[56:59], a[64:65], a[64:65], v[56:59]\n"
      "v_mfma_f32_4x4x4_16b_bf16 v[52:55], v[30:31], a[68:69], v[52:55]\n"
      "v_mfma_f32_4x4x4_16b_bf16 v[60:63], a[68:69], a[68:69], v[60:63]\n"
      "v_mfma_f32_4x4x4_16b_bf16 v[48:51], v[30:31], a[66:67], v[48:51]\n"
      "v_mfma_f32_4x4x4_16b_bf16 v[56:59], a[66:67], a[66:67], v[56:59]\n"
      "v_mfma_f32_4x4x4_16b_bf16 v[52:55], v[30:31], a[70:71], v[52:55]\n"
      "v_mfma_f32_4x4x4_16b_bf16 v[60:63], a[70:71], a[70:71], v[60:63]\n"
      "v_mfma_f32_4x4x4_16b_bf16 v[48:51], v[30:31], a[72:73], v[48:51]\n"
      "v_mfma_f32_4x4x4_16b_bf16 v[56:59], a[72:73], a[72:73], v[56:59]\n"
      "v_mfma_f32_4x4x4_16b_bf16 v[52:55], v[30:31], a[76:77], v[52:55]\n"
      "v_mfma_f32_4x4x4_16b_bf16 v[60:63], a[76:77], a[76:77], v[60:63]\n"
      "v_mfma_f32_4x4x4_16b_bf16 v[48:51], v[30:31], a[74:75], v[48:51]\n"
      "v_mfma_f32_4x4x4_16b_bf16 v[56:59], a[74:75], a[74:75], v[56:59]\n"
      "v_mfma_f32_4x4x4_16b_bf16 v[52:55], v[30:31], a[78:79], v[52:55]\n"
      "v_mfma_f32_4x4x4_16b_bf16 v[60:63], a[78:79], a[78:79], v[60:63]\n"
      "s_waitcnt vmcnt(8) lgkmcnt(0)\n"
      "ds_read_b128 a[96:99], v34 offset:32768\n"
      "ds_read_b128 a[100:103], v34 offset:34816\n"
      "ds_read_b128 a[104:107], v35 offset:32768\n"
      "ds_read_b128 a[108:111], v35 offset:34816\n"
      "v_mfma_f32_4x4x4_16b_bf16 v[48:51], v[30:31], a[80:81], v[48:51]\n"
      "v_mfma_f32_4x4x4_16b_bf16 v[56:59], a[80:81], a[80:81], v[56:59]\n"
      "v_mfma_f32_4x4x4_16b_bf16 v[52:55], v[30:31], a[84:85], v[52:55]\n"
      "v_mfma_f32_4x4x4_16b_bf16 v[60:63], a[84:85], a[84:85], v[60:63]\n"
      "v_mfma_f32_4x4x4_16b_bf16 v[48:51], v[30:31], a[82:83], v[48:51]\n"
      "v_mfma_f32_4x4x4_16b_bf16 v[56:59], a[82:83], a[82:83], v[56:59]\n"
      "v_mfma_f32_4x4x4_16b_bf16 v[52:55], v[30:31], a[86:87], v[52:55]\n"
      "v_mfma_f32_4x4x4_16b_bf16 v[60:63], a[86:87], a[86:87], v[60:63]\n"
      "v_mfma_f32_4x4x4_16b_bf16 v[48:51], v[30:31], a[88:89], v[48:51]\n"
      "v_mfma_f32_4x4x4_16b_bf16 v[56:59], a[88:89], a[88:89], v[56:59]\n"
      "v_mfma_f32_4x4x4_16b_bf16 v[52:55], v[30:31], a[92:93], v[52:55]\n"
      "v_mfma_f32_4x4x4_16b_bf16 v[60:63], a[92:93], a[92:93], v[60:63]\n"
      "v_mfma_f32_4x4x4_16b_bf16 v[48:51], v[30:31], a[90:91], v[48:51]\n"
      "v_mfma_f32_4x4x4_16b_bf16 v[56:59], a[90:91], a[90:91], v[56:59]\n"
      "v_mfma_f32_4x4x4_16b_bf16 v[52:55], v[30:31], a[94:95], v[52:55]\n"
      "v_mfma_f32_4x4x4_16b_bf16 v[60:63], a[94:95], a[94:95], v[60:63]\n"
      "s_waitcnt lgkmcnt(0)\n"
      "v_mfma_f32_4x4x4_16b_bf16 v[48:51], v[30:31], a[96:97], v[48:51]\n"
      "v_mfma_f32_4x4x4_16b_bf16 v[56:59], a[96:97], a[96:97], v[56:59]\n"
      "v_mfma_f32_4x4x4_16b_bf16 v[52:55], v[30:31], a[100:101], v[52:55]\n"
      "v_mfma_f32_4x4x4_16b_bf16 v[60:63], a[100:101], a[100:101], v[60:63]\n"
      "v_mfma_f32_4x4x4_16b_bf16 v[48:51], v[30:31], a[98:99], v[48:51]\n"
      "v_mfma_f32_4x4x4_16b_bf16 v[56:59], a[98:99], a[98:99], v[56:59]\n"
      "v_mfma_f32_4x4x4_16b_bf16 v[52:55], v[30:31], a[102:103], v[52:55]\n"
      "v_mfma_f32_4x4x4_16b_bf16 v[60:63], a[102:103], a[102:103], v[60:63]\n"
      "v_mfma_f32_4x4x4_16b_bf16 v[48:51], v[30:31], a[104:105], v[48:51]\n"
      "v_mfma_f32_4x4x4_16b_bf16 v[56:59], a[104:105], a[104:105], v[56:59]\n"
      "v_mfma_f32_4x4x4_16b_bf16 v[52:55], v[30:31], a[108:109], v[52:55]\n"
      "v_mfma_f32_4x4x4_16b_bf16 v[60:63], a[108:109], a[108:109], v[60:63]\n"
      "v_mfma_f32_4x4x4_16b_bf16 v[48:51], v[30:31], a[106:107], v[48:51]\n"
      "v_mfma_f32_4x4x4_16b_bf16 v[56:59], a[106:107], a[106:107], v[56:59]\n"
      "v_mfma_f32_4x4x4_16b_bf16 v[52:55], v[30:31], a[110:111], v[52:55]\n"
      "v_mfma_f32_4x4x4_16b_bf16 v[60:63], a[110:111], a[110:111], v[60:63]\n"
      "s_nop 3\n"
      "v_permlane16_swap_b32 v48, v52\n"
      "v_add_f32 v32, v48, v52\n"
      "v_add_f32 v33, v48, v52\n"
      "v_xor_b32 v9, 0x18000, v6\n"
      "v_xor_b32 v10, 0x18020, v6\n"
      "v_permlane32_swap_b32 v32, v33\n"
      "v_add_f32 v32, v32, v33\n"
      "v_mul_f32 v34, 0xbb124925, v32\n"
      "v_fmamk_f32 v32, v34, 0x43e00000, v32\n"
      "v_fmamk_f32 v34, v32, 0xbb124925, v34\n"
      "v_cvt_pk_bf16_f32 v24, 0, v34\n"
      "v_cvt_pk_bf16_f32 v40, 0, v34\n"
      "v_xor_b32 v11, 0x18040, v6\n"
      "v_xor_b32 v12, 0x18060, v6\n"
      "v_permlane16_swap_b32 v24, v40\n"
      "v_mov_b32 v25, v24\n"
      "v_mov_b32 v26, v24\n"
      "v_mov_b32 v27, v24\n"
      "v_mov_b32 v41, v40\n"
      "v_mov_b32 v42, v40\n"
      "v_mov_b32 v43, v40\n"
      "v_mfma_f32_4x4x4_16b_bf16 v[112:115], v[28:29], a[0:1], v[24:27]\n"
      "v_mfma_f32_4x4x4_16b_bf16 v[116:119], v[28:29], a[2:3], v[24:27]\n"
      "v_mfma_f32_4x4x4_16b_bf16 v[120:123], v[28:29], a[4:5], v[40:43]\n"
      "v_mfma_f32_4x4x4_16b_bf16 v[124:127], v[28:29], a[6:7], v[40:43]\n"
      "v_mfma_f32_4x4x4_16b_bf16 v[128:131], v[28:29], a[8:9], v[24:27]\n"
      "v_mfma_f32_4x4x4_16b_bf16 v[132:135], v[28:29], a[10:11], v[24:27]\n"
      "v_mfma_f32_4x4x4_16b_bf16 v[136:139], v[28:29], a[12:13], v[40:43]\n"
      "v_mfma_f32_4x4x4_16b_bf16 v[140:143], v[28:29], a[14:15], v[40:43]\n"
      "v_cvt_pk_bf16_f32 v144, v112, v113\n"
      "v_cvt_pk_bf16_f32 v145, v114, v115\n"
      "v_cvt_pk_bf16_f32 v146, v116, v117\n"
      "v_cvt_pk_bf16_f32 v147, v118, v119\n"
      "v_cvt_pk_bf16_f32 v148, v120, v121\n"
      "v_cvt_pk_bf16_f32 v149, v122, v123\n"
      "v_cvt_pk_bf16_f32 v150, v124, v125\n"
      "v_cvt_pk_bf16_f32 v151, v126, v127\n"
      "v_mfma_f32_4x4x4_16b_bf16 v[112:115], v[28:29], a[16:17], v[24:27]\n"
      "v_mfma_f32_4x4x4_16b_bf16 v[116:119], v[28:29], a[18:19], v[24:27]\n"
      "v_mfma_f32_4x4x4_16b_bf16 v[120:123], v[28:29], a[20:21], v[40:43]\n"
      "v_mfma_f32_4x4x4_16b_bf16 v[124:127], v[28:29], a[22:23], v[40:43]\n"
      "v_cvt_pk_bf16_f32 v152, v128, v129\n"
      "v_cvt_pk_bf16_f32 v153, v130, v131\n"
      "v_cvt_pk_bf16_f32 v154, v132, v133\n"
      "v_cvt_pk_bf16_f32 v155, v134, v135\n"
      "v_cvt_pk_bf16_f32 v156, v136, v137\n"
      "v_cvt_pk_bf16_f32 v157, v138, v139\n"
      "v_cvt_pk_bf16_f32 v158, v140, v141\n"
      "v_cvt_pk_bf16_f32 v159, v142, v143\n"
      "v_mfma_f32_4x4x4_16b_bf16 v[128:131], v[28:29], a[24:25], v[24:27]\n"
      "v_mfma_f32_4x4x4_16b_bf16 v[132:135], v[28:29], a[26:27], v[24:27]\n"
      "v_mfma_f32_4x4x4_16b_bf16 v[136:139], v[28:29], a[28:29], v[40:43]\n"
      "v_mfma_f32_4x4x4_16b_bf16 v[140:143], v[28:29], a[30:31], v[40:43]\n"
      "s_waitcnt vmcnt(4) lgkmcnt(0)\n"
      "s_barrier\n"
      "ds_read_b64_tr_b16 a[216:217], v9 offset:16384\n"
      "ds_read_b64_tr_b16 a[218:219], v9 offset:16896\n"
      "ds_read_b64_tr_b16 a[220:221], v10 offset:16384\n"
      "ds_read_b64_tr_b16 a[222:223], v10 offset:16896\n"
      "ds_read_b64_tr_b16 a[224:225], v11 offset:16384\n"
      "ds_read_b64_tr_b16 a[226:227], v11 offset:16896\n"
      "ds_read_b64_tr_b16 a[228:229], v12 offset:16384\n"
      "ds_read_b64_tr_b16 a[230:231], v12 offset:16896\n"
      "ds_read_b64_tr_b16 a[232:233], v9 offset:20480\n"
      "ds_read_b64_tr_b16 a[234:235], v9 offset:20992\n"
      "ds_read_b64_tr_b16 a[236:237], v10 offset:20480\n"
      "ds_read_b64_tr_b16 a[238:239], v10 offset:20992\n"
      "ds_read_b64_tr_b16 a[240:241], v11 offset:20480\n"
      "ds_read_b64_tr_b16 a[242:243], v11 offset:20992\n"
      "s_waitcnt lgkmcnt(6)\n"
      "v_mfma_f32_16x16x32_bf16 v[80:83], a[216:219], v[144:147], 0\n"
      "ds_read_b64_tr_b16 a[244:245], v12 offset:20480\n"
      "ds_read_b64_tr_b16 a[246:247], v12 offset:20992\n"
      "v_mfma_f32_16x16x32_bf16 v[84:87], a[216:219], v[148:151], 0\n"
      "s_add_u32 m0, s95, 147456\n"
      "v_add_u32 v19, s87, v3\n"
      "v_xor_b32 v18, 64, v19\n"
      "buffer_load_dwordx4 v19, s[24:27], 0 offen offset:0 lds\n"
      "v_cvt_pk_bf16_f32 v160, v112, v113\n"
      "v_cvt_pk_bf16_f32 v161, v114, v115\n"
      "v_mfma_f32_16x16x32_bf16 v[88:91], a[220:223], v[144:147], 0\n"
      "ds_read_b64_tr_b16 a[248:249], v9 offset:24576\n"
      "ds_read_b64_tr_b16 a[250:251], v9 offset:25088\n"
      "v_mfma_f32_16x16x32_bf16 v[92:95], a[220:223], v[148:151], 0\n"
      "v_cvt_pk_bf16_f32 v162, v116, v117\n"
      "v_cvt_pk_bf16_f32 v163, v118, v119\n"
      "v_bfe_i32 v35, v0, 0, 1\n"
      "v_bfe_i32 v36, v0, 1, 1\n"
      "v_mfma_f32_16x16x32_bf16 v[96:99], a[224:227], v[144:147], 0\n"
      "ds_read_b64_tr_b16 a[252:253], v10 offset:24576\n"
      "ds_read_b64_tr_b16 a[254:255], v10 offset:25088\n"
      "v_mfma_f32_16x16x32_bf16 v[100:103], a[224:227], v[148:151], 0\n"
      "buffer_load_dwordx4 v18, s[24:27], s62 offen offset:1024 lds\n"
      "v_cvt_pk_bf16_f32 v164, v120, v121\n"
      "v_cvt_pk_bf16_f32 v165, v122, v123\n"
      "v_bfi_b32 v56, v35, v57, v56\n"
      "v_mfma_f32_16x16x32_bf16 v[104:107], a[228:231], v[144:147], 0\n"
      "ds_read_b64_tr_b16 a[216:217], v11 offset:24576\n"
      "ds_read_b64_tr_b16 a[218:219], v11 offset:25088\n"
      "v_mfma_f32_16x16x32_bf16 v[108:111], a[228:231], v[148:151], 0\n"
      "v_cvt_pk_bf16_f32 v166, v124, v125\n"
      "v_cvt_pk_bf16_f32 v167, v126, v127\n"
      "v_bfi_b32 v58, v35, v59, v58\n"
      "v_bfi_b32 v60, v35, v61, v60\n"
      "s_lshl3_add_u32 s60, s53, s52\n"
      "s_lshl_b32 s89, s60, 12\n"
      "s_waitcnt lgkmcnt(6)\n"
      "v_mfma_f32_16x16x32_bf16 v[80:83], a[232:235], v[152:155], v[80:83]\n"
      "ds_read_b64_tr_b16 a[220:221], v12 offset:24576\n"
      "ds_read_b64_tr_b16 a[222:223], v12 offset:25088\n"
      "v_mfma_f32_16x16x32_bf16 v[84:87], a[232:235], v[156:159], v[84:87]\n"
      "buffer_load_dwordx4 v19, s[24:27], s63 offen offset:2048 lds\n"
      "v_cvt_pk_bf16_f32 v168, v128, v129\n"
      "v_cvt_pk_bf16_f32 v169, v130, v131\n"
      "v_mfma_f32_16x16x32_bf16 v[88:91], a[236:239], v[152:155], v[88:91]\n"
      "ds_read_b64_tr_b16 a[224:225], v9 offset:28672\n"
      "ds_read_b64_tr_b16 a[226:227], v9 offset:29184\n"
      "v_mfma_f32_16x16x32_bf16 v[92:95], a[236:239], v[156:159], v[92:95]\n"
      "v_cvt_pk_bf16_f32 v170, v132, v133\n"
      "v_cvt_pk_bf16_f32 v171, v134, v135\n"
      "v_bfi_b32 v62, v35, v63, v62\n"
      "v_bfi_b32 v56, v36, v58, v56\n"
      "s_add_u32 s89, s89, 262144\n"
      "s_mul_i32 s60, s60, 71680\n"
      "v_mfma_f32_16x16x32_bf16 v[96:99], a[240:243], v[152:155], v[96:99]\n"
      "ds_read_b64_tr_b16 a[228:229], v10 offset:28672\n"
      "ds_read_b64_tr_b16 a[230:231], v10 offset:29184\n"
      "v_mfma_f32_16x16x32_bf16 v[100:103], a[240:243], v[156:159], v[100:103]\n"
      "s_mov_b64 exec, 1\n"
      "buffer_load_dwordx2 v[16:17], v0, s[48:51], 0 offen sc1\n"
      "s_mov_b64 exec, -1\n"
      "buffer_load_dwordx4 v18, s[24:27], s64 offen offset:3072 lds\n"
      "v_cvt_pk_bf16_f32 v172, v136, v137\n"
      "v_cvt_pk_bf16_f32 v173, v138, v139\n"
      "v_mfma_f32_16x16x32_bf16 v[104:107], a[244:247], v[152:155], v[104:107]\n"
      "ds_read_b64_tr_b16 a[232:233], v11 offset:28672\n"
      "ds_read_b64_tr_b16 a[234:235], v11 offset:29184\n"
      "v_mfma_f32_16x16x32_bf16 v[108:111], a[244:247], v[156:159], v[108:111]\n"
      "v_cvt_pk_bf16_f32 v174, v140, v141\n"
      "v_cvt_pk_bf16_f32 v175, v142, v143\n"
      "v_bfi_b32 v60, v36, v62, v60\n"
      "v_mul_f32 v34, v34, -v34\n"
      "s_add_u32 s82, s60, 5300224\n"
      "v_mfma_f32_4x4x4_16b_bf16 v[112:115], v[28:29], a[32:33], v[24:27]\n"
      "v_mfma_f32_4x4x4_16b_bf16 v[116:119], v[28:29], a[34:35], v[24:27]\n"
      "v_mfma_f32_4x4x4_16b_bf16 v[120:123], v[28:29], a[36:37], v[40:43]\n"
      "v_mfma_f32_4x4x4_16b_bf16 v[124:127], v[28:29], a[38:39], v[40:43]\n"
      "s_waitcnt vmcnt(5) lgkmcnt(6)\n"
      "s_barrier\n"
      "v_mfma_f32_16x16x32_bf16 v[80:83], a[248:251], v[160:163], v[80:83]\n"
      "ds_read_b64_tr_b16 a[236:237], v12 offset:28672\n"
      "ds_read_b64_tr_b16 a[238:239], v12 offset:29184\n"
      "v_mfma_f32_16x16x32_bf16 v[84:87], a[248:251], v[164:167], v[84:87]\n"
      "s_lshr_b32 s35, s95, 1\n"
      "s_add_u32 m0, s35, 114688\n"
      "v_add_u32 v19, s99, v3\n"
      "v_xor_b32 v18, 64, v19\n"
      "buffer_load_dwordx4 v19, s[24:27], 0 offen offset:0 lds\n"
      "v_cvt_pk_bf16_f32 v176, v112, v113\n"
      "v_cvt_pk_bf16_f32 v177, v114, v115\n"
      "v_mfma_f32_16x16x32_bf16 v[88:91], a[252:255], v[160:163], v[88:91]\n"
      "ds_read_b64_tr_b16 a[240:241], v9 offset:32768\n"
      "ds_read_b64_tr_b16 a[242:243], v9 offset:33280\n"
      "v_mfma_f32_16x16x32_bf16 v[92:95], a[252:255], v[164:167], v[92:95]\n"
      "v_cvt_pk_bf16_f32 v178, v116, v117\n"
      "v_cvt_pk_bf16_f32 v179, v118, v119\n"
      "v_permlane16_swap_b32 v56, v60\n"
      "v_add_f32 v35, v56, v60\n"
      "v_add_f32 v36, v56, v60\n"
      "s_mul_i32 s68, s12, 57344\n"
      "v_mfma_f32_16x16x32_bf16 v[96:99], a[216:219], v[160:163], v[96:99]\n"
      "ds_read_b64_tr_b16 a[244:245], v10 offset:32768\n"
      "ds_read_b64_tr_b16 a[246:247], v10 offset:33280\n"
      "v_mfma_f32_16x16x32_bf16 v[100:103], a[216:219], v[164:167], v[100:103]\n"
      "buffer_load_dwordx4 v18, s[24:27], s62 offen offset:1024 lds\n"
      "v_cvt_pk_bf16_f32 v180, v120, v121\n"
      "v_cvt_pk_bf16_f32 v181, v122, v123\n"
      "s_mul_i32 s69, s58, 57344\n"
      "v_mfma_f32_16x16x32_bf16 v[104:107], a[220:223], v[160:163], v[104:107]\n"
      "ds_read_b64_tr_b16 a[248:249], v11 offset:32768\n"
      "ds_read_b64_tr_b16 a[250:251], v11 offset:33280\n"
      "v_mfma_f32_16x16x32_bf16 v[108:111], a[220:223], v[164:167], v[108:111]\n"
      "v_cvt_pk_bf16_f32 v182, v124, v125\n"
      "v_cvt_pk_bf16_f32 v183, v126, v127\n"
      "v_permlane32_swap_b32 v35, v36\n"
      "v_add_f32 v35, v35, v36\n"
      "s_add_u32 s70, s68, 1077248\n"
      "s_add_u32 s71, s70, 14336\n"
      "v_mfma_f32_4x4x4_16b_bf16 v[128:131], v[28:29], a[40:41], v[24:27]\n"
      "v_mfma_f32_4x4x4_16b_bf16 v[132:135], v[28:29], a[42:43], v[24:27]\n"
      "v_mfma_f32_4x4x4_16b_bf16 v[136:139], v[28:29], a[44:45], v[40:43]\n"
      "v_mfma_f32_4x4x4_16b_bf16 v[140:143], v[28:29], a[46:47], v[40:43]\n"
      "s_waitcnt lgkmcnt(6)\n"
      "v_mfma_f32_16x16x32_bf16 v[80:83], a[224:227], v[168:171], v[80:83]\n"
      "ds_read_b64_tr_b16 a[252:253], v12 offset:32768\n"
      "ds_read_b64_tr_b16 a[254:255], v12 offset:33280\n"
      "v_mfma_f32_16x16x32_bf16 v[84:87], a[224:227], v[172:175], v[84:87]\n"
      "s_add_u32 m0, s95, 0\n"
      "v_add_u32 v3, 128, v3\n"
      "v_add_u32 v19, s34, v3\n"
      "v_xor_b32 v18, 64, v19\n"
      "buffer_load_dwordx4 v19, s[24:27], 0 offen offset:0 lds\n"
      "v_cvt_pk_bf16_f32 v184, v128, v129\n"
      "v_cvt_pk_bf16_f32 v185, v130, v131\n"
      "v_mfma_f32_16x16x32_bf16 v[88:91], a[228:231], v[168:171], v[88:91]\n"
      "ds_read_b64_tr_b16 a[216:217], v9 offset:36864\n"
      "ds_read_b64_tr_b16 a[218:219], v9 offset:37376\n"
      "v_mfma_f32_16x16x32_bf16 v[92:95], a[228:231], v[172:175], v[92:95]\n"
      "v_cvt_pk_bf16_f32 v186, v132, v133\n"
      "v_cvt_pk_bf16_f32 v187, v134, v135\n"
      "v_fmamk_f32 v35, v35, 0x3b124925, v34\n"
      "v_max_f32 v35, 0, v35\n"
      "s_add_u32 s72, s69, 1091592\n"
      "v_mfma_f32_16x16x32_bf16 v[96:99], a[232:235], v[168:171], v[96:99]\n"
      "ds_read_b64_tr_b16 a[220:221], v10 offset:36864\n"
      "ds_read_b64_tr_b16 a[222:223], v10 offset:37376\n"
      "v_mfma_f32_16x16x32_bf16 v[100:103], a[232:235], v[172:175], v[100:103]\n"
      "buffer_load_dwordx4 v18, s[24:27], s62 offen offset:1024 lds\n"
      "v_cvt_pk_bf16_f32 v188, v136, v137\n"
      "v_cvt_pk_bf16_f32 v189, v138, v139\n"
      "v_cvt_pk_bf16_f32 v35, 0, v35\n"
      "s_add_u32 s68, s68, 1048576\n"
      "v_mfma_f32_16x16x32_bf16 v[104:107], a[236:239], v[168:171], v[104:107]\n"
      "ds_read_b64_tr_b16 a[224:225], v11 offset:36864\n"
      "ds_read_b64_tr_b16 a[226:227], v11 offset:37376\n"
      "v_mfma_f32_16x16x32_bf16 v[108:111], a[236:239], v[172:175], v[108:111]\n"
      "v_cvt_pk_bf16_f32 v190, v140, v141\n"
      "v_cvt_pk_bf16_f32 v191, v142, v143\n"
      "v_add_f32 v35, 0x3727c5ac, v35\n"
      "v_cvt_pk_bf16_f32 v35, 0, v35\n"
      "s_add_u32 s69, s69, 1048576\n"
      "v_mfma_f32_4x4x4_16b_bf16 v[112:115], v[28:29], a[48:49], v[24:27]\n"
      "v_mfma_f32_4x4x4_16b_bf16 v[116:119], v[28:29], a[50:51], v[24:27]\n"
      "v_mfma_f32_4x4x4_16b_bf16 v[120:123], v[28:29], a[52:53], v[40:43]\n"
      "v_mfma_f32_4x4x4_16b_bf16 v[124:127], v[28:29], a[54:55], v[40:43]\n"
      "s_waitcnt lgkmcnt(6)\n"
      "v_mfma_f32_16x16x32_bf16 v[80:83], a[240:243], v[176:179], v[80:83]\n"
      "ds_read_b64_tr_b16 a[228:229], v12 offset:36864\n"
      "ds_read_b64_tr_b16 a[230:231], v12 offset:37376\n"
      "v_mfma_f32_16x16x32_bf16 v[84:87], a[240:243], v[180:183], v[84:87]\n"
      "buffer_load_dwordx4 v19, s[24:27], s63 offen offset:2048 lds\n"
      "v_cvt_pk_bf16_f32 v192, v112, v113\n"
      "v_cvt_pk_bf16_f32 v193, v114, v115\n"
      "s_lshl_b32 s88, s58, 11\n"
      "v_mfma_f32_16x16x32_bf16 v[88:91], a[244:247], v[176:179], v[88:91]\n"
      "ds_read_b64_tr_b16 a[232:233], v9 offset:40960\n"
      "ds_read_b64_tr_b16 a[234:235], v9 offset:41472\n"
      "v_mfma_f32_16x16x32_bf16 v[92:95], a[244:247], v[180:183], v[92:95]\n"
      "v_cvt_pk_bf16_f32 v194, v116, v117\n"
      "v_cvt_pk_bf16_f32 v195, v118, v119\n"
      "v_sqrt_f32 v35, v35\n"
      "s_add_u32 s88, s88, 131072\n"
      "v_mfma_f32_16x16x32_bf16 v[96:99], a[248:251], v[176:179], v[96:99]\n"
      "ds_read_b64_tr_b16 a[236:237], v10 offset:40960\n"
      "ds_read_b64_tr_b16 a[238:239], v10 offset:41472\n"
      "v_mfma_f32_16x16x32_bf16 v[100:103], a[248:251], v[180:183], v[100:103]\n"
      "buffer_load_dwordx4 v18, s[24:27], s64 offen offset:3072 lds\n"
      "v_cvt_pk_bf16_f32 v196, v120, v121\n"
      "v_cvt_pk_bf16_f32 v197, v122, v123\n"
      "v_mov_b32 v44, 0\n"
      "v_lshlrev_b32 v46, 10, v0\n"
      "v_mfma_f32_16x16x32_bf16 v[104:107], a[252:255], v[176:179], v[104:107]\n"
      "ds_read_b64_tr_b16 a[240:241], v11 offset:40960\n"
      "ds_read_b64_tr_b16 a[242:243], v11 offset:41472\n"
      "v_mfma_f32_16x16x32_bf16 v[108:111], a[252:255], v[180:183], v[108:111]\n"
      "v_cvt_pk_bf16_f32 v198, v124, v125\n"
      "v_cvt_pk_bf16_f32 v199, v126, v127\n"
      "v_cvt_pk_bf16_f32 v35, 0, v35\n"
      "v_rcp_f32 v23, v35\n"
      "v_mfma_f32_4x4x4_16b_bf16 v[128:131], v[28:29], a[56:57], v[24:27]\n"
      "v_mfma_f32_4x4x4_16b_bf16 v[132:135], v[28:29], a[58:59], v[24:27]\n"
      "v_mfma_f32_4x4x4_16b_bf16 v[136:139], v[28:29], a[60:61], v[40:43]\n"
      "v_mfma_f32_4x4x4_16b_bf16 v[140:143], v[28:29], a[62:63], v[40:43]\n"
      "s_waitcnt lgkmcnt(6)\n"
      "v_mfma_f32_16x16x32_bf16 v[80:83], a[216:219], v[184:187], v[80:83]\n"
      "ds_read_b64_tr_b16 a[244:245], v12 offset:40960\n"
      "ds_read_b64_tr_b16 a[246:247], v12 offset:41472\n"
      "v_mfma_f32_16x16x32_bf16 v[84:87], a[216:219], v[188:191], v[84:87]\n"
      "s_add_u32 m0, s95, 16384\n"
      "v_add_u32 v19, s86, v3\n"
      "v_xor_b32 v18, 64, v19\n"
      "buffer_load_dwordx4 v19, s[24:27], 0 offen offset:0 lds\n"
      "buffer_load_dword a215, v14, s[48:51], s88 offen sc1\n"
      "v_cvt_pk_bf16_f32 v200, v128, v129\n"
      "v_cvt_pk_bf16_f32 v201, v130, v131\n"
      "v_mfma_f32_16x16x32_bf16 v[88:91], a[220:223], v[184:187], v[88:91]\n"
      "ds_read_b64_tr_b16 a[248:249], v9 offset:45056\n"
      "ds_read_b64_tr_b16 a[250:251], v9 offset:45568\n"
      "v_mfma_f32_16x16x32_bf16 v[92:95], a[220:223], v[188:191], v[92:95]\n"
      "s_mov_b64 exec, 3\n"
      "buffer_load_dword a215, v46, s[48:51], s89 offen sc1\n"
      "s_mov_b64 exec, -1\n"
      "v_cvt_pk_bf16_f32 v202, v132, v133\n"
      "v_cvt_pk_bf16_f32 v203, v134, v135\n"
      "v_mul_f32 v20, 0x3e8b97f6, v23\n"
      "v_not_b32 v23, v22\n"
      "v_mfma_f32_16x16x32_bf16 v[96:99], a[224:227], v[184:187], v[96:99]\n"
      "ds_read_b64_tr_b16 a[252:253], v10 offset:45056\n"
      "ds_read_b64_tr_b16 a[254:255], v10 offset:45568\n"
      "v_mfma_f32_16x16x32_bf16 v[100:103], a[224:227], v[188:191], v[100:103]\n"
      "buffer_load_dwordx4 v18, s[24:27], s62 offen offset:1024 lds\n"
      "v_cvt_pk_bf16_f32 v204, v136, v137\n"
      "v_cvt_pk_bf16_f32 v205, v138, v139\n"
      "v_mfma_f32_16x16x32_bf16 v[104:107], a[228:231], v[184:187], v[104:107]\n"
      "ds_read_b64_tr_b16 a[216:217], v11 offset:45056\n"
      "ds_read_b64_tr_b16 a[218:219], v11 offset:45568\n"
      "v_mfma_f32_16x16x32_bf16 v[108:111], a[228:231], v[188:191], v[108:111]\n"
      "v_cvt_pk_bf16_f32 v206, v140, v141\n"
      "v_cvt_pk_bf16_f32 v207, v142, v143\n"
      "v_mfma_f32_4x4x4_16b_bf16 v[112:115], v[28:29], a[64:65], v[24:27]\n"
      "v_mfma_f32_4x4x4_16b_bf16 v[116:119], v[28:29], a[66:67], v[24:27]\n"
      "v_mfma_f32_4x4x4_16b_bf16 v[120:123], v[28:29], a[68:69], v[40:43]\n"
      "v_mfma_f32_4x4x4_16b_bf16 v[124:127], v[28:29], a[70:71], v[40:43]\n"
      "s_waitcnt vmcnt(10) lgkmcnt(6)\n"
      "s_barrier\n"
      "v_mfma_f32_16x16x32_bf16 v[80:83], a[232:235], v[192:195], v[80:83]\n"
      "ds_read_b64_tr_b16 a[220:221], v12 offset:45056\n"
      "ds_read_b64_tr_b16 a[222:223], v12 offset:45568\n"
      "v_mfma_f32_16x16x32_bf16 v[84:87], a[232:235], v[196:199], v[84:87]\n"
      "buffer_load_dwordx4 v19, s[24:27], s63 offen offset:2048 lds\n"
      "v_cvt_pk_bf16_f32 v208, v112, v113\n"
      "v_cvt_pk_bf16_f32 v209, v114, v115\n"
      "v_mfma_f32_16x16x32_bf16 v[88:91], a[236:239], v[192:195], v[88:91]\n"
      "ds_read_b64_tr_b16 a[224:225], v9 offset:49152\n"
      "ds_read_b64_tr_b16 a[226:227], v9 offset:49664\n"
      "v_mfma_f32_16x16x32_bf16 v[92:95], a[236:239], v[196:199], v[92:95]\n"
      "v_cvt_pk_bf16_f32 v210, v116, v117\n"
      "v_cvt_pk_bf16_f32 v211, v118, v119\n"
      "v_mfma_f32_16x16x32_bf16 v[96:99], a[240:243], v[192:195], v[96:99]\n"
      "ds_read_b64_tr_b16 a[228:229], v10 offset:49152\n"
      "ds_read_b64_tr_b16 a[230:231], v10 offset:49664\n"
      "v_mfma_f32_16x16x32_bf16 v[100:103], a[240:243], v[196:199], v[100:103]\n"
      "buffer_load_dwordx4 v18, s[24:27], s64 offen offset:3072 lds\n"
      "v_cvt_pk_bf16_f32 v212, v120, v121\n"
      "v_cvt_pk_bf16_f32 v213, v122, v123\n"
      "v_mfma_f32_16x16x32_bf16 v[104:107], a[244:247], v[192:195], v[104:107]\n"
      "ds_read_b64_tr_b16 a[232:233], v11 offset:49152\n"
      "ds_read_b64_tr_b16 a[234:235], v11 offset:49664\n"
      "v_mfma_f32_16x16x32_bf16 v[108:111], a[244:247], v[196:199], v[108:111]\n"
      "v_cvt_pk_bf16_f32 v214, v124, v125\n"
      "v_cvt_pk_bf16_f32 v215, v126, v127\n"
      "v_mfma_f32_4x4x4_16b_bf16 v[128:131], v[28:29], a[72:73], v[24:27]\n"
      "v_mfma_f32_4x4x4_16b_bf16 v[132:135], v[28:29], a[74:75], v[24:27]\n"
      "v_mfma_f32_4x4x4_16b_bf16 v[136:139], v[28:29], a[76:77], v[40:43]\n"
      "v_mfma_f32_4x4x4_16b_bf16 v[140:143], v[28:29], a[78:79], v[40:43]\n"
      "s_waitcnt lgkmcnt(6)\n"
      "v_mfma_f32_16x16x32_bf16 v[80:83], a[248:251], v[200:203], v[80:83]\n"
      "ds_read_b64_tr_b16 a[236:237], v12 offset:49152\n"
      "ds_read_b64_tr_b16 a[238:239], v12 offset:49664\n"
      "v_mfma_f32_16x16x32_bf16 v[84:87], a[248:251], v[204:207], v[84:87]\n"
      "s_add_u32 m0, s95, 32768\n"
      "v_add_u32 v19, s87, v3\n"
      "v_xor_b32 v18, 64, v19\n"
      "buffer_load_dwordx4 v19, s[24:27], 0 offen offset:0 lds\n"
      "v_cvt_pk_bf16_f32 v216, v128, v129\n"
      "v_cvt_pk_bf16_f32 v217, v130, v131\n"
      "v_mfma_f32_16x16x32_bf16 v[88:91], a[252:255], v[200:203], v[88:91]\n"
      "ds_read_b64_tr_b16 a[240:241], v9 offset:53248\n"
      "ds_read_b64_tr_b16 a[242:243], v9 offset:53760\n"
      "v_mfma_f32_16x16x32_bf16 v[92:95], a[252:255], v[204:207], v[92:95]\n"
      "v_cvt_pk_bf16_f32 v218, v132, v133\n"
      "v_cvt_pk_bf16_f32 v219, v134, v135\n"
      "v_mfma_f32_16x16x32_bf16 v[96:99], a[216:219], v[200:203], v[96:99]\n"
      "ds_read_b64_tr_b16 a[244:245], v10 offset:53248\n"
      "ds_read_b64_tr_b16 a[246:247], v10 offset:53760\n"
      "v_mfma_f32_16x16x32_bf16 v[100:103], a[216:219], v[204:207], v[100:103]\n"
      "buffer_load_dwordx4 v18, s[24:27], s62 offen offset:1024 lds\n"
      "v_cvt_pk_bf16_f32 v220, v136, v137\n"
      "v_cvt_pk_bf16_f32 v221, v138, v139\n"
      "v_mfma_f32_16x16x32_bf16 v[104:107], a[220:223], v[200:203], v[104:107]\n"
      "ds_read_b64_tr_b16 a[248:249], v11 offset:53248\n"
      "ds_read_b64_tr_b16 a[250:251], v11 offset:53760\n"
      "v_mfma_f32_16x16x32_bf16 v[108:111], a[220:223], v[204:207], v[108:111]\n"
      "v_cvt_pk_bf16_f32 v222, v140, v141\n"
      "v_cvt_pk_bf16_f32 v223, v142, v143\n"
      "v_mfma_f32_4x4x4_16b_bf16 v[112:115], v[28:29], a[80:81], v[24:27]\n"
      "v_mfma_f32_4x4x4_16b_bf16 v[116:119], v[28:29], a[82:83], v[24:27]\n"
      "v_mfma_f32_4x4x4_16b_bf16 v[120:123], v[28:29], a[84:85], v[40:43]\n"
      "v_mfma_f32_4x4x4_16b_bf16 v[124:127], v[28:29], a[86:87], v[40:43]\n"
      "s_waitcnt lgkmcnt(6)\n"
      "v_mfma_f32_16x16x32_bf16 v[80:83], a[224:227], v[208:211], v[80:83]\n"
      "ds_read_b64_tr_b16 a[252:253], v12 offset:53248\n"
      "ds_read_b64_tr_b16 a[254:255], v12 offset:53760\n"
      "v_mfma_f32_16x16x32_bf16 v[84:87], a[224:227], v[212:215], v[84:87]\n"
      "buffer_load_dwordx4 v19, s[24:27], s63 offen offset:2048 lds\n"
      "v_cvt_pk_bf16_f32 v224, v112, v113\n"
      "v_cvt_pk_bf16_f32 v225, v114, v115\n"
      "v_mfma_f32_16x16x32_bf16 v[88:91], a[228:231], v[208:211], v[88:91]\n"
      "ds_read_b64_tr_b16 a[216:217], v9 offset:57344\n"
      "ds_read_b64_tr_b16 a[218:219], v9 offset:57856\n"
      "v_mfma_f32_16x16x32_bf16 v[92:95], a[228:231], v[212:215], v[92:95]\n"
      "v_cvt_pk_bf16_f32 v226, v116, v117\n"
      "v_cvt_pk_bf16_f32 v227, v118, v119\n"
      "v_mfma_f32_16x16x32_bf16 v[96:99], a[232:235], v[208:211], v[96:99]\n"
      "ds_read_b64_tr_b16 a[220:221], v10 offset:57344\n"
      "ds_read_b64_tr_b16 a[222:223], v10 offset:57856\n"
      "v_mfma_f32_16x16x32_bf16 v[100:103], a[232:235], v[212:215], v[100:103]\n"
      "buffer_load_dwordx4 v18, s[24:27], s64 offen offset:3072 lds\n"
      "v_cvt_pk_bf16_f32 v228, v120, v121\n"
      "v_cvt_pk_bf16_f32 v229, v122, v123\n"
      "v_mfma_f32_16x16x32_bf16 v[104:107], a[236:239], v[208:211], v[104:107]\n"
      "ds_read_b64_tr_b16 a[224:225], v11 offset:57344\n"
      "ds_read_b64_tr_b16 a[226:227], v11 offset:57856\n"
      "v_mfma_f32_16x16x32_bf16 v[108:111], a[236:239], v[212:215], v[108:111]\n"
      "v_cvt_pk_bf16_f32 v230, v124, v125\n"
      "v_cvt_pk_bf16_f32 v231, v126, v127\n"
      "v_mfma_f32_4x4x4_16b_bf16 v[128:131], v[28:29], a[88:89], v[24:27]\n"
      "v_mfma_f32_4x4x4_16b_bf16 v[132:135], v[28:29], a[90:91], v[24:27]\n"
      "v_mfma_f32_4x4x4_16b_bf16 v[136:139], v[28:29], a[92:93], v[40:43]\n"
      "v_mfma_f32_4x4x4_16b_bf16 v[140:143], v[28:29], a[94:95], v[40:43]\n"
      "s_waitcnt lgkmcnt(6)\n"
      "v_mfma_f32_16x16x32_bf16 v[80:83], a[240:243], v[216:219], v[80:83]\n"
      "ds_read_b64_tr_b16 a[228:229], v12 offset:57344\n"
      "ds_read_b64_tr_b16 a[230:231], v12 offset:57856\n"
      "v_mfma_f32_16x16x32_bf16 v[84:87], a[240:243], v[220:223], v[84:87]\n"
      "s_lshr_b32 s35, s95, 1\n"
      "s_add_u32 m0, s35, 98304\n"
      "v_add_u32 v19, s99, v3\n"
      "v_xor_b32 v18, 64, v19\n"
      "buffer_load_dwordx4 v19, s[24:27], 0 offen offset:0 lds\n"
      "v_cvt_pk_bf16_f32 v232, v128, v129\n"
      "v_cvt_pk_bf16_f32 v233, v130, v131\n"
      "v_mfma_f32_16x16x32_bf16 v[88:91], a[244:247], v[216:219], v[88:91]\n"
      "ds_read_b64_tr_b16 a[232:233], v9 offset:61440\n"
      "ds_read_b64_tr_b16 a[234:235], v9 offset:61952\n"
      "v_mfma_f32_16x16x32_bf16 v[92:95], a[244:247], v[220:223], v[92:95]\n"
      "v_cvt_pk_bf16_f32 v234, v132, v133\n"
      "v_cvt_pk_bf16_f32 v235, v134, v135\n"
      "v_mfma_f32_16x16x32_bf16 v[96:99], a[248:251], v[216:219], v[96:99]\n"
      "ds_read_b64_tr_b16 a[236:237], v10 offset:61440\n"
      "ds_read_b64_tr_b16 a[238:239], v10 offset:61952\n"
      "v_mfma_f32_16x16x32_bf16 v[100:103], a[248:251], v[220:223], v[100:103]\n"
      "buffer_load_dwordx4 v18, s[24:27], s62 offen offset:1024 lds\n"
      "v_cvt_pk_bf16_f32 v236, v136, v137\n"
      "v_cvt_pk_bf16_f32 v237, v138, v139\n"
      "v_mfma_f32_16x16x32_bf16 v[104:107], a[252:255], v[216:219], v[104:107]\n"
      "ds_read_b64_tr_b16 a[240:241], v11 offset:61440\n"
      "ds_read_b64_tr_b16 a[242:243], v11 offset:61952\n"
      "v_mfma_f32_16x16x32_bf16 v[108:111], a[252:255], v[220:223], v[108:111]\n"
      "v_cvt_pk_bf16_f32 v238, v140, v141\n"
      "v_cvt_pk_bf16_f32 v239, v142, v143\n"
      "v_mfma_f32_4x4x4_16b_bf16 v[112:115], v[28:29], a[96:97], v[24:27]\n"
      "v_mfma_f32_4x4x4_16b_bf16 v[116:119], v[28:29], a[98:99], v[24:27]\n"
      "v_mfma_f32_4x4x4_16b_bf16 v[120:123], v[28:29], a[100:101], v[40:43]\n"
      "v_mfma_f32_4x4x4_16b_bf16 v[124:127], v[28:29], a[102:103], v[40:43]\n"
      "s_waitcnt vmcnt(16) lgkmcnt(6)\n"
      "s_barrier\n"
      "v_mfma_f32_16x16x32_bf16 v[80:83], a[216:219], v[224:227], v[80:83]\n"
      "ds_read_b64_tr_b16 a[244:245], v12 offset:61440\n"
      "ds_read_b64_tr_b16 a[246:247], v12 offset:61952\n"
      "v_mfma_f32_16x16x32_bf16 v[84:87], a[216:219], v[228:231], v[84:87]\n"
      "v_cvt_pk_bf16_f32 v240, v112, v113\n"
      "v_cvt_pk_bf16_f32 v241, v114, v115\n"
      "v_mfma_f32_16x16x32_bf16 v[88:91], a[220:223], v[224:227], v[88:91]\n"
      ".LPAD10:\n"
      "s_cbranch_execz .LRET\n"
      "ds_read_b64_tr_b16 a[248:249], v9 offset:16384\n"
      "ds_read_b64_tr_b16 a[250:251], v9 offset:16896\n"
      "v_mfma_f32_16x16x32_bf16 v[92:95], a[220:223], v[228:231], v[92:95]\n"
      "s_add_u32 m0, s95, 49152\n"
      "v_add_u32 v3, 128, v3\n"
      "v_add_u32 v19, s34, v3\n"
      "v_xor_b32 v18, 64, v19\n"
      "buffer_load_dwordx4 v19, s[24:27], 0 offen offset:0 lds\n"
      "v_cvt_pk_bf16_f32 v242, v116, v117\n"
      "v_cvt_pk_bf16_f32 v243, v118, v119\n"
      "v_mfma_f32_16x16x32_bf16 v[96:99], a[224:227], v[224:227], v[96:99]\n"
      "ds_read_b64_tr_b16 a[252:253], v10 offset:16384\n"
      "ds_read_b64_tr_b16 a[254:255], v10 offset:16896\n"
      "v_mfma_f32_16x16x32_bf16 v[100:103], a[224:227], v[228:231], v[100:103]\n"
      "v_cvt_pk_bf16_f32 v244, v120, v121\n"
      "v_cvt_pk_bf16_f32 v245, v122, v123\n"
      "v_mfma_f32_16x16x32_bf16 v[104:107], a[228:231], v[224:227], v[104:107]\n"
      "ds_read_b64_tr_b16 a[216:217], v11 offset:16384\n"
      "ds_read_b64_tr_b16 a[218:219], v11 offset:16896\n"
      "v_mfma_f32_16x16x32_bf16 v[108:111], a[228:231], v[228:231], v[108:111]\n"
      "buffer_load_dwordx4 v18, s[24:27], s62 offen offset:1024 lds\n"
      "v_cvt_pk_bf16_f32 v246, v124, v125\n"
      "v_cvt_pk_bf16_f32 v247, v126, v127\n"
      "v_mfma_f32_4x4x4_16b_bf16 v[128:131], v[28:29], a[104:105], v[24:27]\n"
      "v_mfma_f32_4x4x4_16b_bf16 v[132:135], v[28:29], a[106:107], v[24:27]\n"
      "v_mfma_f32_4x4x4_16b_bf16 v[136:139], v[28:29], a[108:109], v[40:43]\n"
      "v_mfma_f32_4x4x4_16b_bf16 v[140:143], v[28:29], a[110:111], v[40:43]\n"
      "s_waitcnt lgkmcnt(6)\n"
      "v_mfma_f32_16x16x32_bf16 v[80:83], a[232:235], v[232:235], v[80:83]\n"
      "ds_read_b64_tr_b16 a[220:221], v12 offset:16384\n"
      "ds_read_b64_tr_b16 a[222:223], v12 offset:16896\n"
      "v_mfma_f32_16x16x32_bf16 v[84:87], a[232:235], v[236:239], v[84:87]\n"
      "v_cvt_pk_bf16_f32 v248, v128, v129\n"
      "v_cvt_pk_bf16_f32 v249, v130, v131\n"
      "v_mfma_f32_16x16x32_bf16 v[88:91], a[236:239], v[232:235], v[88:91]\n"
      "ds_read_b64_tr_b16 a[224:225], v9 offset:20480\n"
      "ds_read_b64_tr_b16 a[226:227], v9 offset:20992\n"
      "v_mfma_f32_16x16x32_bf16 v[92:95], a[236:239], v[236:239], v[92:95]\n"
      "buffer_load_dwordx4 v19, s[24:27], s63 offen offset:2048 lds\n"
      "v_cvt_pk_bf16_f32 v250, v132, v133\n"
      "v_cvt_pk_bf16_f32 v251, v134, v135\n"
      "v_mfma_f32_16x16x32_bf16 v[96:99], a[240:243], v[232:235], v[96:99]\n"
      "ds_read_b64_tr_b16 a[228:229], v10 offset:20480\n"
      "ds_read_b64_tr_b16 a[230:231], v10 offset:20992\n"
      "v_mfma_f32_16x16x32_bf16 v[100:103], a[240:243], v[236:239], v[100:103]\n"
      "v_cvt_pk_bf16_f32 v252, v136, v137\n"
      "v_cvt_pk_bf16_f32 v253, v138, v139\n"
      "v_mfma_f32_16x16x32_bf16 v[104:107], a[244:247], v[232:235], v[104:107]\n"
      "ds_read_b64_tr_b16 a[232:233], v11 offset:20480\n"
      "ds_read_b64_tr_b16 a[234:235], v11 offset:20992\n"
      "v_mfma_f32_16x16x32_bf16 v[108:111], a[244:247], v[236:239], v[108:111]\n"
      "buffer_load_dwordx4 v18, s[24:27], s64 offen offset:3072 lds\n"
      "v_cvt_pk_bf16_f32 v254, v140, v141\n"
      "v_cvt_pk_bf16_f32 v255, v142, v143\n"
      "s_waitcnt vmcnt(10) lgkmcnt(6)\n"
      "s_barrier\n"
      "v_mfma_f32_16x16x32_bf16 v[80:83], a[248:251], v[240:243], v[80:83]\n"
      "ds_read_b64_tr_b16 a[236:237], v12 offset:20480\n"
      "ds_read_b64_tr_b16 a[238:239], v12 offset:20992\n"
      "v_mfma_f32_16x16x32_bf16 v[84:87], a[248:251], v[244:247], v[84:87]\n"
      "v_xor_b32 v9, 0x0, v6\n"
      "v_mfma_f32_16x16x32_bf16 v[88:91], a[252:255], v[240:243], v[88:91]\n"
      "ds_read_b64_tr_b16 a[240:241], v9 offset:0\n"
      "ds_read_b64_tr_b16 a[242:243], v9 offset:512\n"
      "v_mfma_f32_16x16x32_bf16 v[92:95], a[252:255], v[244:247], v[92:95]\n"
      "s_add_u32 m0, s95, 65536\n"
      "v_add_u32 v19, s86, v3\n"
      "v_xor_b32 v18, 64, v19\n"
      "buffer_load_dwordx4 v19, s[24:27], 0 offen offset:0 lds\n"
      "v_xor_b32 v10, 0x20, v6\n"
      "v_mfma_f32_16x16x32_bf16 v[96:99], a[216:219], v[240:243], v[96:99]\n"
      "ds_read_b64_tr_b16 a[244:245], v10 offset:0\n"
      "ds_read_b64_tr_b16 a[246:247], v10 offset:512\n"
      "v_mfma_f32_16x16x32_bf16 v[100:103], a[216:219], v[244:247], v[100:103]\n"
      "v_xor_b32 v11, 0x40, v6\n"
      "v_mfma_f32_16x16x32_bf16 v[104:107], a[220:223], v[240:243], v[104:107]\n"
      "ds_read_b64_tr_b16 a[248:249], v11 offset:0\n"
      "ds_read_b64_tr_b16 a[250:251], v11 offset:512\n"
      "v_mfma_f32_16x16x32_bf16 v[108:111], a[220:223], v[244:247], v[108:111]\n"
      "buffer_load_dwordx4 v18, s[24:27], s62 offen offset:1024 lds\n"
      "v_xor_b32 v12, 0x60, v6\n"
      "s_waitcnt lgkmcnt(6)\n"
      "v_mfma_f32_16x16x32_bf16 v[80:83], a[224:227], v[248:251], v[80:83]\n"
      "ds_read_b64_tr_b16 a[252:253], v12 offset:0\n"
      "ds_read_b64_tr_b16 a[254:255], v12 offset:512\n"
      "v_mfma_f32_16x16x32_bf16 v[84:87], a[224:227], v[252:255], v[84:87]\n"
      "v_readfirstlane_b32 s96, v16\n"
      "v_readfirstlane_b32 s97, v17\n"
      "v_mfma_f32_16x16x32_bf16 v[88:91], a[228:231], v[248:251], v[88:91]\n"
      "ds_read_b64_tr_b16 a[216:217], v9 offset:4096\n"
      "ds_read_b64_tr_b16 a[218:219], v9 offset:4608\n"
      "v_mfma_f32_16x16x32_bf16 v[92:95], a[228:231], v[252:255], v[92:95]\n"
      "buffer_load_dwordx4 v19, s[24:27], s63 offen offset:2048 lds\n"
      "s_add_u32 s96, s96, 0x7f4a7c15\n"
      "s_addc_u32 s97, s97, 0x9e3779b9\n"
      "v_mfma_f32_16x16x32_bf16 v[96:99], a[232:235], v[248:251], v[96:99]\n"
      "ds_read_b64_tr_b16 a[220:221], v10 offset:4096\n"
      "ds_read_b64_tr_b16 a[222:223], v10 offset:4608\n"
      "v_mfma_f32_16x16x32_bf16 v[100:103], a[232:235], v[252:255], v[100:103]\n"
      "v_mov_b32 v16, s96\n"
      "v_mov_b32 v17, s97\n"
      "v_mfma_f32_16x16x32_bf16 v[104:107], a[236:239], v[248:251], v[104:107]\n"
      "ds_read_b64_tr_b16 a[224:225], v11 offset:4096\n"
      "ds_read_b64_tr_b16 a[226:227], v11 offset:4608\n"
      "v_mfma_f32_16x16x32_bf16 v[108:111], a[236:239], v[252:255], v[108:111]\n"
      "buffer_load_dwordx4 v18, s[24:27], s64 offen offset:3072 lds\n"
      "s_waitcnt lgkmcnt(6)\n"
      "v_mfma_f32_16x16x32_bf16 v[112:115], a[240:243], v[144:147], 0\n"
      "ds_read_b64_tr_b16 a[228:229], v12 offset:4096\n"
      "ds_read_b64_tr_b16 a[230:231], v12 offset:4608\n"
      "v_mfma_f32_16x16x32_bf16 v[116:119], a[240:243], v[148:151], 0\n"
      "v_mfma_f32_16x16x32_bf16 v[120:123], a[244:247], v[144:147], 0\n"
      "ds_read_b64_tr_b16 a[232:233], v9 offset:8192\n"
      "ds_read_b64_tr_b16 a[234:235], v9 offset:8704\n"
      "v_mfma_f32_16x16x32_bf16 v[124:127], a[244:247], v[148:151], 0\n"
      "s_add_u32 m0, s95, 81920\n"
      "v_add_u32 v19, s87, v3\n"
      "v_xor_b32 v18, 64, v19\n"
      "buffer_load_dwordx4 v19, s[24:27], 0 offen offset:0 lds\n"
      "v_mfma_f32_16x16x32_bf16 v[128:131], a[248:251], v[144:147], 0\n"
      "ds_read_b64_tr_b16 a[236:237], v10 offset:8192\n"
      "ds_read_b64_tr_b16 a[238:239], v10 offset:8704\n"
      "v_mfma_f32_16x16x32_bf16 v[132:135], a[248:251], v[148:151], 0\n"
      "v_mfma_f32_16x16x32_bf16 v[136:139], a[252:255], v[144:147], 0\n"
      "ds_read_b64_tr_b16 a[240:241], v11 offset:8192\n"
      "ds_read_b64_tr_b16 a[242:243], v11 offset:8704\n"
      "v_mfma_f32_16x16x32_bf16 v[140:143], a[252:255], v[148:151], 0\n"
      "buffer_load_dwordx4 v18, s[24:27], s62 offen offset:1024 lds\n"
      "s_waitcnt lgkmcnt(6)\n"
      "v_mfma_f32_16x16x32_bf16 v[112:115], a[216:219], v[152:155], v[112:115]\n"
      "ds_read_b64_tr_b16 a[244:245], v12 offset:8192\n"
      "ds_read_b64_tr_b16 a[246:247], v12 offset:8704\n"
      "v_mfma_f32_16x16x32_bf16 v[116:119], a[216:219], v[156:159], v[116:119]\n"
      "v_cvt_pk_bf16_f32 v32, v80, v81\n"
      "v_cvt_pk_bf16_f32 v33, v82, v83\n"
      "v_mfma_f32_16x16x32_bf16 v[120:123], a[220:223], v[152:155], v[120:123]\n"
      "ds_read_b64_tr_b16 a[248:249], v9 offset:12288\n"
      "ds_read_b64_tr_b16 a[250:251], v9 offset:12800\n"
      "v_mfma_f32_16x16x32_bf16 v[124:127], a[220:223], v[156:159], v[124:127]\n"
      "buffer_load_dwordx4 v19, s[24:27], s63 offen offset:2048 lds\n"
      "v_mfma_f32_16x16x32_bf16 v[128:131], a[224:227], v[152:155], v[128:131]\n"
      "ds_read_b64_tr_b16 a[252:253], v10 offset:12288\n"
      "ds_read_b64_tr_b16 a[254:255], v10 offset:12800\n"
      "v_mfma_f32_16x16x32_bf16 v[132:135], a[224:227], v[156:159], v[132:135]\n"
      "v_cvt_pk_bf16_f32 v34, v84, v85\n"
      "v_cvt_pk_bf16_f32 v35, v86, v87\n"
      "v_cvt_pk_bf16_f32 v36, v88, v89\n"
      "v_mfma_f32_16x16x32_bf16 v[136:139], a[228:231], v[152:155], v[136:139]\n"
      "ds_read_b64_tr_b16 a[216:217], v11 offset:12288\n"
      "ds_read_b64_tr_b16 a[218:219], v11 offset:12800\n"
      "v_mfma_f32_16x16x32_bf16 v[140:143], a[228:231], v[156:159], v[140:143]\n"
      "buffer_load_dwordx4 v18, s[24:27], s64 offen offset:3072 lds\n"
      "s_waitcnt lgkmcnt(6)\n"
      "v_mfma_f32_16x16x32_bf16 v[112:115], a[232:235], v[160:163], v[112:115]\n"
      "ds_read_b64_tr_b16 a[220:221], v12 offset:12288\n"
      "ds_read_b64_tr_b16 a[222:223], v12 offset:12800\n"
      "v_mfma_f32_16x16x32_bf16 v[116:119], a[232:235], v[164:167], v[116:119]\n"
      "v_cvt_pk_bf16_f32 v37, v90, v91\n"
      "v_cvt_pk_bf16_f32 v38, v92, v93\n"
      "v_mfma_f32_16x16x32_bf16 v[120:123], a[236:239], v[160:163], v[120:123]\n"
      "ds_read_b64_tr_b16 a[224:225], v9 offset:16384\n"
      "ds_read_b64_tr_b16 a[226:227], v9 offset:16896\n"
      "v_mfma_f32_16x16x32_bf16 v[124:127], a[236:239], v[164:167], v[124:127]\n"
      "s_lshr_b32 s35, s95, 1\n"
      "s_add_u32 m0, s35, 106496\n"
      "v_add_u32 v19, s99, v3\n"
      "v_xor_b32 v18, 64, v19\n"
      "buffer_load_dwordx4 v19, s[24:27], 0 offen offset:0 lds\n"
      "v_mfma_f32_16x16x32_bf16 v[128:131], a[240:243], v[160:163], v[128:131]\n"
      "ds_read_b64_tr_b16 a[228:229], v10 offset:16384\n"
      "ds_read_b64_tr_b16 a[230:231], v10 offset:16896\n"
      "v_mfma_f32_16x16x32_bf16 v[132:135], a[240:243], v[164:167], v[132:135]\n"
      "v_cvt_pk_bf16_f32 v39, v94, v95\n"
      "v_permlane16_swap_b32 v32, v34\n"
      "v_mfma_f32_16x16x32_bf16 v[136:139], a[244:247], v[160:163], v[136:139]\n"
      "ds_read_b64_tr_b16 a[232:233], v11 offset:16384\n"
      "ds_read_b64_tr_b16 a[234:235], v11 offset:16896\n"
      "v_mfma_f32_16x16x32_bf16 v[140:143], a[244:247], v[164:167], v[140:143]\n"
      "buffer_load_dwordx4 v18, s[24:27], s62 offen offset:1024 lds\n"
      "v_permlane16_swap_b32 v33, v35\n"
      "s_waitcnt lgkmcnt(6)\n"
      "v_mfma_f32_16x16x32_bf16 v[112:115], a[248:251], v[168:171], v[112:115]\n"
      "ds_read_b64_tr_b16 a[236:237], v12 offset:16384\n"
      "ds_read_b64_tr_b16 a[238:239], v12 offset:16896\n"
      "v_mfma_f32_16x16x32_bf16 v[116:119], a[248:251], v[172:175], v[116:119]\n"
      "v_permlane16_swap_b32 v36, v38\n"
      "v_mfma_f32_16x16x32_bf16 v[120:123], a[252:255], v[168:171], v[120:123]\n"
      "ds_read_b64_tr_b16 a[240:241], v9 offset:20480\n"
      "ds_read_b64_tr_b16 a[242:243], v9 offset:20992\n"
      "v_mfma_f32_16x16x32_bf16 v[124:127], a[252:255], v[172:175], v[124:127]\n"
      "v_permlane16_swap_b32 v37, v39\n"
      "v_cvt_pk_bf16_f32 v40, v96, v97\n"
      "v_cvt_pk_bf16_f32 v41, v98, v99\n"
      "v_mfma_f32_16x16x32_bf16 v[128:131], a[216:219], v[168:171], v[128:131]\n"
      "ds_read_b64_tr_b16 a[244:245], v10 offset:20480\n"
      "ds_read_b64_tr_b16 a[246:247], v10 offset:20992\n"
      "v_mfma_f32_16x16x32_bf16 v[132:135], a[216:219], v[172:175], v[132:135]\n"
      "v_cvt_pk_bf16_f32 v42, v100, v101\n"
      "v_cvt_pk_bf16_f32 v43, v102, v103\n"
      "v_mfma_f32_16x16x32_bf16 v[136:139], a[220:223], v[168:171], v[136:139]\n"
      "ds_read_b64_tr_b16 a[248:249], v11 offset:20480\n"
      "ds_read_b64_tr_b16 a[250:251], v11 offset:20992\n"
      "v_mfma_f32_16x16x32_bf16 v[140:143], a[220:223], v[172:175], v[140:143]\n"
      "v_cvt_pk_bf16_f32 v44, v104, v105\n"
      "v_cvt_pk_bf16_f32 v45, v106, v107\n"
      "v_cvt_pk_bf16_f32 v46, v108, v109\n"
      "s_waitcnt vmcnt(14) lgkmcnt(6)\n"
      "s_barrier\n"
      "v_mfma_f32_16x16x32_bf16 v[112:115], a[224:227], v[176:179], v[112:115]\n"
      "ds_read_b64_tr_b16 a[252:253], v12 offset:20480\n"
      "ds_read_b64_tr_b16 a[254:255], v12 offset:20992\n"
      "v_mfma_f32_16x16x32_bf16 v[116:119], a[224:227], v[180:183], v[116:119]\n"
      "s_add_u32 m0, s95, 114688\n"
      "v_add_u32 v3, 128, v3\n"
      "v_add_u32 v19, s34, v3\n"
      "v_xor_b32 v18, 64, v19\n"
      "buffer_load_dwordx4 v19, s[24:27], 0 offen offset:0 lds\n"
      "v_mfma_f32_16x16x32_bf16 v[120:123], a[228:231], v[176:179], v[120:123]\n"
      "ds_read_b64_tr_b16 a[216:217], v9 offset:24576\n"
      "ds_read_b64_tr_b16 a[218:219], v9 offset:25088\n"
      "v_mfma_f32_16x16x32_bf16 v[124:127], a[228:231], v[180:183], v[124:127]\n"
      "v_cvt_pk_bf16_f32 v47, v110, v111\n"
      "v_permlane16_swap_b32 v40, v42\n"
      "v_mfma_f32_16x16x32_bf16 v[128:131], a[232:235], v[176:179], v[128:131]\n"
      "ds_read_b64_tr_b16 a[220:221], v10 offset:24576\n"
      "ds_read_b64_tr_b16 a[222:223], v10 offset:25088\n"
      "v_mfma_f32_16x16x32_bf16 v[132:135], a[232:235], v[180:183], v[132:135]\n"
      "buffer_load_dwordx4 v18, s[24:27], s62 offen offset:1024 lds\n"
      "v_permlane16_swap_b32 v41, v43\n"
      "v_mfma_f32_16x16x32_bf16 v[136:139], a[236:239], v[176:179], v[136:139]\n"
      "ds_read_b64_tr_b16 a[224:225], v11 offset:24576\n"
      "ds_read_b64_tr_b16 a[226:227], v11 offset:25088\n"
      "v_mfma_f32_16x16x32_bf16 v[140:143], a[236:239], v[180:183], v[140:143]\n"
      "v_permlane16_swap_b32 v44, v46\n"
      "v_permlane16_swap_b32 v45, v47\n"
      "s_waitcnt lgkmcnt(6)\n"
      "v_mfma_f32_16x16x32_bf16 v[112:115], a[240:243], v[184:187], v[112:115]\n"
      "ds_read_b64_tr_b16 a[228:229], v12 offset:24576\n"
      "ds_read_b64_tr_b16 a[230:231], v12 offset:25088\n"
      "v_mfma_f32_16x16x32_bf16 v[116:119], a[240:243], v[188:191], v[116:119]\n"
      "buffer_load_dwordx4 v19, s[24:27], s63 offen offset:2048 lds\n"
      "v_mfma_f32_16x16x32_bf16 v[120:123], a[244:247], v[184:187], v[120:123]\n"
      "ds_read_b64_tr_b16 a[232:233], v9 offset:28672\n"
      "ds_read_b64_tr_b16 a[234:235], v9 offset:29184\n"
      "v_mfma_f32_16x16x32_bf16 v[124:127], a[244:247], v[188:191], v[124:127]\n"
      "v_mfma_f32_16x16x32_bf16 v[128:131], a[248:251], v[184:187], v[128:131]\n"
      "ds_read_b64_tr_b16 a[236:237], v10 offset:28672\n"
      "ds_read_b64_tr_b16 a[238:239], v10 offset:29184\n"
      "v_mfma_f32_16x16x32_bf16 v[132:135], a[248:251], v[188:191], v[132:135]\n"
      "buffer_load_dwordx4 v18, s[24:27], s64 offen offset:3072 lds\n"
      "v_mfma_f32_16x16x32_bf16 v[136:139], a[252:255], v[184:187], v[136:139]\n"
      "ds_read_b64_tr_b16 a[240:241], v11 offset:28672\n"
      "ds_read_b64_tr_b16 a[242:243], v11 offset:29184\n"
      "v_mfma_f32_16x16x32_bf16 v[140:143], a[252:255], v[188:191], v[140:143]\n"
      "s_waitcnt lgkmcnt(6)\n"
      "v_mfma_f32_16x16x32_bf16 v[112:115], a[216:219], v[192:195], v[112:115]\n"
      "ds_read_b64_tr_b16 a[244:245], v12 offset:28672\n"
      "ds_read_b64_tr_b16 a[246:247], v12 offset:29184\n"
      "v_mfma_f32_16x16x32_bf16 v[116:119], a[216:219], v[196:199], v[116:119]\n"
      "s_add_u32 m0, s95, 131072\n"
      "v_add_u32 v19, s86, v3\n"
      "v_xor_b32 v18, 64, v19\n"
      "buffer_load_dwordx4 v19, s[24:27], 0 offen offset:0 lds\n"
      "v_mfma_f32_16x16x32_bf16 v[120:123], a[220:223], v[192:195], v[120:123]\n"
      "ds_read_b64_tr_b16 a[248:249], v9 offset:32768\n"
      "ds_read_b64_tr_b16 a[250:251], v9 offset:33280\n"
      "v_mfma_f32_16x16x32_bf16 v[124:127], a[220:223], v[196:199], v[124:127]\n"
      "v_mfma_f32_16x16x32_bf16 v[128:131], a[224:227], v[192:195], v[128:131]\n"
      "ds_read_b64_tr_b16 a[252:253], v10 offset:32768\n"
      "ds_read_b64_tr_b16 a[254:255], v10 offset:33280\n"
      "v_mfma_f32_16x16x32_bf16 v[132:135], a[224:227], v[196:199], v[132:135]\n"
      "buffer_load_dwordx4 v18, s[24:27], s62 offen offset:1024 lds\n"
      "v_mfma_f32_16x16x32_bf16 v[136:139], a[228:231], v[192:195], v[136:139]\n"
      "ds_read_b64_tr_b16 a[216:217], v11 offset:32768\n"
      "ds_read_b64_tr_b16 a[218:219], v11 offset:33280\n"
      "v_mfma_f32_16x16x32_bf16 v[140:143], a[228:231], v[196:199], v[140:143]\n"
      "s_waitcnt lgkmcnt(6)\n"
      "v_mfma_f32_16x16x32_bf16 v[112:115], a[232:235], v[200:203], v[112:115]\n"
      "ds_read_b64_tr_b16 a[220:221], v12 offset:32768\n"
      "ds_read_b64_tr_b16 a[222:223], v12 offset:33280\n"
      "v_mfma_f32_16x16x32_bf16 v[116:119], a[232:235], v[204:207], v[116:119]\n"
      "buffer_load_dwordx4 v19, s[24:27], s63 offen offset:2048 lds\n"
      "v_mfma_f32_16x16x32_bf16 v[120:123], a[236:239], v[200:203], v[120:123]\n"
      "ds_read_b64_tr_b16 a[224:225], v9 offset:36864\n"
      "ds_read_b64_tr_b16 a[226:227], v9 offset:37376\n"
      "v_mfma_f32_16x16x32_bf16 v[124:127], a[236:239], v[204:207], v[124:127]\n"
      "v_mfma_f32_16x16x32_bf16 v[128:131], a[240:243], v[200:203], v[128:131]\n"
      "ds_read_b64_tr_b16 a[228:229], v10 offset:36864\n"
      "ds_read_b64_tr_b16 a[230:231], v10 offset:37376\n"
      "v_mfma_f32_16x16x32_bf16 v[132:135], a[240:243], v[204:207], v[132:135]\n"
      "buffer_load_dwordx4 v18, s[24:27], s64 offen offset:3072 lds\n"
      "v_mfma_f32_16x16x32_bf16 v[136:139], a[244:247], v[200:203], v[136:139]\n"
      "ds_read_b64_tr_b16 a[232:233], v11 offset:36864\n"
      "ds_read_b64_tr_b16 a[234:235], v11 offset:37376\n"
      "v_mfma_f32_16x16x32_bf16 v[140:143], a[244:247], v[204:207], v[140:143]\n"
      "s_waitcnt lgkmcnt(6)\n"
      "v_mfma_f32_16x16x32_bf16 v[112:115], a[248:251], v[208:211], v[112:115]\n"
      "ds_read_b64_tr_b16 a[236:237], v12 offset:36864\n"
      "ds_read_b64_tr_b16 a[238:239], v12 offset:37376\n"
      "v_mfma_f32_16x16x32_bf16 v[116:119], a[248:251], v[212:215], v[116:119]\n"
      "s_add_u32 m0, s95, 147456\n"
      "v_add_u32 v19, s87, v3\n"
      "v_xor_b32 v18, 64, v19\n"
      "buffer_load_dwordx4 v19, s[24:27], 0 offen offset:0 lds\n"
      "v_mfma_f32_16x16x32_bf16 v[120:123], a[252:255], v[208:211], v[120:123]\n"
      "ds_read_b64_tr_b16 a[240:241], v9 offset:40960\n"
      "ds_read_b64_tr_b16 a[242:243], v9 offset:41472\n"
      "v_mfma_f32_16x16x32_bf16 v[124:127], a[252:255], v[212:215], v[124:127]\n"
      "v_mfma_f32_16x16x32_bf16 v[128:131], a[216:219], v[208:211], v[128:131]\n"
      "ds_read_b64_tr_b16 a[244:245], v10 offset:40960\n"
      "ds_read_b64_tr_b16 a[246:247], v10 offset:41472\n"
      "v_mfma_f32_16x16x32_bf16 v[132:135], a[216:219], v[212:215], v[132:135]\n"
      "buffer_load_dwordx4 v18, s[24:27], s62 offen offset:1024 lds\n"
      "v_mfma_f32_16x16x32_bf16 v[136:139], a[220:223], v[208:211], v[136:139]\n"
      "ds_read_b64_tr_b16 a[248:249], v11 offset:40960\n"
      "ds_read_b64_tr_b16 a[250:251], v11 offset:41472\n"
      "v_mfma_f32_16x16x32_bf16 v[140:143], a[220:223], v[212:215], v[140:143]\n"
      "s_waitcnt lgkmcnt(6)\n"
      "v_mfma_f32_16x16x32_bf16 v[112:115], a[224:227], v[216:219], v[112:115]\n"
      "ds_read_b64_tr_b16 a[252:253], v12 offset:40960\n"
      "ds_read_b64_tr_b16 a[254:255], v12 offset:41472\n"
      "v_mfma_f32_16x16x32_bf16 v[116:119], a[224:227], v[220:223], v[116:119]\n"
      "buffer_load_dwordx4 v19, s[24:27], s63 offen offset:2048 lds\n"
      "v_mfma_f32_16x16x32_bf16 v[120:123], a[228:231], v[216:219], v[120:123]\n"
      "ds_read_b64_tr_b16 a[216:217], v9 offset:45056\n"
      "ds_read_b64_tr_b16 a[218:219], v9 offset:45568\n"
      "v_mfma_f32_16x16x32_bf16 v[124:127], a[228:231], v[220:223], v[124:127]\n"
      "v_mfma_f32_16x16x32_bf16 v[128:131], a[232:235], v[216:219], v[128:131]\n"
      "ds_read_b64_tr_b16 a[220:221], v10 offset:45056\n"
      "ds_read_b64_tr_b16 a[222:223], v10 offset:45568\n"
      "v_mfma_f32_16x16x32_bf16 v[132:135], a[232:235], v[220:223], v[132:135]\n"
      "buffer_load_dwordx4 v18, s[24:27], s64 offen offset:3072 lds\n"
      "v_mfma_f32_16x16x32_bf16 v[136:139], a[236:239], v[216:219], v[136:139]\n"
      "ds_read_b64_tr_b16 a[224:225], v11 offset:45056\n"
      "ds_read_b64_tr_b16 a[226:227], v11 offset:45568\n"
      "v_mfma_f32_16x16x32_bf16 v[140:143], a[236:239], v[220:223], v[140:143]\n"
      "s_waitcnt lgkmcnt(6)\n"
      "v_mfma_f32_16x16x32_bf16 v[112:115], a[240:243], v[224:227], v[112:115]\n"
      "ds_read_b64_tr_b16 a[228:229], v12 offset:45056\n"
      "ds_read_b64_tr_b16 a[230:231], v12 offset:45568\n"
      "v_mfma_f32_16x16x32_bf16 v[116:119], a[240:243], v[228:231], v[116:119]\n"
      "s_lshr_b32 s35, s95, 1\n"
      "s_add_u32 m0, s35, 0\n"
      "v_add_u32 v19, s99, v3\n"
      "v_xor_b32 v18, 64, v19\n"
      "buffer_load_dwordx4 v19, s[24:27], 0 offen offset:0 lds\n"
      "v_xor_b32 v9, 0xc000, v6\n"
      "v_mfma_f32_16x16x32_bf16 v[120:123], a[244:247], v[224:227], v[120:123]\n"
      "ds_read_b64_tr_b16 a[232:233], v9 offset:49152\n"
      "ds_read_b64_tr_b16 a[234:235], v9 offset:49664\n"
      "v_mfma_f32_16x16x32_bf16 v[124:127], a[244:247], v[228:231], v[124:127]\n"
      "v_xor_b32 v10, 0xc020, v6\n"
      "v_mfma_f32_16x16x32_bf16 v[128:131], a[248:251], v[224:227], v[128:131]\n"
      "ds_read_b64_tr_b16 a[236:237], v10 offset:49152\n"
      "ds_read_b64_tr_b16 a[238:239], v10 offset:49664\n"
      "v_mfma_f32_16x16x32_bf16 v[132:135], a[248:251], v[228:231], v[132:135]\n"
      "buffer_load_dwordx4 v18, s[24:27], s62 offen offset:1024 lds\n"
      "v_xor_b32 v11, 0xc040, v6\n"
      "v_mfma_f32_16x16x32_bf16 v[136:139], a[252:255], v[224:227], v[136:139]\n"
      "ds_read_b64_tr_b16 a[240:241], v11 offset:49152\n"
      "ds_read_b64_tr_b16 a[242:243], v11 offset:49664\n"
      "v_mfma_f32_16x16x32_bf16 v[140:143], a[252:255], v[228:231], v[140:143]\n"
      "v_xor_b32 v12, 0xc060, v6\n"
      "s_waitcnt lgkmcnt(6)\n"
      "v_mfma_f32_16x16x32_bf16 v[112:115], a[216:219], v[232:235], v[112:115]\n"
      "ds_read_b64_tr_b16 a[244:245], v12 offset:49152\n"
      "ds_read_b64_tr_b16 a[246:247], v12 offset:49664\n"
      "v_mfma_f32_16x16x32_bf16 v[116:119], a[216:219], v[236:239], v[116:119]\n"
      "v_mfma_f32_16x16x32_bf16 v[120:123], a[220:223], v[232:235], v[120:123]\n"
      "ds_read_b64_tr_b16 a[248:249], v9 offset:53248\n"
      "ds_read_b64_tr_b16 a[250:251], v9 offset:53760\n"
      "v_mfma_f32_16x16x32_bf16 v[124:127], a[220:223], v[236:239], v[124:127]\n"
      "v_mfma_f32_16x16x32_bf16 v[128:131], a[224:227], v[232:235], v[128:131]\n"
      "ds_read_b64_tr_b16 a[252:253], v10 offset:53248\n"
      "ds_read_b64_tr_b16 a[254:255], v10 offset:53760\n"
      "v_mfma_f32_16x16x32_bf16 v[132:135], a[224:227], v[236:239], v[132:135]\n"
      "v_mfma_f32_16x16x32_bf16 v[136:139], a[228:231], v[232:235], v[136:139]\n"
      "ds_read_b64_tr_b16 a[216:217], v11 offset:53248\n"
      "ds_read_b64_tr_b16 a[218:219], v11 offset:53760\n"
      "v_mfma_f32_16x16x32_bf16 v[140:143], a[228:231], v[236:239], v[140:143]\n"
      "s_waitcnt vmcnt(14) lgkmcnt(6)\n"
      "s_barrier\n"
      "v_mfma_f32_16x16x32_bf16 v[112:115], a[232:235], v[240:243], v[112:115]\n"
      "ds_read_b64_tr_b16 a[220:221], v12 offset:53248\n"
      "ds_read_b64_tr_b16 a[222:223], v12 offset:53760\n"
      "v_mfma_f32_16x16x32_bf16 v[116:119], a[232:235], v[244:247], v[116:119]\n"
      "v_mfma_f32_16x16x32_bf16 v[120:123], a[236:239], v[240:243], v[120:123]\n"
      "ds_read_b64_tr_b16 a[224:225], v9 offset:0\n"
      "ds_read_b64_tr_b16 a[226:227], v9 offset:512\n"
      "v_mfma_f32_16x16x32_bf16 v[124:127], a[236:239], v[244:247], v[124:127]\n"
      "v_mfma_f32_16x16x32_bf16 v[128:131], a[240:243], v[240:243], v[128:131]\n"
      "ds_read_b64_tr_b16 a[228:229], v10 offset:0\n"
      "ds_read_b64_tr_b16 a[230:231], v10 offset:512\n"
      "v_mfma_f32_16x16x32_bf16 v[132:135], a[240:243], v[244:247], v[132:135]\n"
      "v_mfma_f32_16x16x32_bf16 v[136:139], a[244:247], v[240:243], v[136:139]\n"
      "ds_read_b64_tr_b16 a[232:233], v11 offset:0\n"
      "ds_read_b64_tr_b16 a[234:235], v11 offset:512\n"
      "v_mfma_f32_16x16x32_bf16 v[140:143], a[244:247], v[244:247], v[140:143]\n"
      "s_waitcnt lgkmcnt(6)\n"
      "v_mfma_f32_16x16x32_bf16 v[112:115], a[248:251], v[248:251], v[112:115]\n"
      "ds_read_b64_tr_b16 a[236:237], v12 offset:0\n"
      "ds_read_b64_tr_b16 a[238:239], v12 offset:512\n"
      "v_mfma_f32_16x16x32_bf16 v[116:119], a[248:251], v[252:255], v[116:119]\n"
      "v_mfma_f32_16x16x32_bf16 v[120:123], a[252:255], v[248:251], v[120:123]\n"
      "ds_read_b64_tr_b16 a[240:241], v9 offset:4096\n"
      "ds_read_b64_tr_b16 a[242:243], v9 offset:4608\n"
      "v_mfma_f32_16x16x32_bf16 v[124:127], a[252:255], v[252:255], v[124:127]\n"
      "v_mfma_f32_16x16x32_bf16 v[128:131], a[216:219], v[248:251], v[128:131]\n"
      "ds_read_b64_tr_b16 a[244:245], v10 offset:4096\n"
      "ds_read_b64_tr_b16 a[246:247], v10 offset:4608\n"
      "v_mfma_f32_16x16x32_bf16 v[132:135], a[216:219], v[252:255], v[132:135]\n"
      "v_mfma_f32_16x16x32_bf16 v[136:139], a[220:223], v[248:251], v[136:139]\n"
      "ds_read_b64_tr_b16 a[248:249], v11 offset:4096\n"
      "ds_read_b64_tr_b16 a[250:251], v11 offset:4608\n"
      "v_mfma_f32_16x16x32_bf16 v[140:143], a[220:223], v[252:255], v[140:143]\n"
      "s_waitcnt lgkmcnt(6)\n"
      "v_mfma_f32_16x16x32_bf16 v[80:83], a[224:227], v[144:147], 0\n"
      "ds_read_b64_tr_b16 a[252:253], v12 offset:4096\n"
      "ds_read_b64_tr_b16 a[254:255], v12 offset:4608\n"
      "v_mfma_f32_16x16x32_bf16 v[84:87], a[224:227], v[148:151], 0\n"
      "v_mfma_f32_16x16x32_bf16 v[88:91], a[228:231], v[144:147], 0\n"
      "ds_read_b64_tr_b16 a[216:217], v9 offset:8192\n"
      "ds_read_b64_tr_b16 a[218:219], v9 offset:8704\n"
      "v_mfma_f32_16x16x32_bf16 v[92:95], a[228:231], v[148:151], 0\n"
      "v_mfma_f32_16x16x32_bf16 v[96:99], a[232:235], v[144:147], 0\n"
      "ds_read_b64_tr_b16 a[220:221], v10 offset:8192\n"
      "ds_read_b64_tr_b16 a[222:223], v10 offset:8704\n"
      "v_mfma_f32_16x16x32_bf16 v[100:103], a[232:235], v[148:151], 0\n"
      "v_mfma_f32_16x16x32_bf16 v[104:107], a[236:239], v[144:147], 0\n"
      "ds_read_b64_tr_b16 a[224:225], v11 offset:8192\n"
      "ds_read_b64_tr_b16 a[226:227], v11 offset:8704\n"
      "v_mfma_f32_16x16x32_bf16 v[108:111], a[236:239], v[148:151], 0\n"
      "v_cvt_pk_bf16_f32 v48, v112, v113\n"
      "v_cvt_pk_bf16_f32 v49, v114, v115\n"
      "s_waitcnt lgkmcnt(6)\n"
      "v_mfma_f32_16x16x32_bf16 v[80:83], a[240:243], v[152:155], v[80:83]\n"
      "ds_read_b64_tr_b16 a[228:229], v12 offset:8192\n"
      "ds_read_b64_tr_b16 a[230:231], v12 offset:8704\n"
      "v_mfma_f32_16x16x32_bf16 v[84:87], a[240:243], v[156:159], v[84:87]\n"
      "v_cvt_pk_bf16_f32 v50, v116, v117\n"
      "v_mfma_f32_16x16x32_bf16 v[88:91], a[244:247], v[152:155], v[88:91]\n"
      "ds_read_b64_tr_b16 a[232:233], v9 offset:12288\n"
      "ds_read_b64_tr_b16 a[234:235], v9 offset:12800\n"
      "v_mfma_f32_16x16x32_bf16 v[92:95], a[244:247], v[156:159], v[92:95]\n"
      "v_cvt_pk_bf16_f32 v51, v118, v119\n"
      "v_mfma_f32_16x16x32_bf16 v[96:99], a[248:251], v[152:155], v[96:99]\n"
      "ds_read_b64_tr_b16 a[236:237], v10 offset:12288\n"
      "ds_read_b64_tr_b16 a[238:239], v10 offset:12800\n"
      "v_mfma_f32_16x16x32_bf16 v[100:103], a[248:251], v[156:159], v[100:103]\n"
      "v_cvt_pk_bf16_f32 v52, v120, v121\n"
      "v_cvt_pk_bf16_f32 v53, v122, v123\n"
      "v_mfma_f32_16x16x32_bf16 v[104:107], a[252:255], v[152:155], v[104:107]\n"
      "ds_read_b64_tr_b16 a[240:241], v11 offset:12288\n"
      "ds_read_b64_tr_b16 a[242:243], v11 offset:12800\n"
      "v_mfma_f32_16x16x32_bf16 v[108:111], a[252:255], v[156:159], v[108:111]\n"
      "v_cvt_pk_bf16_f32 v54, v124, v125\n"
      "v_cvt_pk_bf16_f32 v55, v126, v127\n"
      "s_waitcnt lgkmcnt(6)\n"
      "v_mfma_f32_16x16x32_bf16 v[80:83], a[216:219], v[160:163], v[80:83]\n"
      "ds_read_b64_tr_b16 a[244:245], v12 offset:12288\n"
      "ds_read_b64_tr_b16 a[246:247], v12 offset:12800\n"
      "v_mfma_f32_16x16x32_bf16 v[84:87], a[216:219], v[164:167], v[84:87]\n"
      "v_permlane16_swap_b32 v48, v50\n"
      "v_mfma_f32_16x16x32_bf16 v[88:91], a[220:223], v[160:163], v[88:91]\n"
      "ds_read_b64_tr_b16 a[248:249], v9 offset:16384\n"
      "ds_read_b64_tr_b16 a[250:251], v9 offset:16896\n"
      "v_mfma_f32_16x16x32_bf16 v[92:95], a[220:223], v[164:167], v[92:95]\n"
      "v_permlane16_swap_b32 v49, v51\n"
      "v_permlane16_swap_b32 v52, v54\n"
      "v_mfma_f32_16x16x32_bf16 v[96:99], a[224:227], v[160:163], v[96:99]\n"
      "ds_read_b64_tr_b16 a[252:253], v10 offset:16384\n"
      "ds_read_b64_tr_b16 a[254:255], v10 offset:16896\n"
      "v_mfma_f32_16x16x32_bf16 v[100:103], a[224:227], v[164:167], v[100:103]\n"
      "v_permlane16_swap_b32 v53, v55\n"
      "v_mfma_f32_16x16x32_bf16 v[104:107], a[228:231], v[160:163], v[104:107]\n"
      "ds_read_b64_tr_b16 a[216:217], v11 offset:16384\n"
      "ds_read_b64_tr_b16 a[218:219], v11 offset:16896\n"
      "v_mfma_f32_16x16x32_bf16 v[108:111], a[228:231], v[164:167], v[108:111]\n"
      "v_mov_b32 v18, 0\n"
      "v_mov_b32 v19, 0\n"
      "s_waitcnt lgkmcnt(6)\n"
      "v_mfma_f32_16x16x32_bf16 v[80:83], a[232:235], v[168:171], v[80:83]\n"
      "ds_read_b64_tr_b16 a[220:221], v12 offset:16384\n"
      "ds_read_b64_tr_b16 a[222:223], v12 offset:16896\n"
      "v_mfma_f32_16x16x32_bf16 v[84:87], a[232:235], v[172:175], v[84:87]\n"
      "buffer_atomic_or_x2 v[18:19], v14, s[48:51], s88 offen offset:0 sc0 sc1\n"
      "v_mfma_f32_16x16x32_bf16 v[88:91], a[236:239], v[168:171], v[88:91]\n"
      "ds_read_b64_tr_b16 a[224:225], v9 offset:20480\n"
      "ds_read_b64_tr_b16 a[226:227], v9 offset:20992\n"
      "v_mfma_f32_16x16x32_bf16 v[92:95], a[236:239], v[172:175], v[92:95]\n"
      "v_cvt_pk_bf16_f32 v56, v128, v129\n"
      "v_cvt_pk_bf16_f32 v57, v130, v131\n"
      "v_mfma_f32_16x16x32_bf16 v[96:99], a[240:243], v[168:171], v[96:99]\n"
      "ds_read_b64_tr_b16 a[228:229], v10 offset:20480\n"
      "ds_read_b64_tr_b16 a[230:231], v10 offset:20992\n"
      "v_mfma_f32_16x16x32_bf16 v[100:103], a[240:243], v[172:175], v[100:103]\n"
      "v_cvt_pk_bf16_f32 v58, v132, v133\n"
      "v_cvt_pk_bf16_f32 v59, v134, v135\n"
      "v_mfma_f32_16x16x32_bf16 v[104:107], a[244:247], v[168:171], v[104:107]\n"
      "ds_read_b64_tr_b16 a[232:233], v11 offset:20480\n"
      "ds_read_b64_tr_b16 a[234:235], v11 offset:20992\n"
      "v_mfma_f32_16x16x32_bf16 v[108:111], a[244:247], v[172:175], v[108:111]\n"
      "v_cvt_pk_bf16_f32 v60, v136, v137\n"
      "v_cvt_pk_bf16_f32 v61, v138, v139\n"
      "s_waitcnt lgkmcnt(6)\n"
      "v_mfma_f32_16x16x32_bf16 v[80:83], a[248:251], v[176:179], v[80:83]\n"
      "ds_read_b64_tr_b16 a[236:237], v12 offset:20480\n"
      "ds_read_b64_tr_b16 a[238:239], v12 offset:20992\n"
      "v_mfma_f32_16x16x32_bf16 v[84:87], a[248:251], v[180:183], v[84:87]\n"
      "v_cvt_pk_bf16_f32 v62, v140, v141\n"
      "v_mfma_f32_16x16x32_bf16 v[88:91], a[252:255], v[176:179], v[88:91]\n"
      "ds_read_b64_tr_b16 a[240:241], v9 offset:24576\n"
      "ds_read_b64_tr_b16 a[242:243], v9 offset:25088\n"
      "v_mfma_f32_16x16x32_bf16 v[92:95], a[252:255], v[180:183], v[92:95]\n"
      "v_cvt_pk_bf16_f32 v63, v142, v143\n"
      "v_mfma_f32_16x16x32_bf16 v[96:99], a[216:219], v[176:179], v[96:99]\n"
      "ds_read_b64_tr_b16 a[244:245], v10 offset:24576\n"
      "ds_read_b64_tr_b16 a[246:247], v10 offset:25088\n"
      "v_mfma_f32_16x16x32_bf16 v[100:103], a[216:219], v[180:183], v[100:103]\n"
      "v_permlane16_swap_b32 v56, v58\n"
      "v_permlane16_swap_b32 v57, v59\n"
      "v_mfma_f32_16x16x32_bf16 v[104:107], a[220:223], v[176:179], v[104:107]\n"
      "ds_read_b64_tr_b16 a[248:249], v11 offset:24576\n"
      "ds_read_b64_tr_b16 a[250:251], v11 offset:25088\n"
      "v_mfma_f32_16x16x32_bf16 v[108:111], a[220:223], v[180:183], v[108:111]\n"
      "v_permlane16_swap_b32 v60, v62\n"
      "v_permlane16_swap_b32 v61, v63\n"
      "s_waitcnt lgkmcnt(6)\n"
      "v_mfma_f32_16x16x32_bf16 v[80:83], a[224:227], v[184:187], v[80:83]\n"
      "ds_read_b64_tr_b16 a[252:253], v12 offset:24576\n"
      "ds_read_b64_tr_b16 a[254:255], v12 offset:25088\n"
      "v_mfma_f32_16x16x32_bf16 v[84:87], a[224:227], v[188:191], v[84:87]\n"
      "v_mfma_f32_16x16x32_bf16 v[88:91], a[228:231], v[184:187], v[88:91]\n"
      "ds_read_b64_tr_b16 a[216:217], v9 offset:28672\n"
      "ds_read_b64_tr_b16 a[218:219], v9 offset:29184\n"
      "v_mfma_f32_16x16x32_bf16 v[92:95], a[228:231], v[188:191], v[92:95]\n"
      "v_mfma_f32_16x16x32_bf16 v[96:99], a[232:235], v[184:187], v[96:99]\n"
      "ds_read_b64_tr_b16 a[220:221], v10 offset:28672\n"
      "ds_read_b64_tr_b16 a[222:223], v10 offset:29184\n"
      "v_mfma_f32_16x16x32_bf16 v[100:103], a[232:235], v[188:191], v[100:103]\n"
      "v_mfma_f32_16x16x32_bf16 v[104:107], a[236:239], v[184:187], v[104:107]\n"
      "ds_read_b64_tr_b16 a[224:225], v11 offset:28672\n"
      "ds_read_b64_tr_b16 a[226:227], v11 offset:29184\n"
      "v_mfma_f32_16x16x32_bf16 v[108:111], a[236:239], v[188:191], v[108:111]\n"
      "s_waitcnt lgkmcnt(6)\n"
      "v_mfma_f32_16x16x32_bf16 v[80:83], a[240:243], v[192:195], v[80:83]\n"
      "ds_read_b64_tr_b16 a[228:229], v12 offset:28672\n"
      ".LPAD11:\n"
      "s_cbranch_execz .LRET\n"
      "ds_read_b64_tr_b16 a[230:231], v12 offset:29184\n"
      "v_mfma_f32_16x16x32_bf16 v[84:87], a[240:243], v[196:199], v[84:87]\n"
      "v_mfma_f32_16x16x32_bf16 v[88:91], a[244:247], v[192:195], v[88:91]\n"
      "ds_read_b64_tr_b16 a[232:233], v9 offset:32768\n"
      "ds_read_b64_tr_b16 a[234:235], v9 offset:33280\n"
      "v_mfma_f32_16x16x32_bf16 v[92:95], a[244:247], v[196:199], v[92:95]\n"
      "v_mfma_f32_16x16x32_bf16 v[96:99], a[248:251], v[192:195], v[96:99]\n"
      "ds_read_b64_tr_b16 a[236:237], v10 offset:32768\n"
      "ds_read_b64_tr_b16 a[238:239], v10 offset:33280\n"
      "v_mfma_f32_16x16x32_bf16 v[100:103], a[248:251], v[196:199], v[100:103]\n"
      "v_mfma_f32_16x16x32_bf16 v[104:107], a[252:255], v[192:195], v[104:107]\n"
      "ds_read_b64_tr_b16 a[240:241], v11 offset:32768\n"
      "ds_read_b64_tr_b16 a[242:243], v11 offset:33280\n"
      "v_mfma_f32_16x16x32_bf16 v[108:111], a[252:255], v[196:199], v[108:111]\n"
      "s_waitcnt lgkmcnt(6)\n"
      "v_mfma_f32_16x16x32_bf16 v[80:83], a[216:219], v[200:203], v[80:83]\n"
      "ds_read_b64_tr_b16 a[244:245], v12 offset:32768\n"
      "ds_read_b64_tr_b16 a[246:247], v12 offset:33280\n"
      "v_mfma_f32_16x16x32_bf16 v[84:87], a[216:219], v[204:207], v[84:87]\n"
      "s_waitcnt vmcnt(0)\n"
      "v_cmp_ne_u64 vcc, s[96:97], v[18:19]\n"
      "s_and_b64 vcc, vcc, 0x3fff3fff\n"
      "s_cbranch_scc1 .LS_KVA\n"
      ".LK_KVA:\n"
      "v_mfma_f32_16x16x32_bf16 v[88:91], a[220:223], v[200:203], v[88:91]\n"
      "ds_read_b64_tr_b16 a[248:249], v9 offset:36864\n"
      "ds_read_b64_tr_b16 a[250:251], v9 offset:37376\n"
      "v_mfma_f32_16x16x32_bf16 v[92:95], a[220:223], v[204:207], v[92:95]\n"
      "s_add_u32 s56, s68, 0\n"
      "buffer_load_dwordx4 a[0:3], v1, s[48:51], s56 offen offset:0 sc1\n"
      "v_mfma_f32_16x16x32_bf16 v[96:99], a[224:227], v[200:203], v[96:99]\n"
      "ds_read_b64_tr_b16 a[252:253], v10 offset:36864\n"
      "ds_read_b64_tr_b16 a[254:255], v10 offset:37376\n"
      "v_mfma_f32_16x16x32_bf16 v[100:103], a[224:227], v[204:207], v[100:103]\n"
      "v_mov_b32 v24, 0xff\n"
      "v_mov_b32 v25, 0\n"
      "v_mfma_f32_16x16x32_bf16 v[104:107], a[228:231], v[200:203], v[104:107]\n"
      "ds_read_b64_tr_b16 a[216:217], v11 offset:36864\n"
      "ds_read_b64_tr_b16 a[218:219], v11 offset:37376\n"
      "v_mfma_f32_16x16x32_bf16 v[108:111], a[228:231], v[204:207], v[108:111]\n"
      "buffer_load_dwordx4 a[4:7], v1, s[48:51], s56 offen offset:1024 sc1\n"
      "s_waitcnt lgkmcnt(6)\n"
      "v_mfma_f32_16x16x32_bf16 v[80:83], a[232:235], v[208:211], v[80:83]\n"
      "ds_read_b64_tr_b16 a[220:221], v12 offset:36864\n"
      "ds_read_b64_tr_b16 a[222:223], v12 offset:37376\n"
      "v_mfma_f32_16x16x32_bf16 v[84:87], a[232:235], v[212:215], v[84:87]\n"
      "v_lshlrev_b64 v[24:25], v21, v[24:25]\n"
      "v_mfma_f32_16x16x32_bf16 v[88:91], a[236:239], v[208:211], v[88:91]\n"
      "ds_read_b64_tr_b16 a[224:225], v9 offset:40960\n"
      "ds_read_b64_tr_b16 a[226:227], v9 offset:41472\n"
      "v_mfma_f32_16x16x32_bf16 v[92:95], a[236:239], v[212:215], v[92:95]\n"
      "s_add_u32 s57, s69, 0\n"
      "buffer_load_dwordx4 a[8:11], v2, s[48:51], s57 offen offset:0 sc1\n"
      "buffer_load_dwordx4 a[12:15], v2, s[48:51], s57 offen offset:1024 sc1\n"
      "v_mfma_f32_16x16x32_bf16 v[96:99], a[240:243], v[208:211], v[96:99]\n"
      "ds_read_b64_tr_b16 a[228:229], v10 offset:40960\n"
      "ds_read_b64_tr_b16 a[230:231], v10 offset:41472\n"
      "v_mfma_f32_16x16x32_bf16 v[100:103], a[240:243], v[212:215], v[100:103]\n"
      "v_and_b32 v24, s74, v24\n"
      "v_mov_b32 v26, 0xf149f2ca\n"
      "v_mfma_f32_16x16x32_bf16 v[104:107], a[244:247], v[208:211], v[104:107]\n"
      "ds_read_b64_tr_b16 a[232:233], v11 offset:40960\n"
      "ds_read_b64_tr_b16 a[234:235], v11 offset:41472\n"
      "v_mfma_f32_16x16x32_bf16 v[108:111], a[244:247], v[212:215], v[108:111]\n"
      "buffer_load_dwordx4 a[16:19], v4, s[48:51], s70 offen offset:0 sc1\n"
      "s_waitcnt lgkmcnt(6)\n"
      "v_mfma_f32_16x16x32_bf16 v[80:83], a[248:251], v[216:219], v[80:83]\n"
      "ds_read_b64_tr_b16 a[236:237], v12 offset:40960\n"
      "ds_read_b64_tr_b16 a[238:239], v12 offset:41472\n"
      "v_mfma_f32_16x16x32_bf16 v[84:87], a[248:251], v[220:223], v[84:87]\n"
      "v_mov_b32 v27, 0xffff\n"
      "v_mfma_f32_16x16x32_bf16 v[88:91], a[252:255], v[216:219], v[88:91]\n"
      "ds_read_b64_tr_b16 a[240:241], v9 offset:45056\n"
      "ds_read_b64_tr_b16 a[242:243], v9 offset:45568\n"
      "v_mfma_f32_16x16x32_bf16 v[92:95], a[252:255], v[220:223], v[92:95]\n"
      "buffer_load_dwordx4 a[20:23], v4, s[48:51], s71 offen offset:0 sc1\n"
      "v_mfma_f32_16x16x32_bf16 v[96:99], a[216:219], v[216:219], v[96:99]\n"
      "ds_read_b64_tr_b16 a[244:245], v10 offset:45056\n"
      "ds_read_b64_tr_b16 a[246:247], v10 offset:45568\n"
      "v_mfma_f32_16x16x32_bf16 v[100:103], a[216:219], v[220:223], v[100:103]\n"
      "v_mfma_f32_16x16x32_bf16 v[104:107], a[220:223], v[216:219], v[104:107]\n"
      "ds_read_b64_tr_b16 a[248:249], v11 offset:45056\n"
      "ds_read_b64_tr_b16 a[250:251], v11 offset:45568\n"
      "v_mfma_f32_16x16x32_bf16 v[108:111], a[220:223], v[220:223], v[108:111]\n"
      "buffer_load_dwordx2 a[24:25], v4, s[48:51], s72 offen offset:0 sc1\n"
      "s_waitcnt lgkmcnt(6)\n"
      "v_mfma_f32_16x16x32_bf16 v[80:83], a[224:227], v[224:227], v[80:83]\n"
      "ds_read_b64_tr_b16 a[252:253], v12 offset:45056\n"
      "ds_read_b64_tr_b16 a[254:255], v12 offset:45568\n"
      "v_mfma_f32_16x16x32_bf16 v[84:87], a[224:227], v[228:231], v[84:87]\n"
      "v_mfma_f32_16x16x32_bf16 v[88:91], a[228:231], v[224:227], v[88:91]\n"
      "ds_read_b64_tr_b16 a[216:217], v9 offset:57344\n"
      "ds_read_b64_tr_b16 a[218:219], v9 offset:57856\n"
      "v_mfma_f32_16x16x32_bf16 v[92:95], a[228:231], v[228:231], v[92:95]\n"
      "s_add_u32 s56, s68, 1536\n"
      "buffer_load_dwordx4 a[26:29], v1, s[48:51], s56 offen offset:512 sc1\n"
      "v_mfma_f32_16x16x32_bf16 v[96:99], a[232:235], v[224:227], v[96:99]\n"
      "ds_read_b64_tr_b16 a[220:221], v10 offset:57344\n"
      "ds_read_b64_tr_b16 a[222:223], v10 offset:57856\n"
      "v_mfma_f32_16x16x32_bf16 v[100:103], a[232:235], v[228:231], v[100:103]\n"
      "v_mfma_f32_16x16x32_bf16 v[104:107], a[236:239], v[224:227], v[104:107]\n"
      "ds_read_b64_tr_b16 a[224:225], v11 offset:57344\n"
      "ds_read_b64_tr_b16 a[226:227], v11 offset:57856\n"
      "v_mfma_f32_16x16x32_bf16 v[108:111], a[236:239], v[228:231], v[108:111]\n"
      "buffer_load_dwordx4 a[30:33], v7, s[48:51], s56 offen offset:0 sc1\n"
      "s_waitcnt lgkmcnt(6)\n"
      "v_mfma_f32_16x16x32_bf16 v[80:83], a[240:243], v[232:235], v[80:83]\n"
      "ds_read_b64_tr_b16 a[228:229], v12 offset:57344\n"
      "ds_read_b64_tr_b16 a[230:231], v12 offset:57856\n"
      "v_mfma_f32_16x16x32_bf16 v[84:87], a[240:243], v[236:239], v[84:87]\n"
      "v_mfma_f32_16x16x32_bf16 v[88:91], a[244:247], v[232:235], v[88:91]\n"
      "ds_read_b64_tr_b16 a[232:233], v9 offset:61440\n"
      "ds_read_b64_tr_b16 a[234:235], v9 offset:61952\n"
      "v_mfma_f32_16x16x32_bf16 v[92:95], a[244:247], v[236:239], v[92:95]\n"
      "s_add_u32 s57, s69, 1536\n"
      "buffer_load_dwordx4 a[34:37], v2, s[48:51], s57 offen offset:512 sc1\n"
      "buffer_load_dwordx4 a[38:41], v8, s[48:51], s57 offen offset:0 sc1\n"
      "v_mfma_f32_16x16x32_bf16 v[96:99], a[248:251], v[232:235], v[96:99]\n"
      "ds_read_b64_tr_b16 a[236:237], v10 offset:61440\n"
      "ds_read_b64_tr_b16 a[238:239], v10 offset:61952\n"
      "v_mfma_f32_16x16x32_bf16 v[100:103], a[248:251], v[236:239], v[100:103]\n"
      "v_mfma_f32_16x16x32_bf16 v[104:107], a[252:255], v[232:235], v[104:107]\n"
      "ds_read_b64_tr_b16 a[240:241], v11 offset:61440\n"
      "ds_read_b64_tr_b16 a[242:243], v11 offset:61952\n"
      "v_mfma_f32_16x16x32_bf16 v[108:111], a[252:255], v[236:239], v[108:111]\n"
      "buffer_load_dwordx4 a[42:45], v4, s[48:51], s70 offen offset:384 sc1\n"
      "s_waitcnt vmcnt(13) lgkmcnt(6)\n"
      "s_barrier\n"
      "v_mfma_f32_16x16x32_bf16 v[80:83], a[216:219], v[240:243], v[80:83]\n"
      "ds_read_b64_tr_b16 a[244:245], v12 offset:61440\n"
      "ds_read_b64_tr_b16 a[246:247], v12 offset:61952\n"
      "v_mfma_f32_16x16x32_bf16 v[84:87], a[216:219], v[244:247], v[84:87]\n"
      "v_xor_b32 v9, 0x18000, v6\n"
      "v_mfma_f32_16x16x32_bf16 v[88:91], a[220:223], v[240:243], v[88:91]\n"
      "ds_read_b64_tr_b16 a[248:249], v9 offset:16384\n"
      "ds_read_b64_tr_b16 a[250:251], v9 offset:16896\n"
      "v_mfma_f32_16x16x32_bf16 v[92:95], a[220:223], v[244:247], v[92:95]\n"
      "buffer_load_dwordx4 a[46:49], v4, s[48:51], s71 offen offset:384 sc1\n"
      "v_xor_b32 v10, 0x18020, v6\n"
      "v_mfma_f32_16x16x32_bf16 v[96:99], a[224:227], v[240:243], v[96:99]\n"
      "ds_read_b64_tr_b16 a[252:253], v10 offset:16384\n"
      "ds_read_b64_tr_b16 a[254:255], v10 offset:16896\n"
      "v_mfma_f32_16x16x32_bf16 v[100:103], a[224:227], v[244:247], v[100:103]\n"
      "v_mfma_f32_16x16x32_bf16 v[104:107], a[228:231], v[240:243], v[104:107]\n"
      "ds_read_b64_tr_b16 a[216:217], v9 offset:20480\n"
      "ds_read_b64_tr_b16 a[218:219], v9 offset:20992\n"
      "v_mfma_f32_16x16x32_bf16 v[108:111], a[228:231], v[244:247], v[108:111]\n"
      "buffer_load_dwordx2 a[50:51], v4, s[48:51], s72 offen offset:384 sc1\n"
      "s_waitcnt lgkmcnt(6)\n"
      "v_mfma_f32_16x16x32_bf16 v[80:83], a[232:235], v[248:251], v[80:83]\n"
      "ds_read_b64_tr_b16 a[220:221], v10 offset:20480\n"
      "ds_read_b64_tr_b16 a[222:223], v10 offset:20992\n"
      "v_mfma_f32_16x16x32_bf16 v[84:87], a[232:235], v[252:255], v[84:87]\n"
      "v_mfma_f32_16x16x32_bf16 v[88:91], a[236:239], v[248:251], v[88:91]\n"
      "ds_read_b64_tr_b16 a[224:225], v9 offset:24576\n"
      "ds_read_b64_tr_b16 a[226:227], v9 offset:25088\n"
      "v_mfma_f32_16x16x32_bf16 v[92:95], a[236:239], v[252:255], v[92:95]\n"
      "s_add_u32 s56, s68, 3584\n"
      "buffer_load_dwordx4 a[52:55], v1, s[48:51], s56 offen offset:512 sc1\n"
      "v_mfma_f32_16x16x32_bf16 v[96:99], a[240:243], v[248:251], v[96:99]\n"
      "ds_read_b64_tr_b16 a[228:229], v10 offset:24576\n"
      "ds_read_b64_tr_b16 a[230:231], v10 offset:25088\n"
      "v_mfma_f32_16x16x32_bf16 v[100:103], a[240:243], v[252:255], v[100:103]\n"
      "v_mfma_f32_16x16x32_bf16 v[104:107], a[244:247], v[248:251], v[104:107]\n"
      "ds_read_b64_tr_b16 a[232:233], v9 offset:28672\n"
      "ds_read_b64_tr_b16 a[234:235], v9 offset:29184\n"
      "v_mfma_f32_16x16x32_bf16 v[108:111], a[244:247], v[252:255], v[108:111]\n"
      "buffer_load_dwordx4 a[56:59], v7, s[48:51], s56 offen offset:0 sc1\n"
      "s_waitcnt lgkmcnt(6)\n"
      "v_mfma_f32_16x16x32_bf16 v[112:115], a[248:251], v[144:147], 0\n"
      "ds_read_b64_tr_b16 a[236:237], v10 offset:28672\n"
      "ds_read_b64_tr_b16 a[238:239], v10 offset:29184\n"
      "v_mfma_f32_16x16x32_bf16 v[116:119], a[248:251], v[148:151], 0\n"
      "v_mfma_f32_16x16x32_bf16 v[120:123], a[252:255], v[144:147], 0\n"
      "ds_read_b64_tr_b16 a[240:241], v9 offset:32768\n"
      "ds_read_b64_tr_b16 a[242:243], v9 offset:33280\n"
      "v_mfma_f32_16x16x32_bf16 v[124:127], a[252:255], v[148:151], 0\n"
      "s_add_u32 s57, s69, 3584\n"
      "buffer_load_dwordx4 a[60:63], v2, s[48:51], s57 offen offset:512 sc1\n"
      "buffer_load_dwordx4 a[64:67], v8, s[48:51], s57 offen offset:0 sc1\n"
      "v_mfma_f32_16x16x32_bf16 v[112:115], a[216:219], v[152:155], v[112:115]\n"
      "ds_read_b64_tr_b16 a[244:245], v10 offset:32768\n"
      "ds_read_b64_tr_b16 a[246:247], v10 offset:33280\n"
      "v_mfma_f32_16x16x32_bf16 v[116:119], a[216:219], v[156:159], v[116:119]\n"
      "v_mfma_f32_16x16x32_bf16 v[120:123], a[220:223], v[152:155], v[120:123]\n"
      "ds_read_b64_tr_b16 a[248:249], v9 offset:36864\n"
      "ds_read_b64_tr_b16 a[250:251], v9 offset:37376\n"
      "v_mfma_f32_16x16x32_bf16 v[124:127], a[220:223], v[156:159], v[124:127]\n"
      "buffer_load_dwordx4 a[68:71], v4, s[48:51], s70 offen offset:896 sc1\n"
      "v_cvt_pk_bf16_f32 v64, v80, v81\n"
      "v_cvt_pk_bf16_f32 v65, v82, v83\n"
      "s_waitcnt lgkmcnt(6)\n"
      "v_mfma_f32_16x16x32_bf16 v[112:115], a[224:227], v[160:163], v[112:115]\n"
      "ds_read_b64_tr_b16 a[252:253], v10 offset:36864\n"
      "ds_read_b64_tr_b16 a[254:255], v10 offset:37376\n"
      "v_mfma_f32_16x16x32_bf16 v[116:119], a[224:227], v[164:167], v[116:119]\n"
      "v_cvt_pk_bf16_f32 v66, v84, v85\n"
      "v_cvt_pk_bf16_f32 v67, v86, v87\n"
      "v_cvt_pk_bf16_f32 v68, v88, v89\n"
      "v_mfma_f32_16x16x32_bf16 v[120:123], a[228:231], v[160:163], v[120:123]\n"
      "ds_read_b64_tr_b16 a[216:217], v9 offset:40960\n"
      "ds_read_b64_tr_b16 a[218:219], v9 offset:41472\n"
      "v_mfma_f32_16x16x32_bf16 v[124:127], a[228:231], v[164:167], v[124:127]\n"
      "buffer_load_dwordx4 a[72:75], v4, s[48:51], s71 offen offset:896 sc1\n"
      "v_cvt_pk_bf16_f32 v69, v90, v91\n"
      "v_cvt_pk_bf16_f32 v70, v92, v93\n"
      "v_mfma_f32_16x16x32_bf16 v[112:115], a[232:235], v[168:171], v[112:115]\n"
      "ds_read_b64_tr_b16 a[220:221], v10 offset:40960\n"
      "ds_read_b64_tr_b16 a[222:223], v10 offset:41472\n"
      "v_mfma_f32_16x16x32_bf16 v[116:119], a[232:235], v[172:175], v[116:119]\n"
      "v_cvt_pk_bf16_f32 v71, v94, v95\n"
      "v_permlane16_swap_b32 v64, v66\n"
      "v_permlane16_swap_b32 v65, v67\n"
      "v_permlane16_swap_b32 v68, v70\n"
      "v_mfma_f32_16x16x32_bf16 v[120:123], a[236:239], v[168:171], v[120:123]\n"
      "ds_read_b64_tr_b16 a[224:225], v9 offset:45056\n"
      "ds_read_b64_tr_b16 a[226:227], v9 offset:45568\n"
      "v_mfma_f32_16x16x32_bf16 v[124:127], a[236:239], v[172:175], v[124:127]\n"
      "buffer_load_dwordx2 a[76:77], v4, s[48:51], s72 offen offset:896 sc1\n"
      "v_permlane16_swap_b32 v69, v71\n"
      "v_cvt_pk_bf16_f32 v72, v96, v97\n"
      "v_cvt_pk_bf16_f32 v73, v98, v99\n"
      "s_waitcnt lgkmcnt(6)\n"
      "v_mfma_f32_16x16x32_bf16 v[112:115], a[240:243], v[176:179], v[112:115]\n"
      "ds_read_b64_tr_b16 a[228:229], v10 offset:45056\n"
      "ds_read_b64_tr_b16 a[230:231], v10 offset:45568\n"
      "v_mfma_f32_16x16x32_bf16 v[116:119], a[240:243], v[180:183], v[116:119]\n"
      "v_cvt_pk_bf16_f32 v74, v100, v101\n"
      "v_cvt_pk_bf16_f32 v75, v102, v103\n"
      "v_cvt_pk_bf16_f32 v76, v104, v105\n"
      "v_mfma_f32_16x16x32_bf16 v[120:123], a[244:247], v[176:179], v[120:123]\n"
      "ds_read_b64_tr_b16 a[232:233], v9 offset:49152\n"
      "ds_read_b64_tr_b16 a[234:235], v9 offset:49664\n"
      "v_mfma_f32_16x16x32_bf16 v[124:127], a[244:247], v[180:183], v[124:127]\n"
      "s_add_u32 s56, s68, 5120\n"
      "buffer_load_dwordx4 a[78:81], v1, s[48:51], s56 offen offset:0 sc1\n"
      "v_cvt_pk_bf16_f32 v77, v106, v107\n"
      "v_mfma_f32_16x16x32_bf16 v[112:115], a[248:251], v[184:187], v[112:115]\n"
      "ds_read_b64_tr_b16 a[236:237], v10 offset:49152\n"
      "ds_read_b64_tr_b16 a[238:239], v10 offset:49664\n"
      "v_mfma_f32_16x16x32_bf16 v[116:119], a[248:251], v[188:191], v[116:119]\n"
      "v_cvt_pk_bf16_f32 v78, v108, v109\n"
      "v_cvt_pk_bf16_f32 v79, v110, v111\n"
      "v_permlane16_swap_b32 v72, v74\n"
      "v_permlane16_swap_b32 v73, v75\n"
      "v_mfma_f32_16x16x32_bf16 v[120:123], a[252:255], v[184:187], v[120:123]\n"
      "ds_read_b64_tr_b16 a[240:241], v9 offset:53248\n"
      "ds_read_b64_tr_b16 a[242:243], v9 offset:53760\n"
      "v_mfma_f32_16x16x32_bf16 v[124:127], a[252:255], v[188:191], v[124:127]\n"
      "buffer_load_dwordx4 a[82:85], v1, s[48:51], s56 offen offset:1024 sc1\n"
      "v_permlane16_swap_b32 v76, v78\n"
      "v_permlane16_swap_b32 v77, v79\n"
      "s_waitcnt lgkmcnt(6)\n"
      "v_mfma_f32_16x16x32_bf16 v[112:115], a[216:219], v[192:195], v[112:115]\n"
      "ds_read_b64_tr_b16 a[244:245], v10 offset:53248\n"
      "ds_read_b64_tr_b16 a[246:247], v10 offset:53760\n"
      "v_mfma_f32_16x16x32_bf16 v[116:119], a[216:219], v[196:199], v[116:119]\n"
      "v_bfe_i32 v28, v25, 0, 1\n"
      "v_bfi_b32 v144, v28, 0, v26\n"
      "v_bfe_i32 v29, v25, 1, 1\n"
      "v_bfi_b32 v145, v29, 0, v26\n"
      "v_mfma_f32_16x16x32_bf16 v[120:123], a[220:223], v[192:195], v[120:123]\n"
      "ds_read_b64_tr_b16 a[248:249], v9 offset:57344\n"
      "ds_read_b64_tr_b16 a[250:251], v9 offset:57856\n"
      "v_mfma_f32_16x16x32_bf16 v[124:127], a[220:223], v[196:199], v[124:127]\n"
      "s_add_u32 s57, s69, 5120\n"
      "buffer_load_dwordx4 a[86:89], v2, s[48:51], s57 offen offset:0 sc1\n"
      "buffer_load_dwordx4 a[90:93], v2, s[48:51], s57 offen offset:1024 sc1\n"
      "v_mfma_f32_16x16x32_bf16 v[112:115], a[224:227], v[200:203], v[112:115]\n"
      "ds_read_b64_tr_b16 a[252:253], v10 offset:57344\n"
      "ds_read_b64_tr_b16 a[254:255], v10 offset:57856\n"
      "v_mfma_f32_16x16x32_bf16 v[116:119], a[224:227], v[204:207], v[116:119]\n"
      "v_bfi_b32 v164, v27, v28, v29\n"
      "v_bfe_i32 v28, v25, 2, 1\n"
      "v_bfi_b32 v146, v28, 0, v26\n"
      "v_bfe_i32 v29, v25, 3, 1\n"
      "v_mfma_f32_16x16x32_bf16 v[120:123], a[228:231], v[200:203], v[120:123]\n"
      "ds_read_b64_tr_b16 a[216:217], v9 offset:61440\n"
      "ds_read_b64_tr_b16 a[218:219], v9 offset:61952\n"
      "v_mfma_f32_16x16x32_bf16 v[124:127], a[228:231], v[204:207], v[124:127]\n"
      "buffer_load_dwordx4 a[94:97], v4, s[48:51], s70 offen offset:1280 sc1\n"
      "v_bfi_b32 v147, v29, 0, v26\n"
      "v_bfi_b32 v165, v27, v28, v29\n"
      "v_bfe_i32 v28, v25, 8, 1\n"
      "s_waitcnt lgkmcnt(6)\n"
      "v_mfma_f32_16x16x32_bf16 v[112:115], a[232:235], v[208:211], v[112:115]\n"
      "ds_read_b64_tr_b16 a[220:221], v10 offset:61440\n"
      "ds_read_b64_tr_b16 a[222:223], v10 offset:61952\n"
      "v_mfma_f32_16x16x32_bf16 v[116:119], a[232:235], v[212:215], v[116:119]\n"
      "v_xor_b32 v11, 0x0, v6\n"
      "v_bfi_b32 v148, v28, 0, v26\n"
      "v_bfe_i32 v29, v25, 9, 1\n"
      "v_bfi_b32 v149, v29, 0, v26\n"
      "v_mfma_f32_16x16x32_bf16 v[120:123], a[236:239], v[208:211], v[120:123]\n"
      "ds_read_b64_tr_b16 a[224:225], v11 offset:0\n"
      "ds_read_b64_tr_b16 a[226:227], v11 offset:512\n"
      "v_mfma_f32_16x16x32_bf16 v[124:127], a[236:239], v[212:215], v[124:127]\n"
      "buffer_load_dwordx4 a[98:101], v4, s[48:51], s71 offen offset:1280 sc1\n"
      "v_xor_b32 v12, 0x20, v6\n"
      "v_bfi_b32 v166, v27, v28, v29\n"
      "v_mfma_f32_16x16x32_bf16 v[112:115], a[240:243], v[216:219], v[112:115]\n"
      "ds_read_b64_tr_b16 a[228:229], v12 offset:0\n"
      "ds_read_b64_tr_b16 a[230:231], v12 offset:512\n"
      "v_mfma_f32_16x16x32_bf16 v[116:119], a[240:243], v[220:223], v[116:119]\n"
      "v_bfe_i32 v28, v25, 10, 1\n"
      "v_bfi_b32 v150, v28, 0, v26\n"
      "v_bfe_i32 v29, v25, 11, 1\n"
      "v_bfi_b32 v151, v29, 0, v26\n"
      "v_bfi_b32 v167, v27, v28, v29\n"
      "v_mfma_f32_16x16x32_bf16 v[120:123], a[244:247], v[216:219], v[120:123]\n"
      "ds_read_b64_tr_b16 a[232:233], v11 offset:4096\n"
      "ds_read_b64_tr_b16 a[234:235], v11 offset:4608\n"
      "v_mfma_f32_16x16x32_bf16 v[124:127], a[244:247], v[220:223], v[124:127]\n"
      "buffer_load_dwordx2 a[102:103], v4, s[48:51], s72 offen offset:1280 sc1\n"
      "v_bfe_i32 v28, v25, 16, 1\n"
      "v_bfi_b32 v152, v28, 0, v26\n"
      "v_bfe_i32 v29, v25, 17, 1\n"
      "s_waitcnt lgkmcnt(6)\n"
      "v_mfma_f32_16x16x32_bf16 v[112:115], a[248:251], v[224:227], v[112:115]\n"
      "ds_read_b64_tr_b16 a[236:237], v12 offset:4096\n"
      "ds_read_b64_tr_b16 a[238:239], v12 offset:4608\n"
      "v_mfma_f32_16x16x32_bf16 v[116:119], a[248:251], v[228:231], v[116:119]\n"
      "v_bfi_b32 v153, v29, 0, v26\n"
      "v_bfi_b32 v168, v27, v28, v29\n"
      "v_bfe_i32 v28, v25, 18, 1\n"
      "v_mfma_f32_16x16x32_bf16 v[120:123], a[252:255], v[224:227], v[120:123]\n"
      "v_mfma_f32_16x16x32_bf16 v[124:127], a[252:255], v[228:231], v[124:127]\n"
      "s_add_u32 s56, s68, 7168\n"
      "buffer_load_dwordx4 a[104:107], v1, s[48:51], s56 offen offset:0 sc1\n"
      "v_bfi_b32 v154, v28, 0, v26\n"
      "v_bfe_i32 v29, v25, 19, 1\n"
      "v_bfi_b32 v155, v29, 0, v26\n"
      "v_bfi_b32 v169, v27, v28, v29\n"
      "v_mfma_f32_16x16x32_bf16 v[112:115], a[216:219], v[232:235], v[112:115]\n"
      "v_mfma_f32_16x16x32_bf16 v[116:119], a[216:219], v[236:239], v[116:119]\n"
      "v_bfe_i32 v28, v25, 24, 1\n"
      "v_bfi_b32 v156, v28, 0, v26\n"
      "v_bfe_i32 v29, v25, 25, 1\n"
      "v_bfi_b32 v157, v29, 0, v26\n"
      "v_bfi_b32 v170, v27, v28, v29\n"
      "v_bfe_i32 v28, v25, 26, 1\n"
      "v_bfi_b32 v158, v28, 0, v26\n"
      "v_mfma_f32_16x16x32_bf16 v[120:123], a[220:223], v[232:235], v[120:123]\n"
      "v_mfma_f32_16x16x32_bf16 v[124:127], a[220:223], v[236:239], v[124:127]\n"
      "buffer_load_dwordx4 a[108:111], v1, s[48:51], s56 offen offset:1024 sc1\n"
      "v_bfe_i32 v29, v25, 27, 1\n"
      "v_bfi_b32 v159, v29, 0, v26\n"
      "v_bfi_b32 v171, v27, v28, v29\n"
      "v_bfe_i32 v28, v24, 24, 1\n"
      "s_waitcnt lgkmcnt(0)\n"
      "v_mfma_f32_16x16x32_bf16 v[112:115], a[224:227], v[240:243], v[112:115]\n"
      "v_mfma_f32_16x16x32_bf16 v[116:119], a[224:227], v[244:247], v[116:119]\n"
      "v_bfi_b32 v160, v28, 0, v26\n"
      "v_bfe_i32 v29, v24, 25, 1\n"
      "v_bfi_b32 v161, v29, 0, v26\n"
      "v_bfi_b32 v172, v27, v28, v29\n"
      "v_bfe_i32 v28, v24, 26, 1\n"
      "v_bfi_b32 v162, v28, 0, v26\n"
      "v_mfma_f32_16x16x32_bf16 v[120:123], a[228:231], v[240:243], v[120:123]\n"
      "v_mfma_f32_16x16x32_bf16 v[124:127], a[228:231], v[244:247], v[124:127]\n"
      "s_add_u32 s57, s69, 7168\n"
      "buffer_load_dwordx4 a[112:115], v2, s[48:51], s57 offen offset:0 sc1\n"
      "buffer_load_dwordx4 a[116:119], v2, s[48:51], s57 offen offset:1024 sc1\n"
      "v_bfe_i32 v29, v24, 27, 1\n"
      "v_bfi_b32 v163, v29, 0, v26\n"
      "v_mfma_f32_16x16x32_bf16 v[112:115], a[232:235], v[248:251], v[112:115]\n"
      "v_mfma_f32_16x16x32_bf16 v[116:119], a[232:235], v[252:255], v[116:119]\n"
      "v_bfi_b32 v173, v27, v28, v29\n"
      "v_and_b32 v184, v22, v44\n"
      "v_and_b32 v185, v22, v45\n"
      "v_bfi_b32 v186, v22, v46, v38\n"
      "v_bfi_b32 v187, v22, v47, v39\n"
      "v_bfi_b32 v44, v22, v52, v44\n"
      "v_mfma_f32_16x16x32_bf16 v[120:123], a[236:239], v[248:251], v[120:123]\n"
      "v_mfma_f32_16x16x32_bf16 v[124:127], a[236:239], v[252:255], v[124:127]\n"
      "v_bfi_b32 v45, v22, v53, v45\n"
      "v_and_b32 v46, v23, v46\n"
      "v_and_b32 v47, v23, v47\n"
      "v_and_b32 v38, v22, v38\n"
      "v_and_b32 v39, v22, v39\n"
      "v_and_b32 v52, v23, v52\n"
      "v_and_b32 v53, v23, v53\n"
      "buffer_load_dwordx4 a[120:123], v4, s[48:51], s70 offen offset:1792 sc1\n"
      "s_waitcnt vmcnt(29)\n"
      "v_mfma_f32_32x32x16_bf16 v[80:95], a[0:3], v[32:35], v[144:159]\n"
      "s_waitcnt vmcnt(22)\n"
      "s_waitcnt vmcnt(15)\n"
      "v_cvt_pk_bf16_f32 v176, v112, v113\n"
      "v_cvt_pk_bf16_f32 v177, v114, v115\n"
      "v_cvt_pk_bf16_f32 v178, v116, v117\n"
      "v_cvt_pk_bf16_f32 v179, v118, v119\n"
      "v_mfma_f32_32x32x16_bf16 v[96:111], a[8:11], v[32:35], v[148:163]\n"
      "v_cvt_pk_bf16_f32 v180, v120, v121\n"
      "v_cvt_pk_bf16_f32 v181, v122, v123\n"
      "v_cvt_pk_bf16_f32 v182, v124, v125\n"
      "v_cvt_pk_bf16_f32 v183, v126, v127\n"
      "v_permlane16_swap_b32 v176, v178\n"
      "v_permlane16_swap_b32 v177, v179\n"
      "buffer_load_dwordx4 a[124:127], v4, s[48:51], s71 offen offset:1792 sc1\n"
      "v_mfma_f32_32x32x16_bf16 v[80:95], a[4:7], v[36:39], v[80:95]\n"
      "v_permlane16_swap_b32 v180, v182\n"
      "v_permlane16_swap_b32 v181, v183\n"
      "v_and_b32 v188, v22, v72\n"
      "v_and_b32 v189, v22, v73\n"
      "v_bfi_b32 v190, v22, v74, v66\n"
      "v_bfi_b32 v191, v22, v75, v67\n"
      "v_mfma_f32_32x32x16_bf16 v[96:111], a[12:15], v[36:39], v[96:111]\n"
      "v_bfi_b32 v72, v22, v176, v72\n"
      "v_bfi_b32 v73, v22, v177, v73\n"
      "v_and_b32 v74, v23, v74\n"
      "v_and_b32 v75, v23, v75\n"
      "v_and_b32 v66, v22, v66\n"
      "v_max3_f32 v80, v80, v84, v88\n"
      "buffer_load_dwordx2 a[128:129], v4, s[48:51], s72 offen offset:1792 sc1\n"
      "v_max3_f32 v81, v81, v85, v89\n"
      "v_max3_f32 v82, v82, v86, v90\n"
      "v_max3_f32 v83, v83, v87, v91\n"
      "v_mfma_f32_32x32x16_bf16 v[192:207], a[26:29], v[40:43], v[144:159]\n"
      "v_and_b32 v67, v22, v67\n"
      "v_max3_f32 v80, v80, v92, v108\n"
      "v_max3_f32 v81, v81, v93, v109\n"
      "v_max3_f32 v82, v82, v94, v110\n"
      "v_max3_f32 v83, v83, v95, v111\n"
      "v_max_f32 v86, v80, v81\n"
      "v_max3_f32 v84, v86, v82, v83\n"
      "v_mov_b32 v85, v84\n"
      "v_mov_b32 v104, 0\n"
      "v_mov_b32 v105, 0\n"
      "v_permlane32_swap_b32 v84, v85\n"
      "v_max_f32 v84, v84, v85\n"
      "s_add_u32 s56, s68, 8704\n"
      "buffer_load_dwordx4 a[130:133], v1, s[48:51], s56 offen offset:512 sc1\n"
      "v_mul_f32 v84, v84, v20\n"
      "v_fma_f32 v80, v80, v20, -v84\n"
      "v_fma_f32 v81, v81, v20, -v84\n"
      "v_fma_f32 v82, v82, v20, -v84\n"
      "v_fma_f32 v83, v83, v20, -v84\n"
      "v_exp_f32 v80, v80\n"
      "v_exp_f32 v81, v81\n"
      "v_exp_f32 v82, v82\n"
      "v_exp_f32 v83, v83\n"
      "v_add_f32 v86, v80, v81\n"
      "v_add_f32 v87, v82, v83\n"
      "buffer_load_dwordx4 a[134:137], v7, s[48:51], s56 offen offset:0 sc1\n"
      "v_add_f32 v84, v86, v87\n"
      "v_add_f32 v85, v86, v87\n"
      "v_mfma_f32_32x32x16_bf16 v[208:223], a[34:37], v[40:43], v[148:163]\n"
      "v_and_b32 v176, v23, v176\n"
      "v_permlane32_swap_b32 v84, v85\n"
      "v_add_f32 v84, v84, v85\n"
      "v_rcp_f32 v84, v84\n"
      "v_and_b32 v177, v23, v177\n"
      "v_mul_f32 v80, v80, v84\n"
      "v_mul_f32 v81, v81, v84\n"
      "v_mul_f32 v82, v82, v84\n"
      "v_mul_f32 v83, v83, v84\n"
      "v_cvt_pk_bf16_f32 v84, v80, v81\n"
      "v_cvt_pk_bf16_f32 v85, v82, v83\n"
      "v_and_b32 v96, v164, v84\n"
      "s_add_u32 s57, s69, 8704\n"
      "buffer_load_dwordx4 a[138:141], v2, s[48:51], s57 offen offset:512 sc1\n"
      "buffer_load_dwordx4 a[142:145], v8, s[48:51], s57 offen offset:0 sc1\n"
      "v_and_b32 v97, v165, v85\n"
      "v_and_b32 v98, v166, v84\n"
      "v_and_b32 v99, v167, v85\n"
      "v_and_b32 v100, v168, v84\n"
      "v_and_b32 v101, v169, v85\n"
      "v_and_b32 v102, v170, v84\n"
      "v_and_b32 v103, v171, v85\n"
      "v_and_b32 v106, v172, v84\n"
      "v_and_b32 v107, v173, v85\n"
      "s_waitcnt vmcnt(32)\n"
      "v_mfma_f32_32x32x16_bf16 v[80:95], a[16:19], v[96:99], 0\n"
      "s_waitcnt vmcnt(14)\n"
      "buffer_load_dwordx4 a[146:149], v4, s[48:51], s70 offen offset:2176 sc1\n"
      "v_mfma_f32_32x32x16_bf16 v[80:95], a[20:23], v[100:103], v[80:95]\n"
      "v_mfma_f32_32x32x16_bf16 v[80:95], a[22:25], v[104:107], v[80:95]\n"
      "v_mfma_f32_32x32x16_bf16 v[192:207], a[30:33], v[184:187], v[192:207]\n"
      "buffer_load_dwordx4 a[150:153], v4, s[48:51], s71 offen offset:2176 sc1\n"
      "v_mfma_f32_32x32x16_bf16 v[208:223], a[38:41], v[184:187], v[208:223]\n"
      "v_mfma_f32_32x32x16_bf16 v[224:239], a[52:55], v[48:51], v[144:159]\n"
      "buffer_load_dwordx2 a[154:155], v4, s[48:51], s72 offen offset:2176 sc1\n"
      "v_mfma_f32_32x32x16_bf16 v[240:255], a[60:63], v[48:51], v[148:163]\n"
      "s_nop 5\n"
      "v_cvt_pk_bf16_f32 v32, v80, v81\n"
      "v_mfma_f32_32x32x16_bf16 v[224:239], a[56:59], v[44:47], v[224:239]\n"
      "s_add_u32 s56, s68, 10752\n"
      "buffer_load_dwordx4 a[156:159], v1, s[48:51], s56 offen offset:512 sc1\n"
      "v_cvt_pk_bf16_f32 v33, v82, v83\n"
      "v_mfma_f32_32x32x16_bf16 v[240:255], a[64:67], v[44:47], v[240:255]\n"
      "v_cvt_pk_bf16_f32 v34, v84, v85\n"
      "v_cvt_pk_bf16_f32 v35, v86, v87\n"
      "v_cvt_pk_bf16_f32 v36, v88, v89\n"
      "v_cvt_pk_bf16_f32 v37, v90, v91\n"
      "v_cvt_pk_bf16_f32 v38, v92, v93\n"
      "v_cvt_pk_bf16_f32 v39, v94, v95\n"
      "v_max3_f32 v192, v192, v196, v200\n"
      "v_max3_f32 v193, v193, v197, v201\n"
      "v_max3_f32 v194, v194, v198, v202\n"
      "v_max3_f32 v195, v195, v199, v203\n"
      "buffer_load_dwordx4 a[160:163], v7, s[48:51], s56 offen offset:0 sc1\n"
      "v_max3_f32 v192, v192, v204, v220\n"
      "v_max3_f32 v193, v193, v205, v221\n"
      "v_max3_f32 v194, v194, v206, v222\n"
      "v_max3_f32 v195, v195, v207, v223\n"
      "v_max_f32 v198, v192, v193\n"
      "v_max3_f32 v196, v198, v194, v195\n"
      "v_mov_b32 v197, v196\n"
      "v_mov_b32 v216, 0\n"
      "v_mov_b32 v217, 0\n"
      "v_permlane32_swap_b32 v196, v197\n"
      "v_max_f32 v196, v196, v197\n"
      "v_mul_f32 v196, v196, v20\n"
      "v_fma_f32 v192, v192, v20, -v196\n"
      "v_fma_f32 v193, v193, v20, -v196\n"
      "v_fma_f32 v194, v194, v20, -v196\n"
      "v_fma_f32 v195, v195, v20, -v196\n"
      "v_exp_f32 v192, v192\n"
      "s_add_u32 s57, s69, 10752\n"
      "buffer_load_dwordx4 a[164:167], v2, s[48:51], s57 offen offset:512 sc1\n"
      "buffer_load_dwordx4 a[168:171], v8, s[48:51], s57 offen offset:0 sc1\n"
      "v_exp_f32 v193, v193\n"
      "v_exp_f32 v194, v194\n"
      "v_exp_f32 v195, v195\n"
      "v_add_f32 v198, v192, v193\n"
      "v_add_f32 v199, v194, v195\n"
      "v_add_f32 v196, v198, v199\n"
      "v_add_f32 v197, v198, v199\n"
      "v_max3_f32 v224, v224, v228, v232\n"
      "v_max3_f32 v225, v225, v229, v233\n"
      "buffer_load_dwordx4 a[172:175], v4, s[48:51], s70 offen offset:2688 sc1\n"
      "v_permlane32_swap_b32 v196, v197\n"
      "v_add_f32 v196, v196, v197\n"
      "v_rcp_f32 v196, v196\n"
      "v_max3_f32 v226, v226, v230, v234\n"
      "v_mul_f32 v192, v192, v196\n"
      "v_mul_f32 v193, v193, v196\n"
      "v_mul_f32 v194, v194, v196\n"
      "v_mul_f32 v195, v195, v196\n"
      "v_cvt_pk_bf16_f32 v196, v192, v193\n"
      "v_cvt_pk_bf16_f32 v197, v194, v195\n"
      "v_and_b32 v208, v164, v196\n"
      "v_and_b32 v209, v165, v197\n"
      "v_and_b32 v210, v166, v196\n"
      "v_and_b32 v211, v167, v197\n"
      "v_and_b32 v212, v168, v196\n"
      "v_and_b32 v213, v169, v197\n"
      "buffer_load_dwordx4 a[176:179], v4, s[48:51], s71 offen offset:2688 sc1\n"
      "v_and_b32 v214, v170, v196\n"
      "v_and_b32 v215, v171, v197\n"
      "v_and_b32 v218, v172, v196\n"
      "v_and_b32 v219, v173, v197\n"
      "s_waitcnt vmcnt(34)\n"
      "v_mfma_f32_32x32x16_bf16 v[192:207], a[42:45], v[208:211], 0\n"
      "v_max3_f32 v227, v227, v231, v235\n"
      "v_max3_f32 v224, v224, v236, v252\n"
      "v_max3_f32 v225, v225, v237, v253\n"
      "v_max3_f32 v226, v226, v238, v254\n"
      "v_max3_f32 v227, v227, v239, v255\n"
      "v_max_f32 v230, v224, v225\n"
      "v_mfma_f32_32x32x16_bf16 v[192:207], a[46:49], v[212:215], v[192:207]\n"
      "v_max3_f32 v228, v230, v226, v227\n"
      "buffer_load_dwordx2 a[180:181], v4, s[48:51], s72 offen offset:2688 sc1\n"
      "v_mov_b32 v229, v228\n"
      "v_mov_b32 v248, 0\n"
      "v_mov_b32 v249, 0\n"
      "v_mfma_f32_32x32x16_bf16 v[192:207], a[48:51], v[216:219], v[192:207]\n"
      "v_permlane32_swap_b32 v228, v229\n"
      "v_max_f32 v228, v228, v229\n"
      "v_mul_f32 v228, v228, v20\n"
      "v_fma_f32 v224, v224, v20, -v228\n"
      "v_fma_f32 v225, v225, v20, -v228\n"
      "v_fma_f32 v226, v226, v20, -v228\n"
      "v_fma_f32 v227, v227, v20, -v228\n"
      "v_exp_f32 v224, v224\n"
      "v_exp_f32 v225, v225\n"
      "v_exp_f32 v226, v226\n"
      "s_add_u32 s56, s68, 12288\n"
      "buffer_load_dwordx4 a[182:185], v1, s[48:51], s56 offen offset:0 sc1\n"
      ".LPAD12:\n"
      "s_cbranch_execz .LRET\n"
      "v_cvt_pk_bf16_f32 v40, v192, v193\n"
      "v_cvt_pk_bf16_f32 v41, v194, v195\n"
      "v_cvt_pk_bf16_f32 v42, v196, v197\n"
      "v_cvt_pk_bf16_f32 v43, v198, v199\n"
      "v_cvt_pk_bf16_f32 v184, v200, v201\n"
      "v_cvt_pk_bf16_f32 v185, v202, v203\n"
      "v_cvt_pk_bf16_f32 v186, v204, v205\n"
      "v_cvt_pk_bf16_f32 v187, v206, v207\n"
      "v_permlane32_swap_b32 v36, v40\n"
      "v_permlane32_swap_b32 v37, v41\n"
      "s_add_u32 s75, s82, 0\n"
      "v_exp_f32 v227, v227\n"
      "v_add_f32 v230, v224, v225\n"
      "buffer_load_dwordx4 a[186:189], v1, s[48:51], s56 offen offset:1024 sc1\n"
      "v_add_f32 v231, v226, v227\n"
      "v_add_f32 v228, v230, v231\n"
      "v_add_f32 v229, v230, v231\n"
      "v_mfma_f32_32x32x16_bf16 v[112:127], a[78:81], v[52:55], v[144:159]\n"
      "s_waitcnt vmcnt(19)\n"
      "v_permlane32_swap_b32 v228, v229\n"
      "v_add_f32 v228, v228, v229\n"
      "v_rcp_f32 v228, v228\n"
      "s_nop 0\n"
      "v_mul_f32 v224, v224, v228\n"
      "v_mul_f32 v225, v225, v228\n"
      "v_mul_f32 v226, v226, v228\n"
      "v_mul_f32 v227, v227, v228\n"
      "v_cvt_pk_bf16_f32 v228, v224, v225\n"
      "v_cvt_pk_bf16_f32 v229, v226, v227\n"
      "s_add_u32 s57, s69, 12288\n"
      "buffer_load_dwordx4 a[190:193], v2, s[48:51], s57 offen offset:0 sc1\n"
      "buffer_load_dwordx4 a[194:197], v2, s[48:51], s57 offen offset:1024 sc1\n"
      "v_and_b32 v240, v164, v228\n"
      "v_and_b32 v241, v165, v229\n"
      "v_and_b32 v242, v166, v228\n"
      "v_and_b32 v243, v167, v229\n"
      "v_and_b32 v244, v168, v228\n"
      "v_and_b32 v245, v169, v229\n"
      "v_and_b32 v246, v170, v228\n"
      "v_and_b32 v247, v171, v229\n"
      "v_and_b32 v250, v172, v228\n"
      "v_and_b32 v251, v173, v229\n"
      "s_waitcnt vmcnt(32)\n"
      "v_mfma_f32_32x32x16_bf16 v[224:239], a[68:71], v[240:243], 0\n"
      "buffer_load_dwordx4 a[198:201], v4, s[48:51], s70 offen offset:3072 sc1\n"
      "v_mfma_f32_32x32x16_bf16 v[224:239], a[72:75], v[244:247], v[224:239]\n"
      "s_waitcnt vmcnt(15)\n"
      "v_mfma_f32_32x32x16_bf16 v[224:239], a[74:77], v[248:251], v[224:239]\n"
      "v_mfma_f32_32x32x16_bf16 v[128:143], a[86:89], v[52:55], v[148:163]\n"
      "buffer_load_dwordx4 a[202:205], v4, s[48:51], s71 offen offset:3072 sc1\n"
      "v_mfma_f32_32x32x16_bf16 v[112:127], a[82:85], v[56:59], v[112:127]\n"
      "v_mfma_f32_32x32x16_bf16 v[128:143], a[90:93], v[56:59], v[128:143]\n"
      "buffer_load_dwordx2 a[206:207], v4, s[48:51], s72 offen offset:3072 sc1\n"
      "buffer_store_dwordx4 v[32:35], v1, s[48:51], s75 offen offset:0 sc1\n"
      "v_mfma_f32_32x32x16_bf16 v[80:95], a[104:107], v[60:63], v[144:159]\n"
      "s_nop 4\n"
      "v_cvt_pk_bf16_f32 v48, v224, v225\n"
      "v_mfma_f32_32x32x16_bf16 v[96:111], a[112:115], v[60:63], v[148:163]\n"
      "v_cvt_pk_bf16_f32 v49, v226, v227\n"
      "v_cvt_pk_bf16_f32 v50, v228, v229\n"
      "buffer_store_dwordx4 v[36:39], v15, s[48:51], s75 offen offset:1024 sc1\n"
      "v_cvt_pk_bf16_f32 v51, v230, v231\n"
      "v_cvt_pk_bf16_f32 v44, v232, v233\n"
      "v_cvt_pk_bf16_f32 v45, v234, v235\n"
      "v_cvt_pk_bf16_f32 v46, v236, v237\n"
      "v_cvt_pk_bf16_f32 v47, v238, v239\n"
      "v_max3_f32 v112, v112, v116, v120\n"
      "v_max3_f32 v113, v113, v117, v121\n"
      "v_max3_f32 v114, v114, v118, v122\n"
      "v_max3_f32 v115, v115, v119, v123\n"
      "v_max3_f32 v112, v112, v124, v140\n"
      "v_max3_f32 v113, v113, v125, v141\n"
      "v_max3_f32 v114, v114, v126, v142\n"
      "v_max3_f32 v115, v115, v127, v143\n"
      "v_max_f32 v118, v112, v113\n"
      "v_max3_f32 v116, v118, v114, v115\n"
      "v_mov_b32 v117, v116\n"
      "v_mov_b32 v136, 0\n"
      "v_mov_b32 v137, 0\n"
      "buffer_store_dwordx4 v[40:43], v1, s[48:51], s75 offen offset:1536 sc1\n"
      "v_permlane32_swap_b32 v116, v117\n"
      "v_max_f32 v116, v116, v117\n"
      "v_mul_f32 v116, v116, v20\n"
      "v_fma_f32 v112, v112, v20, -v116\n"
      "v_fma_f32 v113, v113, v20, -v116\n"
      "v_fma_f32 v114, v114, v20, -v116\n"
      "v_fma_f32 v115, v115, v20, -v116\n"
      "v_exp_f32 v112, v112\n"
      "v_exp_f32 v113, v113\n"
      "v_exp_f32 v114, v114\n"
      "v_exp_f32 v115, v115\n"
      "v_add_f32 v118, v112, v113\n"
      "v_add_f32 v119, v114, v115\n"
      "v_add_f32 v116, v118, v119\n"
      "buffer_store_dwordx4 v[184:187], v1, s[48:51], s75 offen offset:2560 sc1\n"
      "v_add_f32 v117, v118, v119\n"
      "v_mfma_f32_32x32x16_bf16 v[80:95], a[108:111], v[64:67], v[80:95]\n"
      "s_nop 0\n"
      "v_permlane32_swap_b32 v116, v117\n"
      "v_add_f32 v116, v116, v117\n"
      "v_rcp_f32 v116, v116\n"
      "s_nop 0\n"
      "v_mul_f32 v112, v112, v116\n"
      "v_mul_f32 v113, v113, v116\n"
      "v_mul_f32 v114, v114, v116\n"
      "v_mul_f32 v115, v115, v116\n"
      "v_cvt_pk_bf16_f32 v116, v112, v113\n"
      "v_cvt_pk_bf16_f32 v117, v114, v115\n"
      "v_and_b32 v128, v164, v116\n"
      "v_and_b32 v129, v165, v117\n"
      "v_and_b32 v130, v166, v116\n"
      "v_and_b32 v131, v167, v117\n"
      "v_and_b32 v132, v168, v116\n"
      "v_and_b32 v133, v169, v117\n"
      "v_and_b32 v134, v170, v116\n"
      "v_and_b32 v135, v171, v117\n"
      "v_and_b32 v138, v172, v116\n"
      "v_and_b32 v139, v173, v117\n"
      "s_waitcnt vmcnt(32)\n"
      "v_mfma_f32_32x32x16_bf16 v[112:127], a[94:97], v[128:131], 0\n"
      "s_waitcnt vmcnt(14)\n"
      "v_mfma_f32_32x32x16_bf16 v[112:127], a[98:101], v[132:135], v[112:127]\n"
      "v_mfma_f32_32x32x16_bf16 v[112:127], a[100:103], v[136:139], v[112:127]\n"
      "v_mfma_f32_32x32x16_bf16 v[96:111], a[116:119], v[64:67], v[96:111]\n"
      "v_max3_f32 v80, v80, v84, v88\n"
      "v_max3_f32 v81, v81, v85, v89\n"
      "v_max3_f32 v82, v82, v86, v90\n"
      "v_max3_f32 v83, v83, v87, v91\n"
      "v_mfma_f32_32x32x16_bf16 v[192:207], a[130:133], v[68:71], v[144:159]\n"
      "s_nop 5\n"
      "v_cvt_pk_bf16_f32 v52, v112, v113\n"
      "v_mfma_f32_32x32x16_bf16 v[208:223], a[138:141], v[68:71], v[148:163]\n"
      "v_cvt_pk_bf16_f32 v53, v114, v115\n"
      "v_mfma_f32_32x32x16_bf16 v[192:207], a[134:137], v[188:191], v[192:207]\n"
      "v_cvt_pk_bf16_f32 v54, v116, v117\n"
      "v_cvt_pk_bf16_f32 v55, v118, v119\n"
      "v_cvt_pk_bf16_f32 v56, v120, v121\n"
      "v_cvt_pk_bf16_f32 v57, v122, v123\n"
      "v_cvt_pk_bf16_f32 v58, v124, v125\n"
      "v_cvt_pk_bf16_f32 v59, v126, v127\n"
      "v_permlane32_swap_b32 v44, v52\n"
      "v_permlane32_swap_b32 v45, v53\n"
      "s_add_u32 s75, s82, 3584\n"
      "buffer_store_dwordx4 v[48:51], v1, s[48:51], s75 offen offset:0 sc1\n"
      "v_max3_f32 v80, v80, v92, v108\n"
      "v_max3_f32 v81, v81, v93, v109\n"
      "v_max3_f32 v82, v82, v94, v110\n"
      "v_max3_f32 v83, v83, v95, v111\n"
      "v_max_f32 v86, v80, v81\n"
      "v_max3_f32 v84, v86, v82, v83\n"
      "v_mov_b32 v85, v84\n"
      "v_mov_b32 v104, 0\n"
      "v_mov_b32 v105, 0\n"
      "v_permlane32_swap_b32 v84, v85\n"
      "v_max_f32 v84, v84, v85\n"
      "v_mul_f32 v84, v84, v20\n"
      "v_fma_f32 v80, v80, v20, -v84\n"
      "v_fma_f32 v81, v81, v20, -v84\n"
      "v_fma_f32 v82, v82, v20, -v84\n"
      "v_fma_f32 v83, v83, v20, -v84\n"
      "v_exp_f32 v80, v80\n"
      "buffer_store_dwordx4 v[44:47], v15, s[48:51], s75 offen offset:1024 sc1\n"
      "v_exp_f32 v81, v81\n"
      "v_exp_f32 v82, v82\n"
      "v_exp_f32 v83, v83\n"
      "v_add_f32 v86, v80, v81\n"
      "v_add_f32 v87, v82, v83\n"
      "v_add_f32 v84, v86, v87\n"
      "v_add_f32 v85, v86, v87\n"
      "v_mfma_f32_32x32x16_bf16 v[208:223], a[142:145], v[188:191], v[208:223]\n"
      "v_max3_f32 v192, v192, v196, v200\n"
      "v_permlane32_swap_b32 v84, v85\n"
      "v_add_f32 v84, v84, v85\n"
      "v_rcp_f32 v84, v84\n"
      "v_max3_f32 v193, v193, v197, v201\n"
      "buffer_store_dwordx4 v[52:55], v1, s[48:51], s75 offen offset:1536 sc1\n"
      "v_mul_f32 v80, v80, v84\n"
      "v_mul_f32 v81, v81, v84\n"
      "v_mul_f32 v82, v82, v84\n"
      "v_mul_f32 v83, v83, v84\n"
      "v_cvt_pk_bf16_f32 v84, v80, v81\n"
      "v_cvt_pk_bf16_f32 v85, v82, v83\n"
      "v_and_b32 v96, v164, v84\n"
      "v_and_b32 v97, v165, v85\n"
      "v_and_b32 v98, v166, v84\n"
      "v_and_b32 v99, v167, v85\n"
      "v_and_b32 v100, v168, v84\n"
      "v_and_b32 v101, v169, v85\n"
      "v_and_b32 v102, v170, v84\n"
      "v_and_b32 v103, v171, v85\n"
      "v_and_b32 v106, v172, v84\n"
      "v_and_b32 v107, v173, v85\n"
      "s_waitcnt vmcnt(28)\n"
      "v_mfma_f32_32x32x16_bf16 v[80:95], a[120:123], v[96:99], 0\n"
      "buffer_store_dwordx4 v[56:59], v1, s[48:51], s75 offen offset:2560 sc1\n"
      "v_max3_f32 v194, v194, v198, v202\n"
      "v_max3_f32 v195, v195, v199, v203\n"
      "v_max3_f32 v192, v192, v204, v220\n"
      "v_max3_f32 v193, v193, v205, v221\n"
      "v_mfma_f32_32x32x16_bf16 v[80:95], a[124:127], v[100:103], v[80:95]\n"
      "v_max3_f32 v194, v194, v206, v222\n"
      "v_max3_f32 v195, v195, v207, v223\n"
      "v_max_f32 v198, v192, v193\n"
      "v_max3_f32 v196, v198, v194, v195\n"
      "v_mov_b32 v197, v196\n"
      "v_mov_b32 v216, 0\n"
      "v_mfma_f32_32x32x16_bf16 v[80:95], a[126:129], v[104:107], v[80:95]\n"
      "v_mov_b32 v217, 0\n"
      "v_permlane32_swap_b32 v196, v197\n"
      "v_max_f32 v196, v196, v197\n"
      "v_mul_f32 v196, v196, v20\n"
      "v_fma_f32 v192, v192, v20, -v196\n"
      "v_fma_f32 v193, v193, v20, -v196\n"
      "v_fma_f32 v194, v194, v20, -v196\n"
      "v_fma_f32 v195, v195, v20, -v196\n"
      "v_exp_f32 v192, v192\n"
      "v_exp_f32 v193, v193\n"
      "v_exp_f32 v194, v194\n"
      "v_exp_f32 v195, v195\n"
      "v_cvt_pk_bf16_f32 v60, v80, v81\n"
      "v_cvt_pk_bf16_f32 v61, v82, v83\n"
      "v_cvt_pk_bf16_f32 v62, v84, v85\n"
      "v_cvt_pk_bf16_f32 v63, v86, v87\n"
      "v_cvt_pk_bf16_f32 v64, v88, v89\n"
      "v_cvt_pk_bf16_f32 v65, v90, v91\n"
      "v_cvt_pk_bf16_f32 v66, v92, v93\n"
      "v_cvt_pk_bf16_f32 v67, v94, v95\n"
      "v_add_f32 v198, v192, v193\n"
      "v_add_f32 v199, v194, v195\n"
      "v_add_f32 v196, v198, v199\n"
      "v_add_f32 v197, v198, v199\n"
      "v_mfma_f32_32x32x16_bf16 v[224:239], a[156:159], v[76:79], v[144:159]\n"
      "s_waitcnt vmcnt(11)\n"
      "v_permlane32_swap_b32 v196, v197\n"
      "v_add_f32 v196, v196, v197\n"
      "v_rcp_f32 v196, v196\n"
      "s_nop 0\n"
      "v_mul_f32 v192, v192, v196\n"
      "v_mul_f32 v193, v193, v196\n"
      "v_mul_f32 v194, v194, v196\n"
      "v_mul_f32 v195, v195, v196\n"
      "v_cvt_pk_bf16_f32 v196, v192, v193\n"
      "v_cvt_pk_bf16_f32 v197, v194, v195\n"
      "v_and_b32 v208, v164, v196\n"
      "v_and_b32 v209, v165, v197\n"
      "v_and_b32 v210, v166, v196\n"
      "v_and_b32 v211, v167, v197\n"
      "v_and_b32 v212, v168, v196\n"
      "v_and_b32 v213, v169, v197\n"
      "v_and_b32 v214, v170, v196\n"
      "v_and_b32 v215, v171, v197\n"
      "v_and_b32 v218, v172, v196\n"
      "v_and_b32 v219, v173, v197\n"
      "s_waitcnt vmcnt(22)\n"
      "v_mfma_f32_32x32x16_bf16 v[192:207], a[146:149], v[208:211], 0\n"
      "v_mfma_f32_32x32x16_bf16 v[192:207], a[150:153], v[212:215], v[192:207]\n"
      "v_mfma_f32_32x32x16_bf16 v[192:207], a[152:155], v[216:219], v[192:207]\n"
      "v_mfma_f32_32x32x16_bf16 v[240:255], a[164:167], v[76:79], v[148:163]\n"
      "v_mfma_f32_32x32x16_bf16 v[224:239], a[160:163], v[72:75], v[224:239]\n"
      "v_mfma_f32_32x32x16_bf16 v[240:255], a[168:171], v[72:75], v[240:255]\n"
      "v_mfma_f32_32x32x16_bf16 v[112:127], a[182:185], v[176:179], v[144:159]\n"
      "v_mfma_f32_32x32x16_bf16 v[128:143], a[190:193], v[176:179], v[148:163]\n"
      "v_mfma_f32_32x32x16_bf16 v[112:127], a[186:189], v[180:183], v[112:127]\n"
      "s_nop 5\n"
      "v_cvt_pk_bf16_f32 v68, v192, v193\n"
      "v_mfma_f32_32x32x16_bf16 v[128:143], a[194:197], v[180:183], v[128:143]\n"
      "v_cvt_pk_bf16_f32 v69, v194, v195\n"
      "v_cvt_pk_bf16_f32 v70, v196, v197\n"
      "v_cvt_pk_bf16_f32 v71, v198, v199\n"
      "v_cvt_pk_bf16_f32 v188, v200, v201\n"
      "v_cvt_pk_bf16_f32 v189, v202, v203\n"
      "v_cvt_pk_bf16_f32 v190, v204, v205\n"
      "v_cvt_pk_bf16_f32 v191, v206, v207\n"
      "v_permlane32_swap_b32 v64, v68\n"
      "v_permlane32_swap_b32 v65, v69\n"
      "s_add_u32 s75, s82, 7168\n"
      "buffer_store_dwordx4 v[60:63], v1, s[48:51], s75 offen offset:0 sc1\n"
      "v_max3_f32 v224, v224, v228, v232\n"
      "v_max3_f32 v225, v225, v229, v233\n"
      "v_max3_f32 v226, v226, v230, v234\n"
      "v_max3_f32 v227, v227, v231, v235\n"
      "v_max3_f32 v224, v224, v236, v252\n"
      "v_max3_f32 v225, v225, v237, v253\n"
      "v_max3_f32 v226, v226, v238, v254\n"
      "v_max3_f32 v227, v227, v239, v255\n"
      "v_max_f32 v230, v224, v225\n"
      "v_max3_f32 v228, v230, v226, v227\n"
      "v_mov_b32 v229, v228\n"
      "v_mov_b32 v248, 0\n"
      "v_mov_b32 v249, 0\n"
      "v_permlane32_swap_b32 v228, v229\n"
      "v_max_f32 v228, v228, v229\n"
      "v_mul_f32 v228, v228, v20\n"
      "v_fma_f32 v224, v224, v20, -v228\n"
      "v_fma_f32 v225, v225, v20, -v228\n"
      "buffer_store_dwordx4 v[64:67], v15, s[48:51], s75 offen offset:1024 sc1\n"
      "v_fma_f32 v226, v226, v20, -v228\n"
      "v_fma_f32 v227, v227, v20, -v228\n"
      "v_exp_f32 v224, v224\n"
      "v_exp_f32 v225, v225\n"
      "v_exp_f32 v226, v226\n"
      "v_exp_f32 v227, v227\n"
      "v_add_f32 v230, v224, v225\n"
      "v_add_f32 v231, v226, v227\n"
      "v_add_f32 v228, v230, v231\n"
      "v_add_f32 v229, v230, v231\n"
      "v_max3_f32 v112, v112, v116, v120\n"
      "v_max3_f32 v113, v113, v117, v121\n"
      "v_permlane32_swap_b32 v228, v229\n"
      "v_add_f32 v228, v228, v229\n"
      "buffer_store_dwordx4 v[68:71], v1, s[48:51], s75 offen offset:1536 sc1\n"
      "v_rcp_f32 v228, v228\n"
      "v_max3_f32 v114, v114, v118, v122\n"
      "v_mul_f32 v224, v224, v228\n"
      "v_mul_f32 v225, v225, v228\n"
      "v_mul_f32 v226, v226, v228\n"
      "v_mul_f32 v227, v227, v228\n"
      "v_cvt_pk_bf16_f32 v228, v224, v225\n"
      "v_cvt_pk_bf16_f32 v229, v226, v227\n"
      "v_and_b32 v240, v164, v228\n"
      "v_and_b32 v241, v165, v229\n"
      "v_and_b32 v242, v166, v228\n"
      "v_and_b32 v243, v167, v229\n"
      "v_and_b32 v244, v168, v228\n"
      "v_and_b32 v245, v169, v229\n"
      "v_and_b32 v246, v170, v228\n"
      "v_and_b32 v247, v171, v229\n"
      "v_and_b32 v250, v172, v228\n"
      "buffer_store_dwordx4 v[188:191], v1, s[48:51], s75 offen offset:2560 sc1\n"
      "v_and_b32 v251, v173, v229\n"
      "s_waitcnt vmcnt(19)\n"
      "v_mfma_f32_32x32x16_bf16 v[224:239], a[172:175], v[240:243], 0\n"
      "v_max3_f32 v115, v115, v119, v123\n"
      "v_max3_f32 v112, v112, v124, v140\n"
      "v_max3_f32 v113, v113, v125, v141\n"
      "v_max3_f32 v114, v114, v126, v142\n"
      "v_max3_f32 v115, v115, v127, v143\n"
      "v_max_f32 v118, v112, v113\n"
      "v_mfma_f32_32x32x16_bf16 v[224:239], a[176:179], v[244:247], v[224:239]\n"
      "v_max3_f32 v116, v118, v114, v115\n"
      "v_mov_b32 v117, v116\n"
      "v_mov_b32 v136, 0\n"
      "v_mov_b32 v137, 0\n"
      "v_permlane32_swap_b32 v116, v117\n"
      "v_max_f32 v116, v116, v117\n"
      "v_mfma_f32_32x32x16_bf16 v[224:239], a[178:181], v[248:251], v[224:239]\n"
      "v_mul_f32 v116, v116, v20\n"
      "v_fma_f32 v112, v112, v20, -v116\n"
      "v_fma_f32 v113, v113, v20, -v116\n"
      "v_fma_f32 v114, v114, v20, -v116\n"
      "v_fma_f32 v115, v115, v20, -v116\n"
      "v_exp_f32 v112, v112\n"
      "v_exp_f32 v113, v113\n"
      "v_exp_f32 v114, v114\n"
      "v_exp_f32 v115, v115\n"
      "v_add_f32 v118, v112, v113\n"
      "v_add_f32 v119, v114, v115\n"
      "v_add_f32 v116, v118, v119\n"
      "v_cvt_pk_bf16_f32 v76, v224, v225\n"
      "v_cvt_pk_bf16_f32 v77, v226, v227\n"
      "v_cvt_pk_bf16_f32 v78, v228, v229\n"
      "v_cvt_pk_bf16_f32 v79, v230, v231\n"
      "v_cvt_pk_bf16_f32 v72, v232, v233\n"
      "v_cvt_pk_bf16_f32 v73, v234, v235\n"
      "v_cvt_pk_bf16_f32 v74, v236, v237\n"
      "v_cvt_pk_bf16_f32 v75, v238, v239\n"
      "v_add_f32 v117, v118, v119\n"
      "s_nop 1\n"
      "v_permlane32_swap_b32 v116, v117\n"
      "v_add_f32 v116, v116, v117\n"
      "v_rcp_f32 v116, v116\n"
      "s_nop 0\n"
      "v_mul_f32 v112, v112, v116\n"
      "v_mul_f32 v113, v113, v116\n"
      "v_mul_f32 v114, v114, v116\n"
      "v_mul_f32 v115, v115, v116\n"
      "v_cvt_pk_bf16_f32 v116, v112, v113\n"
      "v_cvt_pk_bf16_f32 v117, v114, v115\n"
      "v_and_b32 v128, v164, v116\n"
      "v_and_b32 v129, v165, v117\n"
      "v_and_b32 v130, v166, v116\n"
      "v_and_b32 v131, v167, v117\n"
      "v_and_b32 v132, v168, v116\n"
      "v_and_b32 v133, v169, v117\n"
      "v_and_b32 v134, v170, v116\n"
      "v_and_b32 v135, v171, v117\n"
      "v_and_b32 v138, v172, v116\n"
      "v_and_b32 v139, v173, v117\n"
      "s_waitcnt vmcnt(12)\n"
      "v_mfma_f32_32x32x16_bf16 v[112:127], a[198:201], v[128:131], 0\n"
      "v_mfma_f32_32x32x16_bf16 v[112:127], a[202:205], v[132:135], v[112:127]\n"
      "v_mfma_f32_32x32x16_bf16 v[112:127], a[204:207], v[136:139], v[112:127]\n"
      "s_nop 11\n"
      "v_cvt_pk_bf16_f32 v176, v112, v113\n"
      "v_cvt_pk_bf16_f32 v177, v114, v115\n"
      "v_cvt_pk_bf16_f32 v178, v116, v117\n"
      "v_cvt_pk_bf16_f32 v179, v118, v119\n"
      "v_cvt_pk_bf16_f32 v180, v120, v121\n"
      "v_cvt_pk_bf16_f32 v181, v122, v123\n"
      "v_cvt_pk_bf16_f32 v182, v124, v125\n"
      "v_cvt_pk_bf16_f32 v183, v126, v127\n"
      "v_permlane32_swap_b32 v72, v176\n"
      "v_permlane32_swap_b32 v73, v177\n"
      "s_add_u32 s75, s82, 10752\n"
      "buffer_store_dwordx4 v[76:79], v1, s[48:51], s75 offen offset:0 sc1\n"
      "buffer_store_dwordx4 v[72:75], v15, s[48:51], s75 offen offset:1024 sc1\n"
      "buffer_store_dwordx4 v[176:179], v1, s[48:51], s75 offen offset:1536 sc1\n"
      "buffer_store_dwordx4 v[180:183], v1, s[48:51], s75 offen offset:2560 sc1\n"
      "s_waitcnt vmcnt(0)\n"
      "buffer_store_dwordx2 v[16:17], v13, s[48:51], s89 offen offset:896 sc1\n"
      "s_or_b32 s56, s53, s52\n"
      "s_cmp_lg_u32 s56, 0\n"
      "s_cbranch_scc1 .LAEND\n"
      "v_and_b32 v32, 15, v0\n"
      "v_min_u32 v32, 13, v32\n"
      "v_lshlrev_b32 v32, 3, v32\n"
      "v_lshrrev_b32 v33, 4, v0\n"
      "v_lshl_add_u32 v32, v33, 13, v32\n"
      "v_and_b32 v33, 31, v0\n"
      "v_lshlrev_b32 v33, 14, v33\n"
      "s_mov_b32 s56, 131072\n"
      "v_mov_b32 v18, 0\n"
      "v_mov_b32 v19, 0\n"
      "buffer_atomic_or_x2 v[18:19], v32, s[48:51], s56 offen offset:0 sc0 sc1\n"
      "s_waitcnt vmcnt(0)\n"
      "v_cmp_ne_u64 vcc, s[96:97], v[18:19]\n"
      "s_and_b64 vcc, vcc, -1\n"
      "s_cbranch_scc1 .LS_E0\n"
      ".LK_E0:\n"
      "s_mov_b32 s56, 163840\n"
      "v_mov_b32 v18, 0\n"
      "v_mov_b32 v19, 0\n"
      "buffer_atomic_or_x2 v[18:19], v32, s[48:51], s56 offen offset:0 sc0 sc1\n"
      "s_waitcnt vmcnt(0)\n"
      "v_cmp_ne_u64 vcc, s[96:97], v[18:19]\n"
      "s_and_b64 vcc, vcc, -1\n"
      "s_cbranch_scc1 .LS_E1\n"
      ".LK_E1:\n"
      "s_mov_b32 s56, 196608\n"
      "v_mov_b32 v18, 0\n"
      "v_mov_b32 v19, 0\n"
      "buffer_atomic_or_x2 v[18:19], v32, s[48:51], s56 offen offset:0 sc0 sc1\n"
      "s_waitcnt vmcnt(0)\n"
      "v_cmp_ne_u64 vcc, s[96:97], v[18:19]\n"
      "s_and_b64 vcc, vcc, -1\n"
      "s_cbranch_scc1 .LS_E2\n"
      ".LK_E2:\n"
      "s_mov_b32 s56, 229376\n"
      "v_mov_b32 v18, 0\n"
      "v_mov_b32 v19, 0\n"
      "buffer_atomic_or_x2 v[18:19], v32, s[48:51], s56 offen offset:0 sc0 sc1\n"
      "s_waitcnt vmcnt(0)\n"
      "v_cmp_ne_u64 vcc, s[96:97], v[18:19]\n"
      "s_and_b64 vcc, vcc, -1\n"
      "s_cbranch_scc1 .LS_E3\n"
      ".LK_E3:\n"
      "s_mov_b32 s56, 263040\n"
      "v_mov_b32 v18, 0\n"
      "v_mov_b32 v19, 0\n"
      "buffer_atomic_or_x2 v[18:19], v33, s[48:51], s56 offen offset:0 sc0 sc1\n"
      "s_waitcnt vmcnt(0)\n"
      "v_cmp_ne_u64 vcc, s[96:97], v[18:19]\n"
      "s_and_b64 vcc, vcc, -1\n"
      "s_cbranch_scc1 .LS_E4\n"
      ".LK_E4:\n"
      "buffer_store_dwordx2 v[16:17], v13, s[48:51], 0 offen sc1\n"
      ".LAEND:\n"
      "s_endpgm\n"
      ".LS_KVA:\n"
      "s_mov_b32 s99, 200000\n"
      ".LR_KVA:\n"
      "s_sleep 2\n"
      "v_mov_b32 v18, 0\n"
      "v_mov_b32 v19, 0\n"
      "buffer_atomic_or_x2 v[18:19], v14, s[48:51], s88 offen offset:0 sc0 sc1\n"
      "s_waitcnt vmcnt(0)\n"
      "v_cmp_ne_u64 vcc, s[96:97], v[18:19]\n"
      "s_nop 0\n"
      "s_and_b64 vcc, vcc, 0x3fff3fff\n"
      "s_cbranch_scc0 .LK_KVA\n"
      "s_sub_u32 s99, s99, 1\n"
      "s_cbranch_scc0 .LR_KVA\n"
      "s_branch .LK_KVA\n"
      ".LS_E0:\n"
      "s_mov_b32 s99, 200000\n"
      ".LR_E0:\n"
      "s_sleep 2\n"
      "v_mov_b32 v18, 0\n"
      "v_mov_b32 v19, 0\n"
      "buffer_atomic_or_x2 v[18:19], v32, s[48:51], s56 offen offset:0 sc0 sc1\n"
      "s_waitcnt vmcnt(0)\n"
      "v_cmp_ne_u64 vcc, s[96:97], v[18:19]\n"
      "s_nop 0\n"
      "s_and_b64 vcc, vcc, -1\n"
      "s_cbranch_scc0 .LK_E0\n"
      "s_sub_u32 s99, s99, 1\n"
      "s_cbranch_scc0 .LR_E0\n"
      "s_branch .LK_E0\n"
      ".LS_E1:\n"
      "s_mov_b32 s99, 200000\n"
      ".LR_E1:\n"
      "s_sleep 2\n"
      "v_mov_b32 v18, 0\n"
      "v_mov_b32 v19, 0\n"
      "buffer_atomic_or_x2 v[18:19], v32, s[48:51], s56 offen offset:0 sc0 sc1\n"
      "s_waitcnt vmcnt(0)\n"
      "v_cmp_ne_u64 vcc, s[96:97], v[18:19]\n"
      "s_nop 0\n"
      "s_and_b64 vcc, vcc, -1\n"
      "s_cbranch_scc0 .LK_E1\n"
      "s_sub_u32 s99, s99, 1\n"
      "s_cbranch_scc0 .LR_E1\n"
      "s_branch .LK_E1\n"
      ".LS_E2:\n"
      "s_mov_b32 s99, 200000\n"
      ".LR_E2:\n"
      "s_sleep 2\n"
      "v_mov_b32 v18, 0\n"
      "v_mov_b32 v19, 0\n"
      "buffer_atomic_or_x2 v[18:19], v32, s[48:51], s56 offen offset:0 sc0 sc1\n"
      "s_waitcnt vmcnt(0)\n"
      "v_cmp_ne_u64 vcc, s[96:97], v[18:19]\n"
      "s_nop 0\n"
      "s_and_b64 vcc, vcc, -1\n"
      "s_cbranch_scc0 .LK_E2\n"
      "s_sub_u32 s99, s99, 1\n"
      "s_cbranch_scc0 .LR_E2\n"
      "s_branch .LK_E2\n"
      ".LS_E3:\n"
      "s_mov_b32 s99, 200000\n"
      ".LR_E3:\n"
      "s_sleep 2\n"
      "v_mov_b32 v18, 0\n"
      "v_mov_b32 v19, 0\n"
      "buffer_atomic_or_x2 v[18:19], v32, s[48:51], s56 offen offset:0 sc0 sc1\n"
      "s_waitcnt vmcnt(0)\n"
      "v_cmp_ne_u64 vcc, s[96:97], v[18:19]\n"
      "s_nop 0\n"
      "s_and_b64 vcc, vcc, -1\n"
      "s_cbranch_scc0 .LK_E3\n"
      "s_sub_u32 s99, s99, 1\n"
      "s_cbranch_scc0 .LR_E3\n"
      "s_branch .LK_E3\n"
      ".LS_E4:\n"
      "s_mov_b32 s99, 200000\n"
      ".LR_E4:\n"
      "s_sleep 2\n"
      "v_mov_b32 v18, 0\n"
      "v_mov_b32 v19, 0\n"
      "buffer_atomic_or_x2 v[18:19], v33, s[48:51], s56 offen offset:0 sc0 sc1\n"
      "s_waitcnt vmcnt(0)\n"
      "v_cmp_ne_u64 vcc, s[96:97], v[18:19]\n"
      "s_nop 0\n"
      "s_and_b64 vcc, vcc, -1\n"
      "s_cbranch_scc0 .LK_E4\n"
      "s_sub_u32 s99, s99, 1\n"
      "s_cbranch_scc0 .LR_E4\n"
      "s_branch .LK_E4\n"
      ".LMB:\n"
      "v_bfe_u32 v36, v0, 1, 3\n"
      "v_lshrrev_b32 v74, 4, v0\n"
      "v_xor_b32 v75, v36, v74\n"
      "v_and_b32 v76, 15, v0\n"
      "v_lshl_add_u32 v75, v75, 4, s95\n"
      "v_lshl_add_u32 v32, v76, 7, v75\n"
      "v_xor_b32 v33, 64, v32\n"
      "v_xor_b32 v34, 0x10000, v32\n"
      "v_xor_b32 v35, 64, v34\n"
      "v_and_b32 v66, 1, v74\n"
      "v_lshrrev_b32 v67, 5, v0\n"
      "v_and_b32 v68, 31, v0\n"
      "v_mov_b32 v30, 0x3f80\n"
      "v_mov_b32 v31, 0\n"
      "v_lshlrev_b32 v75, 4, v69\n"
      "v_lshlrev_b64 v[28:29], v75, v[30:31]\n"
      "v_mov_b32 v30, 0x3f803f80\n"
      "v_mov_b32 v31, v30\n"
      "s_movk_i32 s62, 20480\n"
      "s_lshl_b32 s63, s62, 1\n"
      "s_add_u32 s64, s62, s63\n"
      "s_lshl_b32 s58, s55, 7\n"
      "s_add_u32 s56, s3, s59\n"
      "s_lshl1_add_u32 s56, s56, s60\n"
      "s_sub_u32 s57, s56, 14\n"
      "s_min_u32 s56, s56, s57\n"
      "s_mul_i32 s34, s56, 86016\n"
      "s_add_u32 s56, s56, 4\n"
      "s_sub_u32 s57, s56, 14\n"
      "s_min_u32 s56, s56, s57\n"
      "s_mul_i32 s86, s56, 86016\n"
      "s_add_u32 s56, s56, 4\n"
      "s_sub_u32 s57, s56, 14\n"
      "s_min_u32 s56, s56, s57\n"
      "s_mul_i32 s87, s56, 86016\n"
      "s_sub_u32 s56, s3, 1\n"
      "s_min_u32 s56, s56, 6\n"
      "s_lshl2_add_u32 s56, s56, s52\n"
      "s_mul_i32 s99, s56, 43008\n"
      "v_lshlrev_b32 v75, 1, v66\n"
      "v_xor_b32 v75, v75, v65\n"
      "v_lshlrev_b32 v75, 4, v75\n"
      "v_mul_u32_u24 v3, 2688, v64\n"
      "s_movk_i32 s56, 512\n"
      "v_add3_u32 v3, v3, v75, s56\n"
      "v_bfe_u32 v71, v0, 2, 2\n"
      "v_lshlrev_b32 v70, 7, v71\n"
      "v_lshrrev_b32 v71, 1, v71\n"
      "v_lshl_or_b32 v75, v66, 1, v71\n"
      "v_lshl_add_u32 v75, v75, 5, v70\n"
      "v_lshl_add_u32 v37, v69, 3, v75\n"
      "v_lshl_add_u32 v6, v74, 10, v37\n"
      "v_lshlrev_b32 v1, 4, v0\n"
      "v_cmp_gt_u32 vcc, 16, v0\n"
      "v_bfrev_b32 v73, 1\n"
      "v_lshlrev_b32 v75, 3, v0\n"
      "v_cndmask_b32 v13, v73, v75, vcc\n"
      "s_sub_u32 s58, s12, 2\n"
      "s_cselect_b32 s58, s12, s58\n"
      "s_cselect_b32 s74, 0, -1\n"
      "s_and_b32 s59, s74, 4096\n"
      "v_and_b32 v75, 47, v0\n"
      "v_cmp_gt_u32 vcc, 14, v75\n"
      "v_mul_u32_u24 v76, s59, v66\n"
      "v_lshl_add_u32 v75, v75, 3, v76\n"
      "v_cndmask_b32 v14, v73, v75, vcc\n"
      "v_bfe_u32 v75, v0, 2, 1\n"
      "v_and_b32 v76, 19, v0\n"
      "v_lshl_or_b32 v76, v75, 3, v76\n"
      "v_lshl_or_b32 v76, v71, 2, v76\n"
      "v_mul_u32_u24 v75, 7168, v67\n"
      "v_lshl_add_u32 v4, v76, 4, v75\n"
      "v_lshl_or_b32 v15, v67, 31, v1\n"
      "v_lshlrev_b32 v75, 2, v67\n"
      "v_sub_u32 v75, v68, v75\n"
      "v_add_u32 v21, 25, v75\n"
      "v_add_u32 v22, -1, v67\n"
      "v_and_b32 v75, 0x600, v22\n"
      "v_lshl_or_b32 v7, v68, 4, v75\n"
      "v_or_b32 v2, 0x180, v1\n"
      "buffer_load_dwordx4 v40, s[20:23], s61 offen offset:0 lds\n"
      "buffer_load_dwordx4 v41, s[20:23], s61 offen offset:1024 lds\n"
      "buffer_load_dwordx4 v42, s[20:23], s61 offen offset:2048 lds\n"
      "buffer_load_dwordx4 v43, s[20:23], s61 offen offset:3072 lds\n"
      "s_addk_i32 s61, 128\n"
      "s_addk_i32 m0, 0x4000\n"
      "s_cmp_eq_u32 s61, s11\n"
      "s_cselect_b32 s61, s98, s61\n"
      "s_getpc_b64 s[14:15]\n"
      ".LHGB:\n"
      "s_add_u32 s14, s14, .LHBB-.LHGB\n"
      "s_addc_u32 s15, s15, 0\n"
      "s_mov_b64 exec, 0\n"
      "s_cmp_eq_u32 s52, 0\n"
      "s_cbranch_scc1 .LPAD13\n"
      "s_cmp_eq_u32 s52, 1\n"
      "s_cbranch_scc1 .LPAD14\n"
      "s_cmp_eq_u32 s52, 2\n"
      "s_cbranch_scc1 .LPAD15\n"
      "s_cmp_eq_u32 s52, 3\n"
      "s_cbranch_scc1 .LPAD16\n"
      ".LHBB:\n"
      "s_mov_b64 exec, -1\n"
      "v_or_b32 v8, 0x180, v7\n"
      "s_waitcnt vmcnt(8)\n"
      "ds_read_b128 a[0:3], v32 offset:0\n"
      "ds_read_b128 a[4:7], v32 offset:2048\n"
      "ds_read_b128 a[8:11], v33 offset:0\n"
      "ds_read_b128 a[12:15], v33 offset:2048\n"
      "buffer_load_dwordx4 v40, s[20:23], s61 offen offset:0 lds\n"
      "buffer_load_dwordx4 v41, s[20:23], s61 offen offset:1024 lds\n"
      "buffer_load_dwordx4 v42, s[20:23], s61 offen offset:2048 lds\n"
      "buffer_load_dwordx4 v43, s[20:23], s61 offen offset:3072 lds\n"
      "s_addk_i32 s61, 128\n"
      "s_addk_i32 m0, 0x4000\n"
      "s_cmp_eq_u32 s61, s11\n"
      "s_cselect_b32 s61, s98, s61\n"
      "s_waitcnt vmcnt(8) lgkmcnt(0)\n"
      "ds_read_b128 a[16:19], v32 offset:16384\n"
      "ds_read_b128 a[20:23], v32 offset:18432\n"
      "ds_read_b128 a[24:27], v33 offset:16384\n"
      "ds_read_b128 a[28:31], v33 offset:18432\n"
      "buffer_load_dwordx4 v40, s[20:23], s61 offen offset:0 lds\n"
      "buffer_load_dwordx4 v41, s[20:23], s61 offen offset:1024 lds\n"
      "buffer_load_dwordx4 v42, s[20:23], s61 offen offset:2048 lds\n"
      "buffer_load_dwordx4 v43, s[20:23], s61 offen offset:3072 lds\n"
      "s_addk_i32 s61, 128\n"
      "s_addk_i32 m0, 0x4000\n"
      "s_cmp_eq_u32 s61, s11\n"
      "s_cselect_b32 s61, s98, s61\n"
      "v_mfma_f32_4x4x4_16b_bf16 v[48:51], v[30:31], a[0:1], 0\n"
      "v_mfma_f32_4x4x4_16b_bf16 v[56:59], a[0:1], a[0:1], 0\n"
      "v_mfma_f32_4x4x4_16b_bf16 v[52:55], v[30:31], a[4:5], 0\n"
      "v_mfma_f32_4x4x4_16b_bf16 v[60:63], a[4:5], a[4:5], 0\n"
      "v_mfma_f32_4x4x4_16b_bf16 v[48:51], v[30:31], a[2:3], v[48:51]\n"
      "v_mfma_f32_4x4x4_16b_bf16 v[56:59], a[2:3], a[2:3], v[56:59]\n"
      "v_mfma_f32_4x4x4_16b_bf16 v[52:55], v[30:31], a[6:7], v[52:55]\n"
      "v_mfma_f32_4x4x4_16b_bf16 v[60:63], a[6:7], a[6:7], v[60:63]\n"
      "v_mfma_f32_4x4x4_16b_bf16 v[48:51], v[30:31], a[8:9], v[48:51]\n"
      "v_mfma_f32_4x4x4_16b_bf16 v[56:59], a[8:9], a[8:9], v[56:59]\n"
      "v_mfma_f32_4x4x4_16b_bf16 v[52:55], v[30:31], a[12:13], v[52:55]\n"
      "v_mfma_f32_4x4x4_16b_bf16 v[60:63], a[12:13], a[12:13], v[60:63]\n"
      "v_mfma_f32_4x4x4_16b_bf16 v[48:51], v[30:31], a[10:11], v[48:51]\n"
      "v_mfma_f32_4x4x4_16b_bf16 v[56:59], a[10:11], a[10:11], v[56:59]\n"
      "v_mfma_f32_4x4x4_16b_bf16 v[52:55], v[30:31], a[14:15], v[52:55]\n"
      "v_mfma_f32_4x4x4_16b_bf16 v[60:63], a[14:15], a[14:15], v[60:63]\n"
      "s_waitcnt vmcnt(8) lgkmcnt(0)\n"
      "ds_read_b128 a[32:35], v32 offset:32768\n"
      "ds_read_b128 a[36:39], v32 offset:34816\n"
      "ds_read_b128 a[40:43], v33 offset:32768\n"
      "ds_read_b128 a[44:47], v33 offset:34816\n"
      "buffer_load_dwordx4 v40, s[20:23], s61 offen offset:0 lds\n"
      "buffer_load_dwordx4 v41, s[20:23], s61 offen offset:1024 lds\n"
      "buffer_load_dwordx4 v42, s[20:23], s61 offen offset:2048 lds\n"
      "buffer_load_dwordx4 v43, s[20:23], s61 offen offset:3072 lds\n"
      "s_addk_i32 s61, 128\n"
      "s_addk_i32 m0, 0x4000\n"
      "s_cmp_eq_u32 s61, s11\n"
      "s_cselect_b32 s61, s98, s61\n"
      "v_mfma_f32_4x4x4_16b_bf16 v[48:51], v[30:31], a[16:17], v[48:51]\n"
      "v_mfma_f32_4x4x4_16b_bf16 v[56:59], a[16:17], a[16:17], v[56:59]\n"
      "v_mfma_f32_4x4x4_16b_bf16 v[52:55], v[30:31], a[20:21], v[52:55]\n"
      "v_mfma_f32_4x4x4_16b_bf16 v[60:63], a[20:21], a[20:21], v[60:63]\n"
      "v_mfma_f32_4x4x4_16b_bf16 v[48:51], v[30:31], a[18:19], v[48:51]\n"
      "v_mfma_f32_4x4x4_16b_bf16 v[56:59], a[18:19], a[18:19], v[56:59]\n"
      "v_mfma_f32_4x4x4_16b_bf16 v[52:55], v[30:31], a[22:23], v[52:55]\n"
      "v_mfma_f32_4x4x4_16b_bf16 v[60:63], a[22:23], a[22:23], v[60:63]\n"
      "v_mfma_f32_4x4x4_16b_bf16 v[48:51], v[30:31], a[24:25], v[48:51]\n"
      "v_mfma_f32_4x4x4_16b_bf16 v[56:59], a[24:25], a[24:25], v[56:59]\n"
      "v_mfma_f32_4x4x4_16b_bf16 v[52:55], v[30:31], a[28:29], v[52:55]\n"
      "v_mfma_f32_4x4x4_16b_bf16 v[60:63], a[28:29], a[28:29], v[60:63]\n"
      "v_mfma_f32_4x4x4_16b_bf16 v[48:51], v[30:31], a[26:27], v[48:51]\n"
      "v_mfma_f32_4x4x4_16b_bf16 v[56:59], a[26:27], a[26:27], v[56:59]\n"
      ".LPAD13:\n"
      "s_cbranch_execz .LRET\n"
      "v_mfma_f32_4x4x4_16b_bf16 v[52:55], v[30:31], a[30:31], v[52:55]\n"
      "v_mfma_f32_4x4x4_16b_bf16 v[60:63], a[30:31], a[30:31], v[60:63]\n"
      "s_waitcnt vmcnt(8) lgkmcnt(0)\n"
      "ds_read_b128 a[48:51], v32 offset:49152\n"
      "ds_read_b128 a[52:55], v32 offset:51200\n"
      "ds_read_b128 a[56:59], v33 offset:49152\n"
      "ds_read_b128 a[60:63], v33 offset:51200\n"
      "buffer_load_dwordx4 v40, s[20:23], s61 offen offset:0 lds\n"
      "buffer_load_dwordx4 v41, s[20:23], s61 offen offset:1024 lds\n"
      "buffer_load_dwordx4 v42, s[20:23], s61 offen offset:2048 lds\n"
      "buffer_load_dwordx4 v43, s[20:23], s61 offen offset:3072 lds\n"
      "v_mfma_f32_4x4x4_16b_bf16 v[48:51], v[30:31], a[32:33], v[48:51]\n"
      "v_mfma_f32_4x4x4_16b_bf16 v[56:59], a[32:33], a[32:33], v[56:59]\n"
      "v_mfma_f32_4x4x4_16b_bf16 v[52:55], v[30:31], a[36:37], v[52:55]\n"
      "v_mfma_f32_4x4x4_16b_bf16 v[60:63], a[36:37], a[36:37], v[60:63]\n"
      "v_mfma_f32_4x4x4_16b_bf16 v[48:51], v[30:31], a[34:35], v[48:51]\n"
      "v_mfma_f32_4x4x4_16b_bf16 v[56:59], a[34:35], a[34:35], v[56:59]\n"
      "v_mfma_f32_4x4x4_16b_bf16 v[52:55], v[30:31], a[38:39], v[52:55]\n"
      "v_mfma_f32_4x4x4_16b_bf16 v[60:63], a[38:39], a[38:39], v[60:63]\n"
      "v_mfma_f32_4x4x4_16b_bf16 v[48:51], v[30:31], a[40:41], v[48:51]\n"
      "v_mfma_f32_4x4x4_16b_bf16 v[56:59], a[40:41], a[40:41], v[56:59]\n"
      "v_mfma_f32_4x4x4_16b_bf16 v[52:55], v[30:31], a[44:45], v[52:55]\n"
      "v_mfma_f32_4x4x4_16b_bf16 v[60:63], a[44:45], a[44:45], v[60:63]\n"
      "v_mfma_f32_4x4x4_16b_bf16 v[48:51], v[30:31], a[42:43], v[48:51]\n"
      "v_mfma_f32_4x4x4_16b_bf16 v[56:59], a[42:43], a[42:43], v[56:59]\n"
      "v_mfma_f32_4x4x4_16b_bf16 v[52:55], v[30:31], a[46:47], v[52:55]\n"
      "v_mfma_f32_4x4x4_16b_bf16 v[60:63], a[46:47], a[46:47], v[60:63]\n"
      "s_waitcnt vmcnt(8) lgkmcnt(0)\n"
      "ds_read_b128 a[64:67], v34 offset:0\n"
      "ds_read_b128 a[68:71], v34 offset:2048\n"
      "ds_read_b128 a[72:75], v35 offset:0\n"
      "ds_read_b128 a[76:79], v35 offset:2048\n"
      "s_add_u32 m0, s95, 114688\n"
      "v_add_u32 v19, s34, v3\n"
      "v_xor_b32 v18, 64, v19\n"
      "buffer_load_dwordx4 v19, s[24:27], 0 offen offset:0 lds\n"
      "buffer_load_dwordx4 v18, s[24:27], s62 offen offset:1024 lds\n"
      "buffer_load_dwordx4 v19, s[24:27], s63 offen offset:2048 lds\n"
      "buffer_load_dwordx4 v18, s[24:27], s64 offen offset:3072 lds\n"
      "v_mfma_f32_4x4x4_16b_bf16 v[48:51], v[30:31], a[48:49], v[48:51]\n"
      "v_mfma_f32_4x4x4_16b_bf16 v[56:59], a[48:49], a[48:49], v[56:59]\n"
      "v_mfma_f32_4x4x4_16b_bf16 v[52:55], v[30:31], a[52:53], v[52:55]\n"
      "v_mfma_f32_4x4x4_16b_bf16 v[60:63], a[52:53], a[52:53], v[60:63]\n"
      "v_mfma_f32_4x4x4_16b_bf16 v[48:51], v[30:31], a[50:51], v[48:51]\n"
      "v_mfma_f32_4x4x4_16b_bf16 v[56:59], a[50:51], a[50:51], v[56:59]\n"
      "v_mfma_f32_4x4x4_16b_bf16 v[52:55], v[30:31], a[54:55], v[52:55]\n"
      "v_mfma_f32_4x4x4_16b_bf16 v[60:63], a[54:55], a[54:55], v[60:63]\n"
      "v_mfma_f32_4x4x4_16b_bf16 v[48:51], v[30:31], a[56:57], v[48:51]\n"
      "v_mfma_f32_4x4x4_16b_bf16 v[56:59], a[56:57], a[56:57], v[56:59]\n"
      "v_mfma_f32_4x4x4_16b_bf16 v[52:55], v[30:31], a[60:61], v[52:55]\n"
      "v_mfma_f32_4x4x4_16b_bf16 v[60:63], a[60:61], a[60:61], v[60:63]\n"
      "v_mfma_f32_4x4x4_16b_bf16 v[48:51], v[30:31], a[58:59], v[48:51]\n"
      "v_mfma_f32_4x4x4_16b_bf16 v[56:59], a[58:59], a[58:59], v[56:59]\n"
      "v_mfma_f32_4x4x4_16b_bf16 v[52:55], v[30:31], a[62:63], v[52:55]\n"
      "v_mfma_f32_4x4x4_16b_bf16 v[60:63], a[62:63], a[62:63], v[60:63]\n"
      "s_waitcnt vmcnt(8) lgkmcnt(0)\n"
      "ds_read_b128 a[80:83], v34 offset:16384\n"
      "ds_read_b128 a[84:87], v34 offset:18432\n"
      "ds_read_b128 a[88:91], v35 offset:16384\n"
      "ds_read_b128 a[92:95], v35 offset:18432\n"
      "s_add_u32 m0, s95, 131072\n"
      "v_add_u32 v19, s86, v3\n"
      "v_xor_b32 v18, 64, v19\n"
      "buffer_load_dwordx4 v19, s[24:27], 0 offen offset:0 lds\n"
      "buffer_load_dwordx4 v18, s[24:27], s62 offen offset:1024 lds\n"
      "buffer_load_dwordx4 v19, s[24:27], s63 offen offset:2048 lds\n"
      "buffer_load_dwordx4 v18, s[24:27], s64 offen offset:3072 lds\n"
      "v_mfma_f32_4x4x4_16b_bf16 v[48:51], v[30:31], a[64:65], v[48:51]\n"
      "v_mfma_f32_4x4x4_16b_bf16 v[56:59], a[64:65], a[64:65], v[56:59]\n"
      "v_mfma_f32_4x4x4_16b_bf16 v[52:55], v[30:31], a[68:69], v[52:55]\n"
      "v_mfma_f32_4x4x4_16b_bf16 v[60:63], a[68:69], a[68:69], v[60:63]\n"
      "v_mfma_f32_4x4x4_16b_bf16 v[48:51], v[30:31], a[66:67], v[48:51]\n"
      "v_mfma_f32_4x4x4_16b_bf16 v[56:59], a[66:67], a[66:67], v[56:59]\n"
      "v_mfma_f32_4x4x4_16b_bf16 v[52:55], v[30:31], a[70:71], v[52:55]\n"
      "v_mfma_f32_4x4x4_16b_bf16 v[60:63], a[70:71], a[70:71], v[60:63]\n"
      "v_mfma_f32_4x4x4_16b_bf16 v[48:51], v[30:31], a[72:73], v[48:51]\n"
      "v_mfma_f32_4x4x4_16b_bf16 v[56:59], a[72:73], a[72:73], v[56:59]\n"
      "v_mfma_f32_4x4x4_16b_bf16 v[52:55], v[30:31], a[76:77], v[52:55]\n"
      "v_mfma_f32_4x4x4_16b_bf16 v[60:63], a[76:77], a[76:77], v[60:63]\n"
      "v_mfma_f32_4x4x4_16b_bf16 v[48:51], v[30:31], a[74:75], v[48:51]\n"
      "v_mfma_f32_4x4x4_16b_bf16 v[56:59], a[74:75], a[74:75], v[56:59]\n"
      "v_mfma_f32_4x4x4_16b_bf16 v[52:55], v[30:31], a[78:79], v[52:55]\n"
      "v_mfma_f32_4x4x4_16b_bf16 v[60:63], a[78:79], a[78:79], v[60:63]\n"
      "s_waitcnt vmcnt(8) lgkmcnt(0)\n"
      "ds_read_b128 a[96:99], v34 offset:32768\n"
      "ds_read_b128 a[100:103], v34 offset:34816\n"
      "ds_read_b128 a[104:107], v35 offset:32768\n"
      "ds_read_b128 a[108:111], v35 offset:34816\n"
      "v_mfma_f32_4x4x4_16b_bf16 v[48:51], v[30:31], a[80:81], v[48:51]\n"
      "v_mfma_f32_4x4x4_16b_bf16 v[56:59], a[80:81], a[80:81], v[56:59]\n"
      "v_mfma_f32_4x4x4_16b_bf16 v[52:55], v[30:31], a[84:85], v[52:55]\n"
      "v_mfma_f32_4x4x4_16b_bf16 v[60:63], a[84:85], a[84:85], v[60:63]\n"
      "v_mfma_f32_4x4x4_16b_bf16 v[48:51], v[30:31], a[82:83], v[48:51]\n"
      "v_mfma_f32_4x4x4_16b_bf16 v[56:59], a[82:83], a[82:83], v[56:59]\n"
      "v_mfma_f32_4x4x4_16b_bf16 v[52:55], v[30:31], a[86:87], v[52:55]\n"
      "v_mfma_f32_4x4x4_16b_bf16 v[60:63], a[86:87], a[86:87], v[60:63]\n"
      "v_mfma_f32_4x4x4_16b_bf16 v[48:51], v[30:31], a[88:89], v[48:51]\n"
      "v_mfma_f32_4x4x4_16b_bf16 v[56:59], a[88:89], a[88:89], v[56:59]\n"
      "v_mfma_f32_4x4x4_16b_bf16 v[52:55], v[30:31], a[92:93], v[52:55]\n"
      "v_mfma_f32_4x4x4_16b_bf16 v[60:63], a[92:93], a[92:93], v[60:63]\n"
      "v_mfma_f32_4x4x4_16b_bf16 v[48:51], v[30:31], a[90:91], v[48:51]\n"
      "v_mfma_f32_4x4x4_16b_bf16 v[56:59], a[90:91], a[90:91], v[56:59]\n"
      "v_mfma_f32_4x4x4_16b_bf16 v[52:55], v[30:31], a[94:95], v[52:55]\n"
      "v_mfma_f32_4x4x4_16b_bf16 v[60:63], a[94:95], a[94:95], v[60:63]\n"
      "s_waitcnt lgkmcnt(0)\n"
      "v_mfma_f32_4x4x4_16b_bf16 v[48:51], v[30:31], a[96:97], v[48:51]\n"
      "v_mfma_f32_4x4x4_16b_bf16 v[56:59], a[96:97], a[96:97], v[56:59]\n"
      "v_mfma_f32_4x4x4_16b_bf16 v[52:55], v[30:31], a[100:101], v[52:55]\n"
      "v_mfma_f32_4x4x4_16b_bf16 v[60:63], a[100:101], a[100:101], v[60:63]\n"
      "v_mfma_f32_4x4x4_16b_bf16 v[48:51], v[30:31], a[98:99], v[48:51]\n"
      "v_mfma_f32_4x4x4_16b_bf16 v[56:59], a[98:99], a[98:99], v[56:59]\n"
      "v_mfma_f32_4x4x4_16b_bf16 v[52:55], v[30:31], a[102:103], v[52:55]\n"
      "v_mfma_f32_4x4x4_16b_bf16 v[60:63], a[102:103], a[102:103], v[60:63]\n"
      "v_mfma_f32_4x4x4_16b_bf16 v[48:51], v[30:31], a[104:105], v[48:51]\n"
      "v_mfma_f32_4x4x4_16b_bf16 v[56:59], a[104:105], a[104:105], v[56:59]\n"
      "v_mfma_f32_4x4x4_16b_bf16 v[52:55], v[30:31], a[108:109], v[52:55]\n"
      "v_mfma_f32_4x4x4_16b_bf16 v[60:63], a[108:109], a[108:109], v[60:63]\n"
      "v_mfma_f32_4x4x4_16b_bf16 v[48:51], v[30:31], a[106:107], v[48:51]\n"
      "v_mfma_f32_4x4x4_16b_bf16 v[56:59], a[106:107], a[106:107], v[56:59]\n"
      "v_mfma_f32_4x4x4_16b_bf16 v[52:55], v[30:31], a[110:111], v[52:55]\n"
      "v_mfma_f32_4x4x4_16b_bf16 v[60:63], a[110:111], a[110:111], v[60:63]\n"
      "s_nop 3\n"
      "v_permlane16_swap_b32 v48, v52\n"
      "v_add_f32 v32, v48, v52\n"
      "v_add_f32 v33, v48, v52\n"
      "v_xor_b32 v9, 0x18000, v6\n"
      "v_xor_b32 v10, 0x18020, v6\n"
      "v_permlane32_swap_b32 v32, v33\n"
      "v_add_f32 v32, v32, v33\n"
      "v_mul_f32 v34, 0xbb124925, v32\n"
      "v_fmamk_f32 v32, v34, 0x43e00000, v32\n"
      "v_fmamk_f32 v34, v32, 0xbb124925, v34\n"
      "v_cvt_pk_bf16_f32 v24, 0, v34\n"
      "v_cvt_pk_bf16_f32 v40, 0, v34\n"
      "v_xor_b32 v11, 0x18040, v6\n"
      "v_xor_b32 v12, 0x18060, v6\n"
      "v_permlane16_swap_b32 v24, v40\n"
      "v_mov_b32 v25, v24\n"
      "v_mov_b32 v26, v24\n"
      "v_mov_b32 v27, v24\n"
      "v_mov_b32 v41, v40\n"
      "v_mov_b32 v42, v40\n"
      "v_mov_b32 v43, v40\n"
      "v_mfma_f32_4x4x4_16b_bf16 v[112:115], v[28:29], a[0:1], v[24:27]\n"
      "v_mfma_f32_4x4x4_16b_bf16 v[116:119], v[28:29], a[2:3], v[24:27]\n"
      "v_mfma_f32_4x4x4_16b_bf16 v[120:123], v[28:29], a[4:5], v[40:43]\n"
      "v_mfma_f32_4x4x4_16b_bf16 v[124:127], v[28:29], a[6:7], v[40:43]\n"
      "v_mfma_f32_4x4x4_16b_bf16 v[128:131], v[28:29], a[8:9], v[24:27]\n"
      "v_mfma_f32_4x4x4_16b_bf16 v[132:135], v[28:29], a[10:11], v[24:27]\n"
      "v_mfma_f32_4x4x4_16b_bf16 v[136:139], v[28:29], a[12:13], v[40:43]\n"
      "v_mfma_f32_4x4x4_16b_bf16 v[140:143], v[28:29], a[14:15], v[40:43]\n"
      "v_cvt_pk_bf16_f32 v144, v112, v113\n"
      "v_cvt_pk_bf16_f32 v145, v114, v115\n"
      "v_cvt_pk_bf16_f32 v146, v116, v117\n"
      "v_cvt_pk_bf16_f32 v147, v118, v119\n"
      "v_cvt_pk_bf16_f32 v148, v120, v121\n"
      "v_cvt_pk_bf16_f32 v149, v122, v123\n"
      "v_cvt_pk_bf16_f32 v150, v124, v125\n"
      "v_cvt_pk_bf16_f32 v151, v126, v127\n"
      "v_mfma_f32_4x4x4_16b_bf16 v[112:115], v[28:29], a[16:17], v[24:27]\n"
      "v_mfma_f32_4x4x4_16b_bf16 v[116:119], v[28:29], a[18:19], v[24:27]\n"
      "v_mfma_f32_4x4x4_16b_bf16 v[120:123], v[28:29], a[20:21], v[40:43]\n"
      "v_mfma_f32_4x4x4_16b_bf16 v[124:127], v[28:29], a[22:23], v[40:43]\n"
      "v_cvt_pk_bf16_f32 v152, v128, v129\n"
      "v_cvt_pk_bf16_f32 v153, v130, v131\n"
      "v_cvt_pk_bf16_f32 v154, v132, v133\n"
      "v_cvt_pk_bf16_f32 v155, v134, v135\n"
      "v_cvt_pk_bf16_f32 v156, v136, v137\n"
      "v_cvt_pk_bf16_f32 v157, v138, v139\n"
      "v_cvt_pk_bf16_f32 v158, v140, v141\n"
      "v_cvt_pk_bf16_f32 v159, v142, v143\n"
      "v_mfma_f32_4x4x4_16b_bf16 v[128:131], v[28:29], a[24:25], v[24:27]\n"
      "v_mfma_f32_4x4x4_16b_bf16 v[132:135], v[28:29], a[26:27], v[24:27]\n"
      "v_mfma_f32_4x4x4_16b_bf16 v[136:139], v[28:29], a[28:29], v[40:43]\n"
      "v_mfma_f32_4x4x4_16b_bf16 v[140:143], v[28:29], a[30:31], v[40:43]\n"
      "s_waitcnt vmcnt(4) lgkmcnt(0)\n"
      "s_barrier\n"
      "ds_read_b64_tr_b16 a[216:217], v9 offset:16384\n"
      "ds_read_b64_tr_b16 a[218:219], v9 offset:16896\n"
      "ds_read_b64_tr_b16 a[220:221], v10 offset:16384\n"
      "ds_read_b64_tr_b16 a[222:223], v10 offset:16896\n"
      "ds_read_b64_tr_b16 a[224:225], v11 offset:16384\n"
      "ds_read_b64_tr_b16 a[226:227], v11 offset:16896\n"
      "ds_read_b64_tr_b16 a[228:229], v12 offset:16384\n"
      "ds_read_b64_tr_b16 a[230:231], v12 offset:16896\n"
      "ds_read_b64_tr_b16 a[232:233], v9 offset:20480\n"
      "ds_read_b64_tr_b16 a[234:235], v9 offset:20992\n"
      "ds_read_b64_tr_b16 a[236:237], v10 offset:20480\n"
      "ds_read_b64_tr_b16 a[238:239], v10 offset:20992\n"
      "ds_read_b64_tr_b16 a[240:241], v11 offset:20480\n"
      "ds_read_b64_tr_b16 a[242:243], v11 offset:20992\n"
      "s_waitcnt lgkmcnt(6)\n"
      "v_mfma_f32_16x16x32_bf16 v[80:83], a[216:219], v[144:147], 0\n"
      "ds_read_b64_tr_b16 a[244:245], v12 offset:20480\n"
      "ds_read_b64_tr_b16 a[246:247], v12 offset:20992\n"
      "v_mfma_f32_16x16x32_bf16 v[84:87], a[216:219], v[148:151], 0\n"
      "s_add_u32 m0, s95, 147456\n"
      "v_add_u32 v19, s87, v3\n"
      "v_xor_b32 v18, 64, v19\n"
      "buffer_load_dwordx4 v19, s[24:27], 0 offen offset:0 lds\n"
      "v_cvt_pk_bf16_f32 v160, v112, v113\n"
      "v_cvt_pk_bf16_f32 v161, v114, v115\n"
      "v_mfma_f32_16x16x32_bf16 v[88:91], a[220:223], v[144:147], 0\n"
      "ds_read_b64_tr_b16 a[248:249], v9 offset:24576\n"
      "ds_read_b64_tr_b16 a[250:251], v9 offset:25088\n"
      "v_mfma_f32_16x16x32_bf16 v[92:95], a[220:223], v[148:151], 0\n"
      "v_cvt_pk_bf16_f32 v162, v116, v117\n"
      "v_cvt_pk_bf16_f32 v163, v118, v119\n"
      "v_bfe_i32 v35, v0, 0, 1\n"
      "v_bfe_i32 v36, v0, 1, 1\n"
      "v_mfma_f32_16x16x32_bf16 v[96:99], a[224:227], v[144:147], 0\n"
      "ds_read_b64_tr_b16 a[252:253], v10 offset:24576\n"
      "ds_read_b64_tr_b16 a[254:255], v10 offset:25088\n"
      "v_mfma_f32_16x16x32_bf16 v[100:103], a[224:227], v[148:151], 0\n"
      "buffer_load_dwordx4 v18, s[24:27], s62 offen offset:1024 lds\n"
      "v_cvt_pk_bf16_f32 v164, v120, v121\n"
      "v_cvt_pk_bf16_f32 v165, v122, v123\n"
      "v_bfi_b32 v56, v35, v57, v56\n"
      "v_mfma_f32_16x16x32_bf16 v[104:107], a[228:231], v[144:147], 0\n"
      "ds_read_b64_tr_b16 a[216:217], v11 offset:24576\n"
      "ds_read_b64_tr_b16 a[218:219], v11 offset:25088\n"
      "v_mfma_f32_16x16x32_bf16 v[108:111], a[228:231], v[148:151], 0\n"
      "v_cvt_pk_bf16_f32 v166, v124, v125\n"
      "v_cvt_pk_bf16_f32 v167, v126, v127\n"
      "v_bfi_b32 v58, v35, v59, v58\n"
      "v_bfi_b32 v60, v35, v61, v60\n"
      "s_lshl3_add_u32 s60, s53, s52\n"
      "s_lshl_b32 s89, s60, 12\n"
      "s_waitcnt lgkmcnt(6)\n"
      "v_mfma_f32_16x16x32_bf16 v[80:83], a[232:235], v[152:155], v[80:83]\n"
      "ds_read_b64_tr_b16 a[220:221], v12 offset:24576\n"
      "ds_read_b64_tr_b16 a[222:223], v12 offset:25088\n"
      "v_mfma_f32_16x16x32_bf16 v[84:87], a[232:235], v[156:159], v[84:87]\n"
      "buffer_load_dwordx4 v19, s[24:27], s63 offen offset:2048 lds\n"
      "v_cvt_pk_bf16_f32 v168, v128, v129\n"
      "v_cvt_pk_bf16_f32 v169, v130, v131\n"
      "v_mfma_f32_16x16x32_bf16 v[88:91], a[236:239], v[152:155], v[88:91]\n"
      "ds_read_b64_tr_b16 a[224:225], v9 offset:28672\n"
      "ds_read_b64_tr_b16 a[226:227], v9 offset:29184\n"
      "v_mfma_f32_16x16x32_bf16 v[92:95], a[236:239], v[156:159], v[92:95]\n"
      "v_cvt_pk_bf16_f32 v170, v132, v133\n"
      "v_cvt_pk_bf16_f32 v171, v134, v135\n"
      "v_bfi_b32 v62, v35, v63, v62\n"
      "v_bfi_b32 v56, v36, v58, v56\n"
      "s_add_u32 s89, s89, 278528\n"
      "s_mul_i32 s60, s60, 71680\n"
      "v_mfma_f32_16x16x32_bf16 v[96:99], a[240:243], v[152:155], v[96:99]\n"
      "ds_read_b64_tr_b16 a[228:229], v10 offset:28672\n"
      "ds_read_b64_tr_b16 a[230:231], v10 offset:29184\n"
      "v_mfma_f32_16x16x32_bf16 v[100:103], a[240:243], v[156:159], v[100:103]\n"
      "s_mov_b64 exec, 1\n"
      "buffer_load_dwordx2 v[16:17], v0, s[48:51], 0 offen sc1\n"
      "s_mov_b64 exec, -1\n"
      "buffer_load_dwordx4 v18, s[24:27], s64 offen offset:3072 lds\n"
      "v_cvt_pk_bf16_f32 v172, v136, v137\n"
      "v_cvt_pk_bf16_f32 v173, v138, v139\n"
      "v_mfma_f32_16x16x32_bf16 v[104:107], a[244:247], v[152:155], v[104:107]\n"
      "ds_read_b64_tr_b16 a[232:233], v11 offset:28672\n"
      "ds_read_b64_tr_b16 a[234:235], v11 offset:29184\n"
      "v_mfma_f32_16x16x32_bf16 v[108:111], a[244:247], v[156:159], v[108:111]\n"
      "v_cvt_pk_bf16_f32 v174, v140, v141\n"
      "v_cvt_pk_bf16_f32 v175, v142, v143\n"
      "v_bfi_b32 v60, v36, v62, v60\n"
      "v_mul_f32 v34, v34, -v34\n"
      "s_add_u32 s82, s60, 5586944\n"
      "v_mfma_f32_4x4x4_16b_bf16 v[112:115], v[28:29], a[32:33], v[24:27]\n"
      "v_mfma_f32_4x4x4_16b_bf16 v[116:119], v[28:29], a[34:35], v[24:27]\n"
      "v_mfma_f32_4x4x4_16b_bf16 v[120:123], v[28:29], a[36:37], v[40:43]\n"
      "v_mfma_f32_4x4x4_16b_bf16 v[124:127], v[28:29], a[38:39], v[40:43]\n"
      "s_waitcnt vmcnt(5) lgkmcnt(6)\n"
      "s_barrier\n"
      "v_mfma_f32_16x16x32_bf16 v[80:83], a[248:251], v[160:163], v[80:83]\n"
      "ds_read_b64_tr_b16 a[236:237], v12 offset:28672\n"
      "ds_read_b64_tr_b16 a[238:239], v12 offset:29184\n"
      "v_mfma_f32_16x16x32_bf16 v[84:87], a[248:251], v[164:167], v[84:87]\n"
      "s_lshr_b32 s35, s95, 1\n"
      "s_add_u32 m0, s35, 114688\n"
      "v_add_u32 v19, s99, v3\n"
      "v_xor_b32 v18, 64, v19\n"
      "buffer_load_dwordx4 v19, s[24:27], 0 offen offset:0 lds\n"
      "v_cvt_pk_bf16_f32 v176, v112, v113\n"
      "v_cvt_pk_bf16_f32 v177, v114, v115\n"
      "v_mfma_f32_16x16x32_bf16 v[88:91], a[252:255], v[160:163], v[88:91]\n"
      "ds_read_b64_tr_b16 a[240:241], v9 offset:32768\n"
      "ds_read_b64_tr_b16 a[242:243], v9 offset:33280\n"
      "v_mfma_f32_16x16x32_bf16 v[92:95], a[252:255], v[164:167], v[92:95]\n"
      "v_cvt_pk_bf16_f32 v178, v116, v117\n"
      "v_cvt_pk_bf16_f32 v179, v118, v119\n"
      "v_permlane16_swap_b32 v56, v60\n"
      "v_add_f32 v35, v56, v60\n"
      "v_add_f32 v36, v56, v60\n"
      "s_mul_i32 s68, s12, 57344\n"
      "v_mfma_f32_16x16x32_bf16 v[96:99], a[216:219], v[160:163], v[96:99]\n"
      "ds_read_b64_tr_b16 a[244:245], v10 offset:32768\n"
      "ds_read_b64_tr_b16 a[246:247], v10 offset:33280\n"
      "v_mfma_f32_16x16x32_bf16 v[100:103], a[216:219], v[164:167], v[100:103]\n"
      "buffer_load_dwordx4 v18, s[24:27], s62 offen offset:1024 lds\n"
      "v_cvt_pk_bf16_f32 v180, v120, v121\n"
      "v_cvt_pk_bf16_f32 v181, v122, v123\n"
      "s_mul_i32 s69, s58, 57344\n"
      "v_mfma_f32_16x16x32_bf16 v[104:107], a[220:223], v[160:163], v[104:107]\n"
      "ds_read_b64_tr_b16 a[248:249], v11 offset:32768\n"
      "ds_read_b64_tr_b16 a[250:251], v11 offset:33280\n"
      "v_mfma_f32_16x16x32_bf16 v[108:111], a[220:223], v[164:167], v[108:111]\n"
      "v_cvt_pk_bf16_f32 v182, v124, v125\n"
      "v_cvt_pk_bf16_f32 v183, v126, v127\n"
      "v_permlane32_swap_b32 v35, v36\n"
      "v_add_f32 v35, v35, v36\n"
      "s_add_u32 s70, s68, 1080832\n"
      "s_add_u32 s71, s70, 14336\n"
      "v_mfma_f32_4x4x4_16b_bf16 v[128:131], v[28:29], a[40:41], v[24:27]\n"
      "v_mfma_f32_4x4x4_16b_bf16 v[132:135], v[28:29], a[42:43], v[24:27]\n"
      "v_mfma_f32_4x4x4_16b_bf16 v[136:139], v[28:29], a[44:45], v[40:43]\n"
      "v_mfma_f32_4x4x4_16b_bf16 v[140:143], v[28:29], a[46:47], v[40:43]\n"
      "s_waitcnt lgkmcnt(6)\n"
      "v_mfma_f32_16x16x32_bf16 v[80:83], a[224:227], v[168:171], v[80:83]\n"
      "ds_read_b64_tr_b16 a[252:253], v12 offset:32768\n"
      "ds_read_b64_tr_b16 a[254:255], v12 offset:33280\n"
      "v_mfma_f32_16x16x32_bf16 v[84:87], a[224:227], v[172:175], v[84:87]\n"
      "s_add_u32 m0, s95, 0\n"
      "v_add_u32 v3, 128, v3\n"
      "v_add_u32 v19, s34, v3\n"
      "v_xor_b32 v18, 64, v19\n"
      "buffer_load_dwordx4 v19, s[24:27], 0 offen offset:0 lds\n"
      "v_cvt_pk_bf16_f32 v184, v128, v129\n"
      "v_cvt_pk_bf16_f32 v185, v130, v131\n"
      "v_mfma_f32_16x16x32_bf16 v[88:91], a[228:231], v[168:171], v[88:91]\n"
      "ds_read_b64_tr_b16 a[216:217], v9 offset:36864\n"
      "ds_read_b64_tr_b16 a[218:219], v9 offset:37376\n"
      "v_mfma_f32_16x16x32_bf16 v[92:95], a[228:231], v[172:175], v[92:95]\n"
      "v_cvt_pk_bf16_f32 v186, v132, v133\n"
      "v_cvt_pk_bf16_f32 v187, v134, v135\n"
      "v_fmamk_f32 v35, v35, 0x3b124925, v34\n"
      "v_max_f32 v35, 0, v35\n"
      "s_add_u32 s72, s69, 1095176\n"
      "v_mfma_f32_16x16x32_bf16 v[96:99], a[232:235], v[168:171], v[96:99]\n"
      "ds_read_b64_tr_b16 a[220:221], v10 offset:36864\n"
      "ds_read_b64_tr_b16 a[222:223], v10 offset:37376\n"
      "v_mfma_f32_16x16x32_bf16 v[100:103], a[232:235], v[172:175], v[100:103]\n"
      "buffer_load_dwordx4 v18, s[24:27], s62 offen offset:1024 lds\n"
      "v_cvt_pk_bf16_f32 v188, v136, v137\n"
      "v_cvt_pk_bf16_f32 v189, v138, v139\n"
      "v_cvt_pk_bf16_f32 v35, 0, v35\n"
      "s_add_u32 s68, s68, 1062912\n"
      "v_mfma_f32_16x16x32_bf16 v[104:107], a[236:239], v[168:171], v[104:107]\n"
      "ds_read_b64_tr_b16 a[224:225], v11 offset:36864\n"
      "ds_read_b64_tr_b16 a[226:227], v11 offset:37376\n"
      "v_mfma_f32_16x16x32_bf16 v[108:111], a[236:239], v[172:175], v[108:111]\n"
      "v_cvt_pk_bf16_f32 v190, v140, v141\n"
      "v_cvt_pk_bf16_f32 v191, v142, v143\n"
      "v_add_f32 v35, 0x3727c5ac, v35\n"
      "v_cvt_pk_bf16_f32 v35, 0, v35\n"
      "s_add_u32 s69, s69, 1062912\n"
      "v_mfma_f32_4x4x4_16b_bf16 v[112:115], v[28:29], a[48:49], v[24:27]\n"
      "v_mfma_f32_4x4x4_16b_bf16 v[116:119], v[28:29], a[50:51], v[24:27]\n"
      "v_mfma_f32_4x4x4_16b_bf16 v[120:123], v[28:29], a[52:53], v[40:43]\n"
      "v_mfma_f32_4x4x4_16b_bf16 v[124:127], v[28:29], a[54:55], v[40:43]\n"
      "s_waitcnt lgkmcnt(6)\n"
      "v_mfma_f32_16x16x32_bf16 v[80:83], a[240:243], v[176:179], v[80:83]\n"
      "ds_read_b64_tr_b16 a[228:229], v12 offset:36864\n"
      "ds_read_b64_tr_b16 a[230:231], v12 offset:37376\n"
      "v_mfma_f32_16x16x32_bf16 v[84:87], a[240:243], v[180:183], v[84:87]\n"
      "buffer_load_dwordx4 v19, s[24:27], s63 offen offset:2048 lds\n"
      "v_cvt_pk_bf16_f32 v192, v112, v113\n"
      "v_cvt_pk_bf16_f32 v193, v114, v115\n"
      "s_lshl_b32 s88, s58, 11\n"
      "v_mfma_f32_16x16x32_bf16 v[88:91], a[244:247], v[176:179], v[88:91]\n"
      "ds_read_b64_tr_b16 a[232:233], v9 offset:40960\n"
      "ds_read_b64_tr_b16 a[234:235], v9 offset:41472\n"
      "v_mfma_f32_16x16x32_bf16 v[92:95], a[244:247], v[180:183], v[92:95]\n"
      "v_cvt_pk_bf16_f32 v194, v116, v117\n"
      "v_cvt_pk_bf16_f32 v195, v118, v119\n"
      "v_sqrt_f32 v35, v35\n"
      "s_add_u32 s88, s88, 131072\n"
      "v_mfma_f32_16x16x32_bf16 v[96:99], a[248:251], v[176:179], v[96:99]\n"
      "ds_read_b64_tr_b16 a[236:237], v10 offset:40960\n"
      "ds_read_b64_tr_b16 a[238:239], v10 offset:41472\n"
      "v_mfma_f32_16x16x32_bf16 v[100:103], a[248:251], v[180:183], v[100:103]\n"
      "buffer_load_dwordx4 v18, s[24:27], s64 offen offset:3072 lds\n"
      "v_cvt_pk_bf16_f32 v196, v120, v121\n"
      "v_cvt_pk_bf16_f32 v197, v122, v123\n"
      "v_mov_b32 v44, 0\n"
      "v_lshlrev_b32 v46, 10, v0\n"
      "v_mfma_f32_16x16x32_bf16 v[104:107], a[252:255], v[176:179], v[104:107]\n"
      "ds_read_b64_tr_b16 a[240:241], v11 offset:40960\n"
      "ds_read_b64_tr_b16 a[242:243], v11 offset:41472\n"
      "v_mfma_f32_16x16x32_bf16 v[108:111], a[252:255], v[180:183], v[108:111]\n"
      "v_cvt_pk_bf16_f32 v198, v124, v125\n"
      "v_cvt_pk_bf16_f32 v199, v126, v127\n"
      "v_cvt_pk_bf16_f32 v35, 0, v35\n"
      "v_rcp_f32 v23, v35\n"
      "v_mfma_f32_4x4x4_16b_bf16 v[128:131], v[28:29], a[56:57], v[24:27]\n"
      "v_mfma_f32_4x4x4_16b_bf16 v[132:135], v[28:29], a[58:59], v[24:27]\n"
      "v_mfma_f32_4x4x4_16b_bf16 v[136:139], v[28:29], a[60:61], v[40:43]\n"
      "v_mfma_f32_4x4x4_16b_bf16 v[140:143], v[28:29], a[62:63], v[40:43]\n"
      "s_waitcnt lgkmcnt(6)\n"
      "v_mfma_f32_16x16x32_bf16 v[80:83], a[216:219], v[184:187], v[80:83]\n"
      "ds_read_b64_tr_b16 a[244:245], v12 offset:40960\n"
      "ds_read_b64_tr_b16 a[246:247], v12 offset:41472\n"
      "v_mfma_f32_16x16x32_bf16 v[84:87], a[216:219], v[188:191], v[84:87]\n"
      "s_add_u32 m0, s95, 16384\n"
      "v_add_u32 v19, s86, v3\n"
      "v_xor_b32 v18, 64, v19\n"
      "buffer_load_dwordx4 v19, s[24:27], 0 offen offset:0 lds\n"
      "buffer_load_dword a215, v14, s[48:51], s88 offen sc1\n"
      "v_cvt_pk_bf16_f32 v200, v128, v129\n"
      "v_cvt_pk_bf16_f32 v201, v130, v131\n"
      "v_mfma_f32_16x16x32_bf16 v[88:91], a[220:223], v[184:187], v[88:91]\n"
      "ds_read_b64_tr_b16 a[248:249], v9 offset:45056\n"
      "ds_read_b64_tr_b16 a[250:251], v9 offset:45568\n"
      "v_mfma_f32_16x16x32_bf16 v[92:95], a[220:223], v[188:191], v[92:95]\n"
      "s_mov_b64 exec, 3\n"
      "buffer_load_dword a215, v46, s[48:51], s89 offen sc1\n"
      "s_mov_b64 exec, -1\n"
      "v_cvt_pk_bf16_f32 v202, v132, v133\n"
      "v_cvt_pk_bf16_f32 v203, v134, v135\n"
      "v_mul_f32 v20, 0x3e8b97f6, v23\n"
      "v_not_b32 v23, v22\n"
      "v_mfma_f32_16x16x32_bf16 v[96:99], a[224:227], v[184:187], v[96:99]\n"
      "ds_read_b64_tr_b16 a[252:253], v10 offset:45056\n"
      "ds_read_b64_tr_b16 a[254:255], v10 offset:45568\n"
      "v_mfma_f32_16x16x32_bf16 v[100:103], a[224:227], v[188:191], v[100:103]\n"
      "buffer_load_dwordx4 v18, s[24:27], s62 offen offset:1024 lds\n"
      "v_cvt_pk_bf16_f32 v204, v136, v137\n"
      "v_cvt_pk_bf16_f32 v205, v138, v139\n"
      "v_mfma_f32_16x16x32_bf16 v[104:107], a[228:231], v[184:187], v[104:107]\n"
      "ds_read_b64_tr_b16 a[216:217], v11 offset:45056\n"
      "ds_read_b64_tr_b16 a[218:219], v11 offset:45568\n"
      "v_mfma_f32_16x16x32_bf16 v[108:111], a[228:231], v[188:191], v[108:111]\n"
      "v_cvt_pk_bf16_f32 v206, v140, v141\n"
      "v_cvt_pk_bf16_f32 v207, v142, v143\n"
      "v_mfma_f32_4x4x4_16b_bf16 v[112:115], v[28:29], a[64:65], v[24:27]\n"
      "v_mfma_f32_4x4x4_16b_bf16 v[116:119], v[28:29], a[66:67], v[24:27]\n"
      "v_mfma_f32_4x4x4_16b_bf16 v[120:123], v[28:29], a[68:69], v[40:43]\n"
      "v_mfma_f32_4x4x4_16b_bf16 v[124:127], v[28:29], a[70:71], v[40:43]\n"
      "s_waitcnt vmcnt(10) lgkmcnt(6)\n"
      "s_barrier\n"
      "v_mfma_f32_16x16x32_bf16 v[80:83], a[232:235], v[192:195], v[80:83]\n"
      "ds_read_b64_tr_b16 a[220:221], v12 offset:45056\n"
      "ds_read_b64_tr_b16 a[222:223], v12 offset:45568\n"
      "v_mfma_f32_16x16x32_bf16 v[84:87], a[232:235], v[196:199], v[84:87]\n"
      "buffer_load_dwordx4 v19, s[24:27], s63 offen offset:2048 lds\n"
      "v_cvt_pk_bf16_f32 v208, v112, v113\n"
      "v_cvt_pk_bf16_f32 v209, v114, v115\n"
      "v_mfma_f32_16x16x32_bf16 v[88:91], a[236:239], v[192:195], v[88:91]\n"
      "ds_read_b64_tr_b16 a[224:225], v9 offset:49152\n"
      "ds_read_b64_tr_b16 a[226:227], v9 offset:49664\n"
      "v_mfma_f32_16x16x32_bf16 v[92:95], a[236:239], v[196:199], v[92:95]\n"
      "v_cvt_pk_bf16_f32 v210, v116, v117\n"
      "v_cvt_pk_bf16_f32 v211, v118, v119\n"
      "v_mfma_f32_16x16x32_bf16 v[96:99], a[240:243], v[192:195], v[96:99]\n"
      "ds_read_b64_tr_b16 a[228:229], v10 offset:49152\n"
      "ds_read_b64_tr_b16 a[230:231], v10 offset:49664\n"
      "v_mfma_f32_16x16x32_bf16 v[100:103], a[240:243], v[196:199], v[100:103]\n"
      "buffer_load_dwordx4 v18, s[24:27], s64 offen offset:3072 lds\n"
      "v_cvt_pk_bf16_f32 v212, v120, v121\n"
      "v_cvt_pk_bf16_f32 v213, v122, v123\n"
      "v_mfma_f32_16x16x32_bf16 v[104:107], a[244:247], v[192:195], v[104:107]\n"
      "ds_read_b64_tr_b16 a[232:233], v11 offset:49152\n"
      "ds_read_b64_tr_b16 a[234:235], v11 offset:49664\n"
      "v_mfma_f32_16x16x32_bf16 v[108:111], a[244:247], v[196:199], v[108:111]\n"
      "v_cvt_pk_bf16_f32 v214, v124, v125\n"
      "v_cvt_pk_bf16_f32 v215, v126, v127\n"
      "v_mfma_f32_4x4x4_16b_bf16 v[128:131], v[28:29], a[72:73], v[24:27]\n"
      "v_mfma_f32_4x4x4_16b_bf16 v[132:135], v[28:29], a[74:75], v[24:27]\n"
      "v_mfma_f32_4x4x4_16b_bf16 v[136:139], v[28:29], a[76:77], v[40:43]\n"
      "v_mfma_f32_4x4x4_16b_bf16 v[140:143], v[28:29], a[78:79], v[40:43]\n"
      "s_waitcnt lgkmcnt(6)\n"
      "v_mfma_f32_16x16x32_bf16 v[80:83], a[248:251], v[200:203], v[80:83]\n"
      "ds_read_b64_tr_b16 a[236:237], v12 offset:49152\n"
      "ds_read_b64_tr_b16 a[238:239], v12 offset:49664\n"
      "v_mfma_f32_16x16x32_bf16 v[84:87], a[248:251], v[204:207], v[84:87]\n"
      "s_add_u32 m0, s95, 32768\n"
      "v_add_u32 v19, s87, v3\n"
      "v_xor_b32 v18, 64, v19\n"
      "buffer_load_dwordx4 v19, s[24:27], 0 offen offset:0 lds\n"
      "v_cvt_pk_bf16_f32 v216, v128, v129\n"
      "v_cvt_pk_bf16_f32 v217, v130, v131\n"
      "v_mfma_f32_16x16x32_bf16 v[88:91], a[252:255], v[200:203], v[88:91]\n"
      "ds_read_b64_tr_b16 a[240:241], v9 offset:53248\n"
      "ds_read_b64_tr_b16 a[242:243], v9 offset:53760\n"
      "v_mfma_f32_16x16x32_bf16 v[92:95], a[252:255], v[204:207], v[92:95]\n"
      "v_cvt_pk_bf16_f32 v218, v132, v133\n"
      "v_cvt_pk_bf16_f32 v219, v134, v135\n"
      "v_mfma_f32_16x16x32_bf16 v[96:99], a[216:219], v[200:203], v[96:99]\n"
      "ds_read_b64_tr_b16 a[244:245], v10 offset:53248\n"
      "ds_read_b64_tr_b16 a[246:247], v10 offset:53760\n"
      "v_mfma_f32_16x16x32_bf16 v[100:103], a[216:219], v[204:207], v[100:103]\n"
      "buffer_load_dwordx4 v18, s[24:27], s62 offen offset:1024 lds\n"
      "v_cvt_pk_bf16_f32 v220, v136, v137\n"
      "v_cvt_pk_bf16_f32 v221, v138, v139\n"
      "v_mfma_f32_16x16x32_bf16 v[104:107], a[220:223], v[200:203], v[104:107]\n"
      "ds_read_b64_tr_b16 a[248:249], v11 offset:53248\n"
      "ds_read_b64_tr_b16 a[250:251], v11 offset:53760\n"
      "v_mfma_f32_16x16x32_bf16 v[108:111], a[220:223], v[204:207], v[108:111]\n"
      "v_cvt_pk_bf16_f32 v222, v140, v141\n"
      "v_cvt_pk_bf16_f32 v223, v142, v143\n"
      "v_mfma_f32_4x4x4_16b_bf16 v[112:115], v[28:29], a[80:81], v[24:27]\n"
      "v_mfma_f32_4x4x4_16b_bf16 v[116:119], v[28:29], a[82:83], v[24:27]\n"
      "v_mfma_f32_4x4x4_16b_bf16 v[120:123], v[28:29], a[84:85], v[40:43]\n"
      "v_mfma_f32_4x4x4_16b_bf16 v[124:127], v[28:29], a[86:87], v[40:43]\n"
      "s_waitcnt lgkmcnt(6)\n"
      "v_mfma_f32_16x16x32_bf16 v[80:83], a[224:227], v[208:211], v[80:83]\n"
      "ds_read_b64_tr_b16 a[252:253], v12 offset:53248\n"
      "ds_read_b64_tr_b16 a[254:255], v12 offset:53760\n"
      "v_mfma_f32_16x16x32_bf16 v[84:87], a[224:227], v[212:215], v[84:87]\n"
      "buffer_load_dwordx4 v19, s[24:27], s63 offen offset:2048 lds\n"
      "v_cvt_pk_bf16_f32 v224, v112, v113\n"
      "v_cvt_pk_bf16_f32 v225, v114, v115\n"
      "v_mfma_f32_16x16x32_bf16 v[88:91], a[228:231], v[208:211], v[88:91]\n"
      "ds_read_b64_tr_b16 a[216:217], v9 offset:57344\n"
      "ds_read_b64_tr_b16 a[218:219], v9 offset:57856\n"
      "v_mfma_f32_16x16x32_bf16 v[92:95], a[228:231], v[212:215], v[92:95]\n"
      "v_cvt_pk_bf16_f32 v226, v116, v117\n"
      "v_cvt_pk_bf16_f32 v227, v118, v119\n"
      "v_mfma_f32_16x16x32_bf16 v[96:99], a[232:235], v[208:211], v[96:99]\n"
      "ds_read_b64_tr_b16 a[220:221], v10 offset:57344\n"
      "ds_read_b64_tr_b16 a[222:223], v10 offset:57856\n"
      "v_mfma_f32_16x16x32_bf16 v[100:103], a[232:235], v[212:215], v[100:103]\n"
      "buffer_load_dwordx4 v18, s[24:27], s64 offen offset:3072 lds\n"
      "v_cvt_pk_bf16_f32 v228, v120, v121\n"
      "v_cvt_pk_bf16_f32 v229, v122, v123\n"
      "v_mfma_f32_16x16x32_bf16 v[104:107], a[236:239], v[208:211], v[104:107]\n"
      "ds_read_b64_tr_b16 a[224:225], v11 offset:57344\n"
      "ds_read_b64_tr_b16 a[226:227], v11 offset:57856\n"
      "v_mfma_f32_16x16x32_bf16 v[108:111], a[236:239], v[212:215], v[108:111]\n"
      "v_cvt_pk_bf16_f32 v230, v124, v125\n"
      "v_cvt_pk_bf16_f32 v231, v126, v127\n"
      "v_mfma_f32_4x4x4_16b_bf16 v[128:131], v[28:29], a[88:89], v[24:27]\n"
      "v_mfma_f32_4x4x4_16b_bf16 v[132:135], v[28:29], a[90:91], v[24:27]\n"
      "v_mfma_f32_4x4x4_16b_bf16 v[136:139], v[28:29], a[92:93], v[40:43]\n"
      "v_mfma_f32_4x4x4_16b_bf16 v[140:143], v[28:29], a[94:95], v[40:43]\n"
      "s_waitcnt lgkmcnt(6)\n"
      "v_mfma_f32_16x16x32_bf16 v[80:83], a[240:243], v[216:219], v[80:83]\n"
      "ds_read_b64_tr_b16 a[228:229], v12 offset:57344\n"
      "ds_read_b64_tr_b16 a[230:231], v12 offset:57856\n"
      "v_mfma_f32_16x16x32_bf16 v[84:87], a[240:243], v[220:223], v[84:87]\n"
      "s_lshr_b32 s35, s95, 1\n"
      "s_add_u32 m0, s35, 98304\n"
      "v_add_u32 v19, s99, v3\n"
      "v_xor_b32 v18, 64, v19\n"
      "buffer_load_dwordx4 v19, s[24:27], 0 offen offset:0 lds\n"
      "v_cvt_pk_bf16_f32 v232, v128, v129\n"
      "v_cvt_pk_bf16_f32 v233, v130, v131\n"
      "v_mfma_f32_16x16x32_bf16 v[88:91], a[244:247], v[216:219], v[88:91]\n"
      "ds_read_b64_tr_b16 a[232:233], v9 offset:61440\n"
      ".LPAD14:\n"
      "s_cbranch_execz .LRET\n"
      "ds_read_b64_tr_b16 a[234:235], v9 offset:61952\n"
      "v_mfma_f32_16x16x32_bf16 v[92:95], a[244:247], v[220:223], v[92:95]\n"
      "v_cvt_pk_bf16_f32 v234, v132, v133\n"
      "v_cvt_pk_bf16_f32 v235, v134, v135\n"
      "v_mfma_f32_16x16x32_bf16 v[96:99], a[248:251], v[216:219], v[96:99]\n"
      "ds_read_b64_tr_b16 a[236:237], v10 offset:61440\n"
      "ds_read_b64_tr_b16 a[238:239], v10 offset:61952\n"
      "v_mfma_f32_16x16x32_bf16 v[100:103], a[248:251], v[220:223], v[100:103]\n"
      "buffer_load_dwordx4 v18, s[24:27], s62 offen offset:1024 lds\n"
      "v_cvt_pk_bf16_f32 v236, v136, v137\n"
      "v_cvt_pk_bf16_f32 v237, v138, v139\n"
      "v_mfma_f32_16x16x32_bf16 v[104:107], a[252:255], v[216:219], v[104:107]\n"
      "ds_read_b64_tr_b16 a[240:241], v11 offset:61440\n"
      "ds_read_b64_tr_b16 a[242:243], v11 offset:61952\n"
      "v_mfma_f32_16x16x32_bf16 v[108:111], a[252:255], v[220:223], v[108:111]\n"
      "v_cvt_pk_bf16_f32 v238, v140, v141\n"
      "v_cvt_pk_bf16_f32 v239, v142, v143\n"
      "v_mfma_f32_4x4x4_16b_bf16 v[112:115], v[28:29], a[96:97], v[24:27]\n"
      "v_mfma_f32_4x4x4_16b_bf16 v[116:119], v[28:29], a[98:99], v[24:27]\n"
      "v_mfma_f32_4x4x4_16b_bf16 v[120:123], v[28:29], a[100:101], v[40:43]\n"
      "v_mfma_f32_4x4x4_16b_bf16 v[124:127], v[28:29], a[102:103], v[40:43]\n"
      "s_waitcnt vmcnt(16) lgkmcnt(6)\n"
      "s_barrier\n"
      "v_mfma_f32_16x16x32_bf16 v[80:83], a[216:219], v[224:227], v[80:83]\n"
      "ds_read_b64_tr_b16 a[244:245], v12 offset:61440\n"
      "ds_read_b64_tr_b16 a[246:247], v12 offset:61952\n"
      "v_mfma_f32_16x16x32_bf16 v[84:87], a[216:219], v[228:231], v[84:87]\n"
      "v_cvt_pk_bf16_f32 v240, v112, v113\n"
      "v_cvt_pk_bf16_f32 v241, v114, v115\n"
      "v_mfma_f32_16x16x32_bf16 v[88:91], a[220:223], v[224:227], v[88:91]\n"
      "ds_read_b64_tr_b16 a[248:249], v9 offset:16384\n"
      "ds_read_b64_tr_b16 a[250:251], v9 offset:16896\n"
      "v_mfma_f32_16x16x32_bf16 v[92:95], a[220:223], v[228:231], v[92:95]\n"
      "s_add_u32 m0, s95, 49152\n"
      "v_add_u32 v3, 128, v3\n"
      "v_add_u32 v19, s34, v3\n"
      "v_xor_b32 v18, 64, v19\n"
      "buffer_load_dwordx4 v19, s[24:27], 0 offen offset:0 lds\n"
      "v_cvt_pk_bf16_f32 v242, v116, v117\n"
      "v_cvt_pk_bf16_f32 v243, v118, v119\n"
      "v_mfma_f32_16x16x32_bf16 v[96:99], a[224:227], v[224:227], v[96:99]\n"
      "ds_read_b64_tr_b16 a[252:253], v10 offset:16384\n"
      "ds_read_b64_tr_b16 a[254:255], v10 offset:16896\n"
      "v_mfma_f32_16x16x32_bf16 v[100:103], a[224:227], v[228:231], v[100:103]\n"
      "v_cvt_pk_bf16_f32 v244, v120, v121\n"
      "v_cvt_pk_bf16_f32 v245, v122, v123\n"
      "v_mfma_f32_16x16x32_bf16 v[104:107], a[228:231], v[224:227], v[104:107]\n"
      "ds_read_b64_tr_b16 a[216:217], v11 offset:16384\n"
      "ds_read_b64_tr_b16 a[218:219], v11 offset:16896\n"
      "v_mfma_f32_16x16x32_bf16 v[108:111], a[228:231], v[228:231], v[108:111]\n"
      "buffer_load_dwordx4 v18, s[24:27], s62 offen offset:1024 lds\n"
      "v_cvt_pk_bf16_f32 v246, v124, v125\n"
      "v_cvt_pk_bf16_f32 v247, v126, v127\n"
      "v_mfma_f32_4x4x4_16b_bf16 v[128:131], v[28:29], a[104:105], v[24:27]\n"
      "v_mfma_f32_4x4x4_16b_bf16 v[132:135], v[28:29], a[106:107], v[24:27]\n"
      "v_mfma_f32_4x4x4_16b_bf16 v[136:139], v[28:29], a[108:109], v[40:43]\n"
      "v_mfma_f32_4x4x4_16b_bf16 v[140:143], v[28:29], a[110:111], v[40:43]\n"
      "s_waitcnt lgkmcnt(6)\n"
      "v_mfma_f32_16x16x32_bf16 v[80:83], a[232:235], v[232:235], v[80:83]\n"
      "ds_read_b64_tr_b16 a[220:221], v12 offset:16384\n"
      "ds_read_b64_tr_b16 a[222:223], v12 offset:16896\n"
      "v_mfma_f32_16x16x32_bf16 v[84:87], a[232:235], v[236:239], v[84:87]\n"
      "v_cvt_pk_bf16_f32 v248, v128, v129\n"
      "v_cvt_pk_bf16_f32 v249, v130, v131\n"
      "v_mfma_f32_16x16x32_bf16 v[88:91], a[236:239], v[232:235], v[88:91]\n"
      "ds_read_b64_tr_b16 a[224:225], v9 offset:20480\n"
      "ds_read_b64_tr_b16 a[226:227], v9 offset:20992\n"
      "v_mfma_f32_16x16x32_bf16 v[92:95], a[236:239], v[236:239], v[92:95]\n"
      "buffer_load_dwordx4 v19, s[24:27], s63 offen offset:2048 lds\n"
      "v_cvt_pk_bf16_f32 v250, v132, v133\n"
      "v_cvt_pk_bf16_f32 v251, v134, v135\n"
      "v_mfma_f32_16x16x32_bf16 v[96:99], a[240:243], v[232:235], v[96:99]\n"
      "ds_read_b64_tr_b16 a[228:229], v10 offset:20480\n"
      "ds_read_b64_tr_b16 a[230:231], v10 offset:20992\n"
      "v_mfma_f32_16x16x32_bf16 v[100:103], a[240:243], v[236:239], v[100:103]\n"
      "v_cvt_pk_bf16_f32 v252, v136, v137\n"
      "v_cvt_pk_bf16_f32 v253, v138, v139\n"
      "v_mfma_f32_16x16x32_bf16 v[104:107], a[244:247], v[232:235], v[104:107]\n"
      "ds_read_b64_tr_b16 a[232:233], v11 offset:20480\n"
      "ds_read_b64_tr_b16 a[234:235], v11 offset:20992\n"
      "v_mfma_f32_16x16x32_bf16 v[108:111], a[244:247], v[236:239], v[108:111]\n"
      "buffer_load_dwordx4 v18, s[24:27], s64 offen offset:3072 lds\n"
      "v_cvt_pk_bf16_f32 v254, v140, v141\n"
      "v_cvt_pk_bf16_f32 v255, v142, v143\n"
      "s_waitcnt vmcnt(10) lgkmcnt(6)\n"
      "s_barrier\n"
      "v_mfma_f32_16x16x32_bf16 v[80:83], a[248:251], v[240:243], v[80:83]\n"
      "ds_read_b64_tr_b16 a[236:237], v12 offset:20480\n"
      "ds_read_b64_tr_b16 a[238:239], v12 offset:20992\n"
      "v_mfma_f32_16x16x32_bf16 v[84:87], a[248:251], v[244:247], v[84:87]\n"
      "v_xor_b32 v9, 0x0, v6\n"
      "v_mfma_f32_16x16x32_bf16 v[88:91], a[252:255], v[240:243], v[88:91]\n"
      "ds_read_b64_tr_b16 a[240:241], v9 offset:0\n"
      "ds_read_b64_tr_b16 a[242:243], v9 offset:512\n"
      "v_mfma_f32_16x16x32_bf16 v[92:95], a[252:255], v[244:247], v[92:95]\n"
      "s_add_u32 m0, s95, 65536\n"
      "v_add_u32 v19, s86, v3\n"
      "v_xor_b32 v18, 64, v19\n"
      "buffer_load_dwordx4 v19, s[24:27], 0 offen offset:0 lds\n"
      "v_xor_b32 v10, 0x20, v6\n"
      "v_mfma_f32_16x16x32_bf16 v[96:99], a[216:219], v[240:243], v[96:99]\n"
      "ds_read_b64_tr_b16 a[244:245], v10 offset:0\n"
      "ds_read_b64_tr_b16 a[246:247], v10 offset:512\n"
      "v_mfma_f32_16x16x32_bf16 v[100:103], a[216:219], v[244:247], v[100:103]\n"
      "v_xor_b32 v11, 0x40, v6\n"
      "v_mfma_f32_16x16x32_bf16 v[104:107], a[220:223], v[240:243], v[104:107]\n"
      "ds_read_b64_tr_b16 a[248:249], v11 offset:0\n"
      "ds_read_b64_tr_b16 a[250:251], v11 offset:512\n"
      "v_mfma_f32_16x16x32_bf16 v[108:111], a[220:223], v[244:247], v[108:111]\n"
      "buffer_load_dwordx4 v18, s[24:27], s62 offen offset:1024 lds\n"
      "v_xor_b32 v12, 0x60, v6\n"
      "s_waitcnt lgkmcnt(6)\n"
      "v_mfma_f32_16x16x32_bf16 v[80:83], a[224:227], v[248:251], v[80:83]\n"
      "ds_read_b64_tr_b16 a[252:253], v12 offset:0\n"
      "ds_read_b64_tr_b16 a[254:255], v12 offset:512\n"
      "v_mfma_f32_16x16x32_bf16 v[84:87], a[224:227], v[252:255], v[84:87]\n"
      "v_readfirstlane_b32 s96, v16\n"
      "v_readfirstlane_b32 s97, v17\n"
      "v_mfma_f32_16x16x32_bf16 v[88:91], a[228:231], v[248:251], v[88:91]\n"
      "ds_read_b64_tr_b16 a[216:217], v9 offset:4096\n"
      "ds_read_b64_tr_b16 a[218:219], v9 offset:4608\n"
      "v_mfma_f32_16x16x32_bf16 v[92:95], a[228:231], v[252:255], v[92:95]\n"
      "buffer_load_dwordx4 v19, s[24:27], s63 offen offset:2048 lds\n"
      "s_add_u32 s96, s96, 0x7f4a7c15\n"
      "s_addc_u32 s97, s97, 0x9e3779b9\n"
      "v_mfma_f32_16x16x32_bf16 v[96:99], a[232:235], v[248:251], v[96:99]\n"
      "ds_read_b64_tr_b16 a[220:221], v10 offset:4096\n"
      "ds_read_b64_tr_b16 a[222:223], v10 offset:4608\n"
      "v_mfma_f32_16x16x32_bf16 v[100:103], a[232:235], v[252:255], v[100:103]\n"
      "v_mov_b32 v16, s96\n"
      "v_mov_b32 v17, s97\n"
      "v_mfma_f32_16x16x32_bf16 v[104:107], a[236:239], v[248:251], v[104:107]\n"
      "ds_read_b64_tr_b16 a[224:225], v11 offset:4096\n"
      "ds_read_b64_tr_b16 a[226:227], v11 offset:4608\n"
      "v_mfma_f32_16x16x32_bf16 v[108:111], a[236:239], v[252:255], v[108:111]\n"
      "buffer_load_dwordx4 v18, s[24:27], s64 offen offset:3072 lds\n"
      "s_waitcnt lgkmcnt(6)\n"
      "v_mfma_f32_16x16x32_bf16 v[112:115], a[240:243], v[144:147], 0\n"
      "ds_read_b64_tr_b16 a[228:229], v12 offset:4096\n"
      "ds_read_b64_tr_b16 a[230:231], v12 offset:4608\n"
      "v_mfma_f32_16x16x32_bf16 v[116:119], a[240:243], v[148:151], 0\n"
      "v_mfma_f32_16x16x32_bf16 v[120:123], a[244:247], v[144:147], 0\n"
      "ds_read_b64_tr_b16 a[232:233], v9 offset:8192\n"
      "ds_read_b64_tr_b16 a[234:235], v9 offset:8704\n"
      "v_mfma_f32_16x16x32_bf16 v[124:127], a[244:247], v[148:151], 0\n"
      "s_add_u32 m0, s95, 81920\n"
      "v_add_u32 v19, s87, v3\n"
      "v_xor_b32 v18, 64, v19\n"
      "buffer_load_dwordx4 v19, s[24:27], 0 offen offset:0 lds\n"
      "v_mfma_f32_16x16x32_bf16 v[128:131], a[248:251], v[144:147], 0\n"
      "ds_read_b64_tr_b16 a[236:237], v10 offset:8192\n"
      "ds_read_b64_tr_b16 a[238:239], v10 offset:8704\n"
      "v_mfma_f32_16x16x32_bf16 v[132:135], a[248:251], v[148:151], 0\n"
      "v_mfma_f32_16x16x32_bf16 v[136:139], a[252:255], v[144:147], 0\n"
      "ds_read_b64_tr_b16 a[240:241], v11 offset:8192\n"
      "ds_read_b64_tr_b16 a[242:243], v11 offset:8704\n"
      "v_mfma_f32_16x16x32_bf16 v[140:143], a[252:255], v[148:151], 0\n"
      "buffer_load_dwordx4 v18, s[24:27], s62 offen offset:1024 lds\n"
      "s_waitcnt lgkmcnt(6)\n"
      "v_mfma_f32_16x16x32_bf16 v[112:115], a[216:219], v[152:155], v[112:115]\n"
      "ds_read_b64_tr_b16 a[244:245], v12 offset:8192\n"
      "ds_read_b64_tr_b16 a[246:247], v12 offset:8704\n"
      "v_mfma_f32_16x16x32_bf16 v[116:119], a[216:219], v[156:159], v[116:119]\n"
      "v_cvt_pk_bf16_f32 v32, v80, v81\n"
      "v_cvt_pk_bf16_f32 v33, v82, v83\n"
      "v_mfma_f32_16x16x32_bf16 v[120:123], a[220:223], v[152:155], v[120:123]\n"
      "ds_read_b64_tr_b16 a[248:249], v9 offset:12288\n"
      "ds_read_b64_tr_b16 a[250:251], v9 offset:12800\n"
      "v_mfma_f32_16x16x32_bf16 v[124:127], a[220:223], v[156:159], v[124:127]\n"
      "buffer_load_dwordx4 v19, s[24:27], s63 offen offset:2048 lds\n"
      "v_mfma_f32_16x16x32_bf16 v[128:131], a[224:227], v[152:155], v[128:131]\n"
      "ds_read_b64_tr_b16 a[252:253], v10 offset:12288\n"
      "ds_read_b64_tr_b16 a[254:255], v10 offset:12800\n"
      "v_mfma_f32_16x16x32_bf16 v[132:135], a[224:227], v[156:159], v[132:135]\n"
      "v_cvt_pk_bf16_f32 v34, v84, v85\n"
      "v_cvt_pk_bf16_f32 v35, v86, v87\n"
      "v_cvt_pk_bf16_f32 v36, v88, v89\n"
      "v_mfma_f32_16x16x32_bf16 v[136:139], a[228:231], v[152:155], v[136:139]\n"
      "ds_read_b64_tr_b16 a[216:217], v11 offset:12288\n"
      "ds_read_b64_tr_b16 a[218:219], v11 offset:12800\n"
      "v_mfma_f32_16x16x32_bf16 v[140:143], a[228:231], v[156:159], v[140:143]\n"
      "buffer_load_dwordx4 v18, s[24:27], s64 offen offset:3072 lds\n"
      "s_waitcnt lgkmcnt(6)\n"
      "v_mfma_f32_16x16x32_bf16 v[112:115], a[232:235], v[160:163], v[112:115]\n"
      "ds_read_b64_tr_b16 a[220:221], v12 offset:12288\n"
      "ds_read_b64_tr_b16 a[222:223], v12 offset:12800\n"
      "v_mfma_f32_16x16x32_bf16 v[116:119], a[232:235], v[164:167], v[116:119]\n"
      "v_cvt_pk_bf16_f32 v37, v90, v91\n"
      "v_cvt_pk_bf16_f32 v38, v92, v93\n"
      "v_mfma_f32_16x16x32_bf16 v[120:123], a[236:239], v[160:163], v[120:123]\n"
      "ds_read_b64_tr_b16 a[224:225], v9 offset:16384\n"
      "ds_read_b64_tr_b16 a[226:227], v9 offset:16896\n"
      "v_mfma_f32_16x16x32_bf16 v[124:127], a[236:239], v[164:167], v[124:127]\n"
      "s_lshr_b32 s35, s95, 1\n"
      "s_add_u32 m0, s35, 106496\n"
      "v_add_u32 v19, s99, v3\n"
      "v_xor_b32 v18, 64, v19\n"
      "buffer_load_dwordx4 v19, s[24:27], 0 offen offset:0 lds\n"
      "v_mfma_f32_16x16x32_bf16 v[128:131], a[240:243], v[160:163], v[128:131]\n"
      "ds_read_b64_tr_b16 a[228:229], v10 offset:16384\n"
      "ds_read_b64_tr_b16 a[230:231], v10 offset:16896\n"
      "v_mfma_f32_16x16x32_bf16 v[132:135], a[240:243], v[164:167], v[132:135]\n"
      "v_cvt_pk_bf16_f32 v39, v94, v95\n"
      "v_permlane16_swap_b32 v32, v34\n"
      "v_mfma_f32_16x16x32_bf16 v[136:139], a[244:247], v[160:163], v[136:139]\n"
      "ds_read_b64_tr_b16 a[232:233], v11 offset:16384\n"
      "ds_read_b64_tr_b16 a[234:235], v11 offset:16896\n"
      "v_mfma_f32_16x16x32_bf16 v[140:143], a[244:247], v[164:167], v[140:143]\n"
      "buffer_load_dwordx4 v18, s[24:27], s62 offen offset:1024 lds\n"
      "v_permlane16_swap_b32 v33, v35\n"
      "s_waitcnt lgkmcnt(6)\n"
      "v_mfma_f32_16x16x32_bf16 v[112:115], a[248:251], v[168:171], v[112:115]\n"
      "ds_read_b64_tr_b16 a[236:237], v12 offset:16384\n"
      "ds_read_b64_tr_b16 a[238:239], v12 offset:16896\n"
      "v_mfma_f32_16x16x32_bf16 v[116:119], a[248:251], v[172:175], v[116:119]\n"
      "v_permlane16_swap_b32 v36, v38\n"
      "v_mfma_f32_16x16x32_bf16 v[120:123], a[252:255], v[168:171], v[120:123]\n"
      "ds_read_b64_tr_b16 a[240:241], v9 offset:20480\n"
      "ds_read_b64_tr_b16 a[242:243], v9 offset:20992\n"
      "v_mfma_f32_16x16x32_bf16 v[124:127], a[252:255], v[172:175], v[124:127]\n"
      "v_permlane16_swap_b32 v37, v39\n"
      "v_cvt_pk_bf16_f32 v40, v96, v97\n"
      "v_cvt_pk_bf16_f32 v41, v98, v99\n"
      "v_mfma_f32_16x16x32_bf16 v[128:131], a[216:219], v[168:171], v[128:131]\n"
      "ds_read_b64_tr_b16 a[244:245], v10 offset:20480\n"
      "ds_read_b64_tr_b16 a[246:247], v10 offset:20992\n"
      "v_mfma_f32_16x16x32_bf16 v[132:135], a[216:219], v[172:175], v[132:135]\n"
      "v_cvt_pk_bf16_f32 v42, v100, v101\n"
      "v_cvt_pk_bf16_f32 v43, v102, v103\n"
      "v_mfma_f32_16x16x32_bf16 v[136:139], a[220:223], v[168:171], v[136:139]\n"
      "ds_read_b64_tr_b16 a[248:249], v11 offset:20480\n"
      "ds_read_b64_tr_b16 a[250:251], v11 offset:20992\n"
      "v_mfma_f32_16x16x32_bf16 v[140:143], a[220:223], v[172:175], v[140:143]\n"
      "v_cvt_pk_bf16_f32 v44, v104, v105\n"
      "v_cvt_pk_bf16_f32 v45, v106, v107\n"
      "v_cvt_pk_bf16_f32 v46, v108, v109\n"
      "s_waitcnt vmcnt(14) lgkmcnt(6)\n"
      "s_barrier\n"
      "v_mfma_f32_16x16x32_bf16 v[112:115], a[224:227], v[176:179], v[112:115]\n"
      "ds_read_b64_tr_b16 a[252:253], v12 offset:20480\n"
      "ds_read_b64_tr_b16 a[254:255], v12 offset:20992\n"
      "v_mfma_f32_16x16x32_bf16 v[116:119], a[224:227], v[180:183], v[116:119]\n"
      "s_add_u32 m0, s95, 114688\n"
      "v_add_u32 v3, 4294966912, v3\n"
      "v_add_u32 v19, s34, v3\n"
      "v_xor_b32 v18, 64, v19\n"
      "buffer_load_dwordx4 v19, s[24:27], 0 offen offset:0 lds\n"
      "v_mfma_f32_16x16x32_bf16 v[120:123], a[228:231], v[176:179], v[120:123]\n"
      "ds_read_b64_tr_b16 a[216:217], v9 offset:24576\n"
      "ds_read_b64_tr_b16 a[218:219], v9 offset:25088\n"
      "v_mfma_f32_16x16x32_bf16 v[124:127], a[228:231], v[180:183], v[124:127]\n"
      "v_cvt_pk_bf16_f32 v47, v110, v111\n"
      "v_permlane16_swap_b32 v40, v42\n"
      "v_mfma_f32_16x16x32_bf16 v[128:131], a[232:235], v[176:179], v[128:131]\n"
      "ds_read_b64_tr_b16 a[220:221], v10 offset:24576\n"
      "ds_read_b64_tr_b16 a[222:223], v10 offset:25088\n"
      "v_mfma_f32_16x16x32_bf16 v[132:135], a[232:235], v[180:183], v[132:135]\n"
      "buffer_load_dwordx4 v18, s[24:27], s62 offen offset:1024 lds\n"
      "v_permlane16_swap_b32 v41, v43\n"
      "v_mfma_f32_16x16x32_bf16 v[136:139], a[236:239], v[176:179], v[136:139]\n"
      "ds_read_b64_tr_b16 a[224:225], v11 offset:24576\n"
      "ds_read_b64_tr_b16 a[226:227], v11 offset:25088\n"
      "v_mfma_f32_16x16x32_bf16 v[140:143], a[236:239], v[180:183], v[140:143]\n"
      "v_permlane16_swap_b32 v44, v46\n"
      "v_permlane16_swap_b32 v45, v47\n"
      "s_waitcnt lgkmcnt(6)\n"
      "v_mfma_f32_16x16x32_bf16 v[112:115], a[240:243], v[184:187], v[112:115]\n"
      "ds_read_b64_tr_b16 a[228:229], v12 offset:24576\n"
      "ds_read_b64_tr_b16 a[230:231], v12 offset:25088\n"
      "v_mfma_f32_16x16x32_bf16 v[116:119], a[240:243], v[188:191], v[116:119]\n"
      "buffer_load_dwordx4 v19, s[24:27], s63 offen offset:2048 lds\n"
      "v_mfma_f32_16x16x32_bf16 v[120:123], a[244:247], v[184:187], v[120:123]\n"
      "ds_read_b64_tr_b16 a[232:233], v9 offset:28672\n"
      "ds_read_b64_tr_b16 a[234:235], v9 offset:29184\n"
      "v_mfma_f32_16x16x32_bf16 v[124:127], a[244:247], v[188:191], v[124:127]\n"
      "v_mfma_f32_16x16x32_bf16 v[128:131], a[248:251], v[184:187], v[128:131]\n"
      "ds_read_b64_tr_b16 a[236:237], v10 offset:28672\n"
      "ds_read_b64_tr_b16 a[238:239], v10 offset:29184\n"
      "v_mfma_f32_16x16x32_bf16 v[132:135], a[248:251], v[188:191], v[132:135]\n"
      "buffer_load_dwordx4 v18, s[24:27], s64 offen offset:3072 lds\n"
      "v_mfma_f32_16x16x32_bf16 v[136:139], a[252:255], v[184:187], v[136:139]\n"
      "ds_read_b64_tr_b16 a[240:241], v11 offset:28672\n"
      "ds_read_b64_tr_b16 a[242:243], v11 offset:29184\n"
      "v_mfma_f32_16x16x32_bf16 v[140:143], a[252:255], v[188:191], v[140:143]\n"
      "s_waitcnt lgkmcnt(6)\n"
      "v_mfma_f32_16x16x32_bf16 v[112:115], a[216:219], v[192:195], v[112:115]\n"
      "ds_read_b64_tr_b16 a[244:245], v12 offset:28672\n"
      "ds_read_b64_tr_b16 a[246:247], v12 offset:29184\n"
      "v_mfma_f32_16x16x32_bf16 v[116:119], a[216:219], v[196:199], v[116:119]\n"
      "s_add_u32 m0, s95, 131072\n"
      "v_add_u32 v19, s86, v3\n"
      "v_xor_b32 v18, 64, v19\n"
      "buffer_load_dwordx4 v19, s[24:27], 0 offen offset:0 lds\n"
      "v_mfma_f32_16x16x32_bf16 v[120:123], a[220:223], v[192:195], v[120:123]\n"
      "ds_read_b64_tr_b16 a[248:249], v9 offset:32768\n"
      "ds_read_b64_tr_b16 a[250:251], v9 offset:33280\n"
      "v_mfma_f32_16x16x32_bf16 v[124:127], a[220:223], v[196:199], v[124:127]\n"
      "v_mfma_f32_16x16x32_bf16 v[128:131], a[224:227], v[192:195], v[128:131]\n"
      "ds_read_b64_tr_b16 a[252:253], v10 offset:32768\n"
      "ds_read_b64_tr_b16 a[254:255], v10 offset:33280\n"
      "v_mfma_f32_16x16x32_bf16 v[132:135], a[224:227], v[196:199], v[132:135]\n"
      "buffer_load_dwordx4 v18, s[24:27], s62 offen offset:1024 lds\n"
      "v_mfma_f32_16x16x32_bf16 v[136:139], a[228:231], v[192:195], v[136:139]\n"
      "ds_read_b64_tr_b16 a[216:217], v11 offset:32768\n"
      "ds_read_b64_tr_b16 a[218:219], v11 offset:33280\n"
      "v_mfma_f32_16x16x32_bf16 v[140:143], a[228:231], v[196:199], v[140:143]\n"
      "s_waitcnt lgkmcnt(6)\n"
      "v_mfma_f32_16x16x32_bf16 v[112:115], a[232:235], v[200:203], v[112:115]\n"
      "ds_read_b64_tr_b16 a[220:221], v12 offset:32768\n"
      "ds_read_b64_tr_b16 a[222:223], v12 offset:33280\n"
      "v_mfma_f32_16x16x32_bf16 v[116:119], a[232:235], v[204:207], v[116:119]\n"
      "buffer_load_dwordx4 v19, s[24:27], s63 offen offset:2048 lds\n"
      "v_mfma_f32_16x16x32_bf16 v[120:123], a[236:239], v[200:203], v[120:123]\n"
      "ds_read_b64_tr_b16 a[224:225], v9 offset:36864\n"
      "ds_read_b64_tr_b16 a[226:227], v9 offset:37376\n"
      "v_mfma_f32_16x16x32_bf16 v[124:127], a[236:239], v[204:207], v[124:127]\n"
      "v_mfma_f32_16x16x32_bf16 v[128:131], a[240:243], v[200:203], v[128:131]\n"
      "ds_read_b64_tr_b16 a[228:229], v10 offset:36864\n"
      "ds_read_b64_tr_b16 a[230:231], v10 offset:37376\n"
      "v_mfma_f32_16x16x32_bf16 v[132:135], a[240:243], v[204:207], v[132:135]\n"
      "buffer_load_dwordx4 v18, s[24:27], s64 offen offset:3072 lds\n"
      "v_mfma_f32_16x16x32_bf16 v[136:139], a[244:247], v[200:203], v[136:139]\n"
      "ds_read_b64_tr_b16 a[232:233], v11 offset:36864\n"
      "ds_read_b64_tr_b16 a[234:235], v11 offset:37376\n"
      "v_mfma_f32_16x16x32_bf16 v[140:143], a[244:247], v[204:207], v[140:143]\n"
      "s_waitcnt lgkmcnt(6)\n"
      "v_mfma_f32_16x16x32_bf16 v[112:115], a[248:251], v[208:211], v[112:115]\n"
      "ds_read_b64_tr_b16 a[236:237], v12 offset:36864\n"
      "ds_read_b64_tr_b16 a[238:239], v12 offset:37376\n"
      "v_mfma_f32_16x16x32_bf16 v[116:119], a[248:251], v[212:215], v[116:119]\n"
      "s_add_u32 m0, s95, 147456\n"
      "v_add_u32 v19, s87, v3\n"
      "v_xor_b32 v18, 64, v19\n"
      "buffer_load_dwordx4 v19, s[24:27], 0 offen offset:0 lds\n"
      "v_mfma_f32_16x16x32_bf16 v[120:123], a[252:255], v[208:211], v[120:123]\n"
      "ds_read_b64_tr_b16 a[240:241], v9 offset:40960\n"
      "ds_read_b64_tr_b16 a[242:243], v9 offset:41472\n"
      "v_mfma_f32_16x16x32_bf16 v[124:127], a[252:255], v[212:215], v[124:127]\n"
      "v_mfma_f32_16x16x32_bf16 v[128:131], a[216:219], v[208:211], v[128:131]\n"
      "ds_read_b64_tr_b16 a[244:245], v10 offset:40960\n"
      "ds_read_b64_tr_b16 a[246:247], v10 offset:41472\n"
      "v_mfma_f32_16x16x32_bf16 v[132:135], a[216:219], v[212:215], v[132:135]\n"
      "buffer_load_dwordx4 v18, s[24:27], s62 offen offset:1024 lds\n"
      "v_mfma_f32_16x16x32_bf16 v[136:139], a[220:223], v[208:211], v[136:139]\n"
      "ds_read_b64_tr_b16 a[248:249], v11 offset:40960\n"
      "ds_read_b64_tr_b16 a[250:251], v11 offset:41472\n"
      "v_mfma_f32_16x16x32_bf16 v[140:143], a[220:223], v[212:215], v[140:143]\n"
      "s_waitcnt lgkmcnt(6)\n"
      "v_mfma_f32_16x16x32_bf16 v[112:115], a[224:227], v[216:219], v[112:115]\n"
      "ds_read_b64_tr_b16 a[252:253], v12 offset:40960\n"
      "ds_read_b64_tr_b16 a[254:255], v12 offset:41472\n"
      "v_mfma_f32_16x16x32_bf16 v[116:119], a[224:227], v[220:223], v[116:119]\n"
      "buffer_load_dwordx4 v19, s[24:27], s63 offen offset:2048 lds\n"
      "v_mfma_f32_16x16x32_bf16 v[120:123], a[228:231], v[216:219], v[120:123]\n"
      "ds_read_b64_tr_b16 a[216:217], v9 offset:45056\n"
      "ds_read_b64_tr_b16 a[218:219], v9 offset:45568\n"
      "v_mfma_f32_16x16x32_bf16 v[124:127], a[228:231], v[220:223], v[124:127]\n"
      "v_mfma_f32_16x16x32_bf16 v[128:131], a[232:235], v[216:219], v[128:131]\n"
      "ds_read_b64_tr_b16 a[220:221], v10 offset:45056\n"
      "ds_read_b64_tr_b16 a[222:223], v10 offset:45568\n"
      "v_mfma_f32_16x16x32_bf16 v[132:135], a[232:235], v[220:223], v[132:135]\n"
      "buffer_load_dwordx4 v18, s[24:27], s64 offen offset:3072 lds\n"
      "v_mfma_f32_16x16x32_bf16 v[136:139], a[236:239], v[216:219], v[136:139]\n"
      "ds_read_b64_tr_b16 a[224:225], v11 offset:45056\n"
      "ds_read_b64_tr_b16 a[226:227], v11 offset:45568\n"
      "v_mfma_f32_16x16x32_bf16 v[140:143], a[236:239], v[220:223], v[140:143]\n"
      "s_waitcnt lgkmcnt(6)\n"
      "v_mfma_f32_16x16x32_bf16 v[112:115], a[240:243], v[224:227], v[112:115]\n"
      "ds_read_b64_tr_b16 a[228:229], v12 offset:45056\n"
      "ds_read_b64_tr_b16 a[230:231], v12 offset:45568\n"
      "v_mfma_f32_16x16x32_bf16 v[116:119], a[240:243], v[228:231], v[116:119]\n"
      "s_lshr_b32 s35, s95, 1\n"
      "s_add_u32 m0, s35, 0\n"
      "v_add_u32 v19, s99, v3\n"
      "v_xor_b32 v18, 64, v19\n"
      "buffer_load_dwordx4 v19, s[24:27], 0 offen offset:0 lds\n"
      "v_xor_b32 v9, 0xc000, v6\n"
      "v_mfma_f32_16x16x32_bf16 v[120:123], a[244:247], v[224:227], v[120:123]\n"
      "ds_read_b64_tr_b16 a[232:233], v9 offset:49152\n"
      "ds_read_b64_tr_b16 a[234:235], v9 offset:49664\n"
      "v_mfma_f32_16x16x32_bf16 v[124:127], a[244:247], v[228:231], v[124:127]\n"
      "v_xor_b32 v10, 0xc020, v6\n"
      "v_mfma_f32_16x16x32_bf16 v[128:131], a[248:251], v[224:227], v[128:131]\n"
      "ds_read_b64_tr_b16 a[236:237], v10 offset:49152\n"
      "ds_read_b64_tr_b16 a[238:239], v10 offset:49664\n"
      "v_mfma_f32_16x16x32_bf16 v[132:135], a[248:251], v[228:231], v[132:135]\n"
      "buffer_load_dwordx4 v18, s[24:27], s62 offen offset:1024 lds\n"
      "v_xor_b32 v11, 0xc040, v6\n"
      "v_mfma_f32_16x16x32_bf16 v[136:139], a[252:255], v[224:227], v[136:139]\n"
      "ds_read_b64_tr_b16 a[240:241], v11 offset:49152\n"
      "ds_read_b64_tr_b16 a[242:243], v11 offset:49664\n"
      "v_mfma_f32_16x16x32_bf16 v[140:143], a[252:255], v[228:231], v[140:143]\n"
      "v_xor_b32 v12, 0xc060, v6\n"
      "s_waitcnt lgkmcnt(6)\n"
      "v_mfma_f32_16x16x32_bf16 v[112:115], a[216:219], v[232:235], v[112:115]\n"
      "ds_read_b64_tr_b16 a[244:245], v12 offset:49152\n"
      "ds_read_b64_tr_b16 a[246:247], v12 offset:49664\n"
      "v_mfma_f32_16x16x32_bf16 v[116:119], a[216:219], v[236:239], v[116:119]\n"
      "v_mfma_f32_16x16x32_bf16 v[120:123], a[220:223], v[232:235], v[120:123]\n"
      "ds_read_b64_tr_b16 a[248:249], v9 offset:53248\n"
      "ds_read_b64_tr_b16 a[250:251], v9 offset:53760\n"
      "v_mfma_f32_16x16x32_bf16 v[124:127], a[220:223], v[236:239], v[124:127]\n"
      "v_mfma_f32_16x16x32_bf16 v[128:131], a[224:227], v[232:235], v[128:131]\n"
      "ds_read_b64_tr_b16 a[252:253], v10 offset:53248\n"
      "ds_read_b64_tr_b16 a[254:255], v10 offset:53760\n"
      "v_mfma_f32_16x16x32_bf16 v[132:135], a[224:227], v[236:239], v[132:135]\n"
      "v_mfma_f32_16x16x32_bf16 v[136:139], a[228:231], v[232:235], v[136:139]\n"
      "ds_read_b64_tr_b16 a[216:217], v11 offset:53248\n"
      "ds_read_b64_tr_b16 a[218:219], v11 offset:53760\n"
      "v_mfma_f32_16x16x32_bf16 v[140:143], a[228:231], v[236:239], v[140:143]\n"
      "s_waitcnt vmcnt(14) lgkmcnt(6)\n"
      "s_barrier\n"
      "v_mfma_f32_16x16x32_bf16 v[112:115], a[232:235], v[240:243], v[112:115]\n"
      "ds_read_b64_tr_b16 a[220:221], v12 offset:53248\n"
      "ds_read_b64_tr_b16 a[222:223], v12 offset:53760\n"
      "v_mfma_f32_16x16x32_bf16 v[116:119], a[232:235], v[244:247], v[116:119]\n"
      "v_mfma_f32_16x16x32_bf16 v[120:123], a[236:239], v[240:243], v[120:123]\n"
      "ds_read_b64_tr_b16 a[224:225], v9 offset:0\n"
      "ds_read_b64_tr_b16 a[226:227], v9 offset:512\n"
      "v_mfma_f32_16x16x32_bf16 v[124:127], a[236:239], v[244:247], v[124:127]\n"
      "v_mfma_f32_16x16x32_bf16 v[128:131], a[240:243], v[240:243], v[128:131]\n"
      "ds_read_b64_tr_b16 a[228:229], v10 offset:0\n"
      "ds_read_b64_tr_b16 a[230:231], v10 offset:512\n"
      "v_mfma_f32_16x16x32_bf16 v[132:135], a[240:243], v[244:247], v[132:135]\n"
      "v_mfma_f32_16x16x32_bf16 v[136:139], a[244:247], v[240:243], v[136:139]\n"
      "ds_read_b64_tr_b16 a[232:233], v11 offset:0\n"
      "ds_read_b64_tr_b16 a[234:235], v11 offset:512\n"
      "v_mfma_f32_16x16x32_bf16 v[140:143], a[244:247], v[244:247], v[140:143]\n"
      "s_waitcnt lgkmcnt(6)\n"
      "v_mfma_f32_16x16x32_bf16 v[112:115], a[248:251], v[248:251], v[112:115]\n"
      "ds_read_b64_tr_b16 a[236:237], v12 offset:0\n"
      "ds_read_b64_tr_b16 a[238:239], v12 offset:512\n"
      "v_mfma_f32_16x16x32_bf16 v[116:119], a[248:251], v[252:255], v[116:119]\n"
      "v_mfma_f32_16x16x32_bf16 v[120:123], a[252:255], v[248:251], v[120:123]\n"
      "ds_read_b64_tr_b16 a[240:241], v9 offset:4096\n"
      "ds_read_b64_tr_b16 a[242:243], v9 offset:4608\n"
      "v_mfma_f32_16x16x32_bf16 v[124:127], a[252:255], v[252:255], v[124:127]\n"
      "v_mfma_f32_16x16x32_bf16 v[128:131], a[216:219], v[248:251], v[128:131]\n"
      "ds_read_b64_tr_b16 a[244:245], v10 offset:4096\n"
      "ds_read_b64_tr_b16 a[246:247], v10 offset:4608\n"
      "v_mfma_f32_16x16x32_bf16 v[132:135], a[216:219], v[252:255], v[132:135]\n"
      "v_mfma_f32_16x16x32_bf16 v[136:139], a[220:223], v[248:251], v[136:139]\n"
      "ds_read_b64_tr_b16 a[248:249], v11 offset:4096\n"
      "ds_read_b64_tr_b16 a[250:251], v11 offset:4608\n"
      "v_mfma_f32_16x16x32_bf16 v[140:143], a[220:223], v[252:255], v[140:143]\n"
      "s_waitcnt lgkmcnt(6)\n"
      "v_mfma_f32_16x16x32_bf16 v[80:83], a[224:227], v[144:147], 0\n"
      "ds_read_b64_tr_b16 a[252:253], v12 offset:4096\n"
      "ds_read_b64_tr_b16 a[254:255], v12 offset:4608\n"
      "v_mfma_f32_16x16x32_bf16 v[84:87], a[224:227], v[148:151], 0\n"
      "v_mfma_f32_16x16x32_bf16 v[88:91], a[228:231], v[144:147], 0\n"
      "ds_read_b64_tr_b16 a[216:217], v9 offset:8192\n"
      "ds_read_b64_tr_b16 a[218:219], v9 offset:8704\n"
      "v_mfma_f32_16x16x32_bf16 v[92:95], a[228:231], v[148:151], 0\n"
      "v_mfma_f32_16x16x32_bf16 v[96:99], a[232:235], v[144:147], 0\n"
      "ds_read_b64_tr_b16 a[220:221], v10 offset:8192\n"
      "ds_read_b64_tr_b16 a[222:223], v10 offset:8704\n"
      "v_mfma_f32_16x16x32_bf16 v[100:103], a[232:235], v[148:151], 0\n"
      "v_mfma_f32_16x16x32_bf16 v[104:107], a[236:239], v[144:147], 0\n"
      "ds_read_b64_tr_b16 a[224:225], v11 offset:8192\n"
      "ds_read_b64_tr_b16 a[226:227], v11 offset:8704\n"
      "v_mfma_f32_16x16x32_bf16 v[108:111], a[236:239], v[148:151], 0\n"
      "v_cvt_pk_bf16_f32 v48, v112, v113\n"
      "v_cvt_pk_bf16_f32 v49, v114, v115\n"
      "s_waitcnt lgkmcnt(6)\n"
      "v_mfma_f32_16x16x32_bf16 v[80:83], a[240:243], v[152:155], v[80:83]\n"
      "ds_read_b64_tr_b16 a[228:229], v12 offset:8192\n"
      "ds_read_b64_tr_b16 a[230:231], v12 offset:8704\n"
      "v_mfma_f32_16x16x32_bf16 v[84:87], a[240:243], v[156:159], v[84:87]\n"
      "v_cvt_pk_bf16_f32 v50, v116, v117\n"
      "v_mfma_f32_16x16x32_bf16 v[88:91], a[244:247], v[152:155], v[88:91]\n"
      "ds_read_b64_tr_b16 a[232:233], v9 offset:12288\n"
      "ds_read_b64_tr_b16 a[234:235], v9 offset:12800\n"
      "v_mfma_f32_16x16x32_bf16 v[92:95], a[244:247], v[156:159], v[92:95]\n"
      "v_cvt_pk_bf16_f32 v51, v118, v119\n"
      "v_mfma_f32_16x16x32_bf16 v[96:99], a[248:251], v[152:155], v[96:99]\n"
      "ds_read_b64_tr_b16 a[236:237], v10 offset:12288\n"
      "ds_read_b64_tr_b16 a[238:239], v10 offset:12800\n"
      "v_mfma_f32_16x16x32_bf16 v[100:103], a[248:251], v[156:159], v[100:103]\n"
      "v_cvt_pk_bf16_f32 v52, v120, v121\n"
      "v_cvt_pk_bf16_f32 v53, v122, v123\n"
      "v_mfma_f32_16x16x32_bf16 v[104:107], a[252:255], v[152:155], v[104:107]\n"
      "ds_read_b64_tr_b16 a[240:241], v11 offset:12288\n"
      "ds_read_b64_tr_b16 a[242:243], v11 offset:12800\n"
      "v_mfma_f32_16x16x32_bf16 v[108:111], a[252:255], v[156:159], v[108:111]\n"
      "v_cvt_pk_bf16_f32 v54, v124, v125\n"
      "v_cvt_pk_bf16_f32 v55, v126, v127\n"
      "s_waitcnt lgkmcnt(6)\n"
      "v_mfma_f32_16x16x32_bf16 v[80:83], a[216:219], v[160:163], v[80:83]\n"
      "ds_read_b64_tr_b16 a[244:245], v12 offset:12288\n"
      "ds_read_b64_tr_b16 a[246:247], v12 offset:12800\n"
      "v_mfma_f32_16x16x32_bf16 v[84:87], a[216:219], v[164:167], v[84:87]\n"
      "v_permlane16_swap_b32 v48, v50\n"
      "v_mfma_f32_16x16x32_bf16 v[88:91], a[220:223], v[160:163], v[88:91]\n"
      "ds_read_b64_tr_b16 a[248:249], v9 offset:16384\n"
      "ds_read_b64_tr_b16 a[250:251], v9 offset:16896\n"
      "v_mfma_f32_16x16x32_bf16 v[92:95], a[220:223], v[164:167], v[92:95]\n"
      "v_permlane16_swap_b32 v49, v51\n"
      "v_permlane16_swap_b32 v52, v54\n"
      "v_mfma_f32_16x16x32_bf16 v[96:99], a[224:227], v[160:163], v[96:99]\n"
      "ds_read_b64_tr_b16 a[252:253], v10 offset:16384\n"
      "ds_read_b64_tr_b16 a[254:255], v10 offset:16896\n"
      "v_mfma_f32_16x16x32_bf16 v[100:103], a[224:227], v[164:167], v[100:103]\n"
      "v_permlane16_swap_b32 v53, v55\n"
      "v_mfma_f32_16x16x32_bf16 v[104:107], a[228:231], v[160:163], v[104:107]\n"
      "ds_read_b64_tr_b16 a[216:217], v11 offset:16384\n"
      "ds_read_b64_tr_b16 a[218:219], v11 offset:16896\n"
      "v_mfma_f32_16x16x32_bf16 v[108:111], a[228:231], v[164:167], v[108:111]\n"
      "v_mov_b32 v18, 0\n"
      "v_mov_b32 v19, 0\n"
      "s_waitcnt lgkmcnt(6)\n"
      "v_mfma_f32_16x16x32_bf16 v[80:83], a[232:235], v[168:171], v[80:83]\n"
      "ds_read_b64_tr_b16 a[220:221], v12 offset:16384\n"
      "ds_read_b64_tr_b16 a[222:223], v12 offset:16896\n"
      "v_mfma_f32_16x16x32_bf16 v[84:87], a[232:235], v[172:175], v[84:87]\n"
      "buffer_atomic_or_x2 v[18:19], v14, s[48:51], s88 offen offset:0 sc0 sc1\n"
      "v_mfma_f32_16x16x32_bf16 v[88:91], a[236:239], v[168:171], v[88:91]\n"
      "ds_read_b64_tr_b16 a[224:225], v9 offset:20480\n"
      "ds_read_b64_tr_b16 a[226:227], v9 offset:20992\n"
      "v_mfma_f32_16x16x32_bf16 v[92:95], a[236:239], v[172:175], v[92:95]\n"
      "v_cvt_pk_bf16_f32 v56, v128, v129\n"
      "v_cvt_pk_bf16_f32 v57, v130, v131\n"
      "v_mfma_f32_16x16x32_bf16 v[96:99], a[240:243], v[168:171], v[96:99]\n"
      "ds_read_b64_tr_b16 a[228:229], v10 offset:20480\n"
      "ds_read_b64_tr_b16 a[230:231], v10 offset:20992\n"
      "v_mfma_f32_16x16x32_bf16 v[100:103], a[240:243], v[172:175], v[100:103]\n"
      "v_cvt_pk_bf16_f32 v58, v132, v133\n"
      "v_cvt_pk_bf16_f32 v59, v134, v135\n"
      "v_mfma_f32_16x16x32_bf16 v[104:107], a[244:247], v[168:171], v[104:107]\n"
      "ds_read_b64_tr_b16 a[232:233], v11 offset:20480\n"
      "ds_read_b64_tr_b16 a[234:235], v11 offset:20992\n"
      "v_mfma_f32_16x16x32_bf16 v[108:111], a[244:247], v[172:175], v[108:111]\n"
      "v_cvt_pk_bf16_f32 v60, v136, v137\n"
      "v_cvt_pk_bf16_f32 v61, v138, v139\n"
      "s_waitcnt lgkmcnt(6)\n"
      "v_mfma_f32_16x16x32_bf16 v[80:83], a[248:251], v[176:179], v[80:83]\n"
      "ds_read_b64_tr_b16 a[236:237], v12 offset:20480\n"
      "ds_read_b64_tr_b16 a[238:239], v12 offset:20992\n"
      "v_mfma_f32_16x16x32_bf16 v[84:87], a[248:251], v[180:183], v[84:87]\n"
      "v_cvt_pk_bf16_f32 v62, v140, v141\n"
      "v_mfma_f32_16x16x32_bf16 v[88:91], a[252:255], v[176:179], v[88:91]\n"
      "ds_read_b64_tr_b16 a[240:241], v9 offset:24576\n"
      "ds_read_b64_tr_b16 a[242:243], v9 offset:25088\n"
      "v_mfma_f32_16x16x32_bf16 v[92:95], a[252:255], v[180:183], v[92:95]\n"
      "v_cvt_pk_bf16_f32 v63, v142, v143\n"
      ".LPAD15:\n"
      "s_cbranch_execz .LRET\n"
      "v_mfma_f32_16x16x32_bf16 v[96:99], a[216:219], v[176:179], v[96:99]\n"
      "ds_read_b64_tr_b16 a[244:245], v10 offset:24576\n"
      "ds_read_b64_tr_b16 a[246:247], v10 offset:25088\n"
      "v_mfma_f32_16x16x32_bf16 v[100:103], a[216:219], v[180:183], v[100:103]\n"
      "v_permlane16_swap_b32 v56, v58\n"
      "v_permlane16_swap_b32 v57, v59\n"
      "v_mfma_f32_16x16x32_bf16 v[104:107], a[220:223], v[176:179], v[104:107]\n"
      "ds_read_b64_tr_b16 a[248:249], v11 offset:24576\n"
      "ds_read_b64_tr_b16 a[250:251], v11 offset:25088\n"
      "v_mfma_f32_16x16x32_bf16 v[108:111], a[220:223], v[180:183], v[108:111]\n"
      "v_permlane16_swap_b32 v60, v62\n"
      "v_permlane16_swap_b32 v61, v63\n"
      "s_waitcnt lgkmcnt(6)\n"
      "v_mfma_f32_16x16x32_bf16 v[80:83], a[224:227], v[184:187], v[80:83]\n"
      "ds_read_b64_tr_b16 a[252:253], v12 offset:24576\n"
      "ds_read_b64_tr_b16 a[254:255], v12 offset:25088\n"
      "v_mfma_f32_16x16x32_bf16 v[84:87], a[224:227], v[188:191], v[84:87]\n"
      "v_mfma_f32_16x16x32_bf16 v[88:91], a[228:231], v[184:187], v[88:91]\n"
      "ds_read_b64_tr_b16 a[216:217], v9 offset:28672\n"
      "ds_read_b64_tr_b16 a[218:219], v9 offset:29184\n"
      "v_mfma_f32_16x16x32_bf16 v[92:95], a[228:231], v[188:191], v[92:95]\n"
      "v_mfma_f32_16x16x32_bf16 v[96:99], a[232:235], v[184:187], v[96:99]\n"
      "ds_read_b64_tr_b16 a[220:221], v10 offset:28672\n"
      "ds_read_b64_tr_b16 a[222:223], v10 offset:29184\n"
      "v_mfma_f32_16x16x32_bf16 v[100:103], a[232:235], v[188:191], v[100:103]\n"
      "v_mfma_f32_16x16x32_bf16 v[104:107], a[236:239], v[184:187], v[104:107]\n"
      "ds_read_b64_tr_b16 a[224:225], v11 offset:28672\n"
      "ds_read_b64_tr_b16 a[226:227], v11 offset:29184\n"
      "v_mfma_f32_16x16x32_bf16 v[108:111], a[236:239], v[188:191], v[108:111]\n"
      "s_waitcnt lgkmcnt(6)\n"
      "v_mfma_f32_16x16x32_bf16 v[80:83], a[240:243], v[192:195], v[80:83]\n"
      "ds_read_b64_tr_b16 a[228:229], v12 offset:28672\n"
      "ds_read_b64_tr_b16 a[230:231], v12 offset:29184\n"
      "v_mfma_f32_16x16x32_bf16 v[84:87], a[240:243], v[196:199], v[84:87]\n"
      "v_mfma_f32_16x16x32_bf16 v[88:91], a[244:247], v[192:195], v[88:91]\n"
      "ds_read_b64_tr_b16 a[232:233], v9 offset:32768\n"
      "ds_read_b64_tr_b16 a[234:235], v9 offset:33280\n"
      "v_mfma_f32_16x16x32_bf16 v[92:95], a[244:247], v[196:199], v[92:95]\n"
      "v_mfma_f32_16x16x32_bf16 v[96:99], a[248:251], v[192:195], v[96:99]\n"
      "ds_read_b64_tr_b16 a[236:237], v10 offset:32768\n"
      "ds_read_b64_tr_b16 a[238:239], v10 offset:33280\n"
      "v_mfma_f32_16x16x32_bf16 v[100:103], a[248:251], v[196:199], v[100:103]\n"
      "v_mfma_f32_16x16x32_bf16 v[104:107], a[252:255], v[192:195], v[104:107]\n"
      "ds_read_b64_tr_b16 a[240:241], v11 offset:32768\n"
      "ds_read_b64_tr_b16 a[242:243], v11 offset:33280\n"
      "v_mfma_f32_16x16x32_bf16 v[108:111], a[252:255], v[196:199], v[108:111]\n"
      "s_waitcnt lgkmcnt(6)\n"
      "v_mfma_f32_16x16x32_bf16 v[80:83], a[216:219], v[200:203], v[80:83]\n"
      "ds_read_b64_tr_b16 a[244:245], v12 offset:32768\n"
      "ds_read_b64_tr_b16 a[246:247], v12 offset:33280\n"
      "v_mfma_f32_16x16x32_bf16 v[84:87], a[216:219], v[204:207], v[84:87]\n"
      "s_waitcnt vmcnt(0)\n"
      "v_cmp_ne_u64 vcc, s[96:97], v[18:19]\n"
      "s_and_b64 vcc, vcc, 0x3fff3fff\n"
      "s_cbranch_scc1 .LS_KVB\n"
      ".LK_KVB:\n"
      "v_mfma_f32_16x16x32_bf16 v[88:91], a[220:223], v[200:203], v[88:91]\n"
      "ds_read_b64_tr_b16 a[248:249], v9 offset:36864\n"
      "ds_read_b64_tr_b16 a[250:251], v9 offset:37376\n"
      "v_mfma_f32_16x16x32_bf16 v[92:95], a[220:223], v[204:207], v[92:95]\n"
      "s_add_u32 s56, s68, 7168\n"
      "buffer_load_dwordx4 a[0:3], v1, s[48:51], s56 offen offset:0 sc1\n"
      "v_mfma_f32_16x16x32_bf16 v[96:99], a[224:227], v[200:203], v[96:99]\n"
      "ds_read_b64_tr_b16 a[252:253], v10 offset:36864\n"
      "ds_read_b64_tr_b16 a[254:255], v10 offset:37376\n"
      "v_mfma_f32_16x16x32_bf16 v[100:103], a[224:227], v[204:207], v[100:103]\n"
      "v_mov_b32 v24, 0xff\n"
      "v_mov_b32 v25, 0\n"
      "v_mfma_f32_16x16x32_bf16 v[104:107], a[228:231], v[200:203], v[104:107]\n"
      "ds_read_b64_tr_b16 a[216:217], v11 offset:36864\n"
      "ds_read_b64_tr_b16 a[218:219], v11 offset:37376\n"
      "v_mfma_f32_16x16x32_bf16 v[108:111], a[228:231], v[204:207], v[108:111]\n"
      "buffer_load_dwordx4 a[4:7], v1, s[48:51], s56 offen offset:1024 sc1\n"
      "s_waitcnt lgkmcnt(6)\n"
      "v_mfma_f32_16x16x32_bf16 v[80:83], a[232:235], v[208:211], v[80:83]\n"
      "ds_read_b64_tr_b16 a[220:221], v12 offset:36864\n"
      "ds_read_b64_tr_b16 a[222:223], v12 offset:37376\n"
      "v_mfma_f32_16x16x32_bf16 v[84:87], a[232:235], v[212:215], v[84:87]\n"
      "v_lshlrev_b64 v[24:25], v21, v[24:25]\n"
      "v_mfma_f32_16x16x32_bf16 v[88:91], a[236:239], v[208:211], v[88:91]\n"
      "ds_read_b64_tr_b16 a[224:225], v9 offset:40960\n"
      "ds_read_b64_tr_b16 a[226:227], v9 offset:41472\n"
      "v_mfma_f32_16x16x32_bf16 v[92:95], a[236:239], v[212:215], v[92:95]\n"
      "s_add_u32 s57, s69, 7168\n"
      "buffer_load_dwordx4 a[8:11], v2, s[48:51], s57 offen offset:0 sc1\n"
      "buffer_load_dwordx4 a[12:15], v2, s[48:51], s57 offen offset:1024 sc1\n"
      "v_mfma_f32_16x16x32_bf16 v[96:99], a[240:243], v[208:211], v[96:99]\n"
      "ds_read_b64_tr_b16 a[228:229], v10 offset:40960\n"
      "ds_read_b64_tr_b16 a[230:231], v10 offset:41472\n"
      "v_mfma_f32_16x16x32_bf16 v[100:103], a[240:243], v[212:215], v[100:103]\n"
      "v_and_b32 v24, s74, v24\n"
      "v_mov_b32 v26, 0xf149f2ca\n"
      "v_mfma_f32_16x16x32_bf16 v[104:107], a[244:247], v[208:211], v[104:107]\n"
      "ds_read_b64_tr_b16 a[232:233], v11 offset:40960\n"
      "ds_read_b64_tr_b16 a[234:235], v11 offset:41472\n"
      "v_mfma_f32_16x16x32_bf16 v[108:111], a[244:247], v[212:215], v[108:111]\n"
      "buffer_load_dwordx4 a[16:19], v4, s[48:51], s70 offen offset:1792 sc1\n"
      "s_waitcnt lgkmcnt(6)\n"
      "v_mfma_f32_16x16x32_bf16 v[80:83], a[248:251], v[216:219], v[80:83]\n"
      "ds_read_b64_tr_b16 a[236:237], v12 offset:40960\n"
      "ds_read_b64_tr_b16 a[238:239], v12 offset:41472\n"
      "v_mfma_f32_16x16x32_bf16 v[84:87], a[248:251], v[220:223], v[84:87]\n"
      "v_mov_b32 v27, 0xffff\n"
      "v_mfma_f32_16x16x32_bf16 v[88:91], a[252:255], v[216:219], v[88:91]\n"
      "ds_read_b64_tr_b16 a[240:241], v9 offset:45056\n"
      "ds_read_b64_tr_b16 a[242:243], v9 offset:45568\n"
      "v_mfma_f32_16x16x32_bf16 v[92:95], a[252:255], v[220:223], v[92:95]\n"
      "buffer_load_dwordx4 a[20:23], v4, s[48:51], s71 offen offset:1792 sc1\n"
      "v_mfma_f32_16x16x32_bf16 v[96:99], a[216:219], v[216:219], v[96:99]\n"
      "ds_read_b64_tr_b16 a[244:245], v10 offset:45056\n"
      "ds_read_b64_tr_b16 a[246:247], v10 offset:45568\n"
      "v_mfma_f32_16x16x32_bf16 v[100:103], a[216:219], v[220:223], v[100:103]\n"
      "v_mfma_f32_16x16x32_bf16 v[104:107], a[220:223], v[216:219], v[104:107]\n"
      "ds_read_b64_tr_b16 a[248:249], v11 offset:45056\n"
      "ds_read_b64_tr_b16 a[250:251], v11 offset:45568\n"
      "v_mfma_f32_16x16x32_bf16 v[108:111], a[220:223], v[220:223], v[108:111]\n"
      "buffer_load_dwordx2 a[24:25], v4, s[48:51], s72 offen offset:1792 sc1\n"
      "s_waitcnt lgkmcnt(6)\n"
      "v_mfma_f32_16x16x32_bf16 v[80:83], a[224:227], v[224:227], v[80:83]\n"
      "ds_read_b64_tr_b16 a[252:253], v12 offset:45056\n"
      "ds_read_b64_tr_b16 a[254:255], v12 offset:45568\n"
      "v_mfma_f32_16x16x32_bf16 v[84:87], a[224:227], v[228:231], v[84:87]\n"
      "v_mfma_f32_16x16x32_bf16 v[88:91], a[228:231], v[224:227], v[88:91]\n"
      "ds_read_b64_tr_b16 a[216:217], v9 offset:57344\n"
      "ds_read_b64_tr_b16 a[218:219], v9 offset:57856\n"
      "v_mfma_f32_16x16x32_bf16 v[92:95], a[228:231], v[228:231], v[92:95]\n"
      "s_add_u32 s56, s68, 8704\n"
      "buffer_load_dwordx4 a[26:29], v1, s[48:51], s56 offen offset:512 sc1\n"
      "v_mfma_f32_16x16x32_bf16 v[96:99], a[232:235], v[224:227], v[96:99]\n"
      "ds_read_b64_tr_b16 a[220:221], v10 offset:57344\n"
      "ds_read_b64_tr_b16 a[222:223], v10 offset:57856\n"
      "v_mfma_f32_16x16x32_bf16 v[100:103], a[232:235], v[228:231], v[100:103]\n"
      "v_mfma_f32_16x16x32_bf16 v[104:107], a[236:239], v[224:227], v[104:107]\n"
      "ds_read_b64_tr_b16 a[224:225], v11 offset:57344\n"
      "ds_read_b64_tr_b16 a[226:227], v11 offset:57856\n"
      "v_mfma_f32_16x16x32_bf16 v[108:111], a[236:239], v[228:231], v[108:111]\n"
      "buffer_load_dwordx4 a[30:33], v7, s[48:51], s56 offen offset:0 sc1\n"
      "s_waitcnt lgkmcnt(6)\n"
      "v_mfma_f32_16x16x32_bf16 v[80:83], a[240:243], v[232:235], v[80:83]\n"
      "ds_read_b64_tr_b16 a[228:229], v12 offset:57344\n"
      "ds_read_b64_tr_b16 a[230:231], v12 offset:57856\n"
      "v_mfma_f32_16x16x32_bf16 v[84:87], a[240:243], v[236:239], v[84:87]\n"
      "v_mfma_f32_16x16x32_bf16 v[88:91], a[244:247], v[232:235], v[88:91]\n"
      "ds_read_b64_tr_b16 a[232:233], v9 offset:61440\n"
      "ds_read_b64_tr_b16 a[234:235], v9 offset:61952\n"
      "v_mfma_f32_16x16x32_bf16 v[92:95], a[244:247], v[236:239], v[92:95]\n"
      "s_add_u32 s57, s69, 8704\n"
      "buffer_load_dwordx4 a[34:37], v2, s[48:51], s57 offen offset:512 sc1\n"
      "buffer_load_dwordx4 a[38:41], v8, s[48:51], s57 offen offset:0 sc1\n"
      "v_mfma_f32_16x16x32_bf16 v[96:99], a[248:251], v[232:235], v[96:99]\n"
      "ds_read_b64_tr_b16 a[236:237], v10 offset:61440\n"
      "ds_read_b64_tr_b16 a[238:239], v10 offset:61952\n"
      "v_mfma_f32_16x16x32_bf16 v[100:103], a[248:251], v[236:239], v[100:103]\n"
      "v_mfma_f32_16x16x32_bf16 v[104:107], a[252:255], v[232:235], v[104:107]\n"
      "ds_read_b64_tr_b16 a[240:241], v11 offset:61440\n"
      "ds_read_b64_tr_b16 a[242:243], v11 offset:61952\n"
      "v_mfma_f32_16x16x32_bf16 v[108:111], a[252:255], v[236:239], v[108:111]\n"
      "buffer_load_dwordx4 a[42:45], v4, s[48:51], s70 offen offset:2176 sc1\n"
      "s_waitcnt vmcnt(13) lgkmcnt(6)\n"
      "s_barrier\n"
      "v_mfma_f32_16x16x32_bf16 v[80:83], a[216:219], v[240:243], v[80:83]\n"
      "ds_read_b64_tr_b16 a[244:245], v12 offset:61440\n"
      "ds_read_b64_tr_b16 a[246:247], v12 offset:61952\n"
      "v_mfma_f32_16x16x32_bf16 v[84:87], a[216:219], v[244:247], v[84:87]\n"
      "v_xor_b32 v9, 0x18040, v6\n"
      "v_mfma_f32_16x16x32_bf16 v[88:91], a[220:223], v[240:243], v[88:91]\n"
      "ds_read_b64_tr_b16 a[248:249], v9 offset:16384\n"
      "ds_read_b64_tr_b16 a[250:251], v9 offset:16896\n"
      "v_mfma_f32_16x16x32_bf16 v[92:95], a[220:223], v[244:247], v[92:95]\n"
      "buffer_load_dwordx4 a[46:49], v4, s[48:51], s71 offen offset:2176 sc1\n"
      "v_xor_b32 v10, 0x18060, v6\n"
      "v_mfma_f32_16x16x32_bf16 v[96:99], a[224:227], v[240:243], v[96:99]\n"
      "ds_read_b64_tr_b16 a[252:253], v10 offset:16384\n"
      "ds_read_b64_tr_b16 a[254:255], v10 offset:16896\n"
      "v_mfma_f32_16x16x32_bf16 v[100:103], a[224:227], v[244:247], v[100:103]\n"
      "v_mfma_f32_16x16x32_bf16 v[104:107], a[228:231], v[240:243], v[104:107]\n"
      "ds_read_b64_tr_b16 a[216:217], v9 offset:20480\n"
      "ds_read_b64_tr_b16 a[218:219], v9 offset:20992\n"
      "v_mfma_f32_16x16x32_bf16 v[108:111], a[228:231], v[244:247], v[108:111]\n"
      "buffer_load_dwordx2 a[50:51], v4, s[48:51], s72 offen offset:2176 sc1\n"
      "s_waitcnt lgkmcnt(6)\n"
      "v_mfma_f32_16x16x32_bf16 v[80:83], a[232:235], v[248:251], v[80:83]\n"
      "ds_read_b64_tr_b16 a[220:221], v10 offset:20480\n"
      "ds_read_b64_tr_b16 a[222:223], v10 offset:20992\n"
      "v_mfma_f32_16x16x32_bf16 v[84:87], a[232:235], v[252:255], v[84:87]\n"
      "v_mfma_f32_16x16x32_bf16 v[88:91], a[236:239], v[248:251], v[88:91]\n"
      "ds_read_b64_tr_b16 a[224:225], v9 offset:24576\n"
      "ds_read_b64_tr_b16 a[226:227], v9 offset:25088\n"
      "v_mfma_f32_16x16x32_bf16 v[92:95], a[236:239], v[252:255], v[92:95]\n"
      "s_add_u32 s56, s68, 10752\n"
      "buffer_load_dwordx4 a[52:55], v1, s[48:51], s56 offen offset:512 sc1\n"
      "v_mfma_f32_16x16x32_bf16 v[96:99], a[240:243], v[248:251], v[96:99]\n"
      "ds_read_b64_tr_b16 a[228:229], v10 offset:24576\n"
      "ds_read_b64_tr_b16 a[230:231], v10 offset:25088\n"
      "v_mfma_f32_16x16x32_bf16 v[100:103], a[240:243], v[252:255], v[100:103]\n"
      "v_mfma_f32_16x16x32_bf16 v[104:107], a[244:247], v[248:251], v[104:107]\n"
      "ds_read_b64_tr_b16 a[232:233], v9 offset:28672\n"
      "ds_read_b64_tr_b16 a[234:235], v9 offset:29184\n"
      "v_mfma_f32_16x16x32_bf16 v[108:111], a[244:247], v[252:255], v[108:111]\n"
      "buffer_load_dwordx4 a[56:59], v7, s[48:51], s56 offen offset:0 sc1\n"
      "s_waitcnt lgkmcnt(6)\n"
      "v_mfma_f32_16x16x32_bf16 v[128:131], a[248:251], v[144:147], 0\n"
      "ds_read_b64_tr_b16 a[236:237], v10 offset:28672\n"
      "ds_read_b64_tr_b16 a[238:239], v10 offset:29184\n"
      "v_mfma_f32_16x16x32_bf16 v[132:135], a[248:251], v[148:151], 0\n"
      "v_mfma_f32_16x16x32_bf16 v[136:139], a[252:255], v[144:147], 0\n"
      "ds_read_b64_tr_b16 a[240:241], v9 offset:32768\n"
      "ds_read_b64_tr_b16 a[242:243], v9 offset:33280\n"
      "v_mfma_f32_16x16x32_bf16 v[140:143], a[252:255], v[148:151], 0\n"
      "s_add_u32 s57, s69, 10752\n"
      "buffer_load_dwordx4 a[60:63], v2, s[48:51], s57 offen offset:512 sc1\n"
      "buffer_load_dwordx4 a[64:67], v8, s[48:51], s57 offen offset:0 sc1\n"
      "v_mfma_f32_16x16x32_bf16 v[128:131], a[216:219], v[152:155], v[128:131]\n"
      "ds_read_b64_tr_b16 a[244:245], v10 offset:32768\n"
      "ds_read_b64_tr_b16 a[246:247], v10 offset:33280\n"
      "v_mfma_f32_16x16x32_bf16 v[132:135], a[216:219], v[156:159], v[132:135]\n"
      "v_mfma_f32_16x16x32_bf16 v[136:139], a[220:223], v[152:155], v[136:139]\n"
      "ds_read_b64_tr_b16 a[248:249], v9 offset:36864\n"
      "ds_read_b64_tr_b16 a[250:251], v9 offset:37376\n"
      "v_mfma_f32_16x16x32_bf16 v[140:143], a[220:223], v[156:159], v[140:143]\n"
      "buffer_load_dwordx4 a[68:71], v4, s[48:51], s70 offen offset:2688 sc1\n"
      "v_cvt_pk_bf16_f32 v64, v80, v81\n"
      "v_cvt_pk_bf16_f32 v65, v82, v83\n"
      "s_waitcnt lgkmcnt(6)\n"
      "v_mfma_f32_16x16x32_bf16 v[128:131], a[224:227], v[160:163], v[128:131]\n"
      "ds_read_b64_tr_b16 a[252:253], v10 offset:36864\n"
      "ds_read_b64_tr_b16 a[254:255], v10 offset:37376\n"
      "v_mfma_f32_16x16x32_bf16 v[132:135], a[224:227], v[164:167], v[132:135]\n"
      "v_cvt_pk_bf16_f32 v66, v84, v85\n"
      "v_cvt_pk_bf16_f32 v67, v86, v87\n"
      "v_cvt_pk_bf16_f32 v68, v88, v89\n"
      "v_mfma_f32_16x16x32_bf16 v[136:139], a[228:231], v[160:163], v[136:139]\n"
      "ds_read_b64_tr_b16 a[216:217], v9 offset:40960\n"
      "ds_read_b64_tr_b16 a[218:219], v9 offset:41472\n"
      "v_mfma_f32_16x16x32_bf16 v[140:143], a[228:231], v[164:167], v[140:143]\n"
      "buffer_load_dwordx4 a[72:75], v4, s[48:51], s71 offen offset:2688 sc1\n"
      "v_cvt_pk_bf16_f32 v69, v90, v91\n"
      "v_cvt_pk_bf16_f32 v70, v92, v93\n"
      "v_mfma_f32_16x16x32_bf16 v[128:131], a[232:235], v[168:171], v[128:131]\n"
      "ds_read_b64_tr_b16 a[220:221], v10 offset:40960\n"
      "ds_read_b64_tr_b16 a[222:223], v10 offset:41472\n"
      "v_mfma_f32_16x16x32_bf16 v[132:135], a[232:235], v[172:175], v[132:135]\n"
      "v_cvt_pk_bf16_f32 v71, v94, v95\n"
      "v_permlane16_swap_b32 v64, v66\n"
      "v_permlane16_swap_b32 v65, v67\n"
      "v_permlane16_swap_b32 v68, v70\n"
      "v_mfma_f32_16x16x32_bf16 v[136:139], a[236:239], v[168:171], v[136:139]\n"
      "ds_read_b64_tr_b16 a[224:225], v9 offset:45056\n"
      "ds_read_b64_tr_b16 a[226:227], v9 offset:45568\n"
      "v_mfma_f32_16x16x32_bf16 v[140:143], a[236:239], v[172:175], v[140:143]\n"
      "buffer_load_dwordx2 a[76:77], v4, s[48:51], s72 offen offset:2688 sc1\n"
      "v_permlane16_swap_b32 v69, v71\n"
      "v_cvt_pk_bf16_f32 v72, v96, v97\n"
      "v_cvt_pk_bf16_f32 v73, v98, v99\n"
      "s_waitcnt lgkmcnt(6)\n"
      "v_mfma_f32_16x16x32_bf16 v[128:131], a[240:243], v[176:179], v[128:131]\n"
      "ds_read_b64_tr_b16 a[228:229], v10 offset:45056\n"
      "ds_read_b64_tr_b16 a[230:231], v10 offset:45568\n"
      "v_mfma_f32_16x16x32_bf16 v[132:135], a[240:243], v[180:183], v[132:135]\n"
      "v_cvt_pk_bf16_f32 v74, v100, v101\n"
      "v_cvt_pk_bf16_f32 v75, v102, v103\n"
      "v_cvt_pk_bf16_f32 v76, v104, v105\n"
      "v_mfma_f32_16x16x32_bf16 v[136:139], a[244:247], v[176:179], v[136:139]\n"
      "ds_read_b64_tr_b16 a[232:233], v9 offset:49152\n"
      "ds_read_b64_tr_b16 a[234:235], v9 offset:49664\n"
      "v_mfma_f32_16x16x32_bf16 v[140:143], a[244:247], v[180:183], v[140:143]\n"
      "s_add_u32 s56, s68, 12288\n"
      "buffer_load_dwordx4 a[78:81], v1, s[48:51], s56 offen offset:0 sc1\n"
      "v_cvt_pk_bf16_f32 v77, v106, v107\n"
      "v_mfma_f32_16x16x32_bf16 v[128:131], a[248:251], v[184:187], v[128:131]\n"
      "ds_read_b64_tr_b16 a[236:237], v10 offset:49152\n"
      "ds_read_b64_tr_b16 a[238:239], v10 offset:49664\n"
      "v_mfma_f32_16x16x32_bf16 v[132:135], a[248:251], v[188:191], v[132:135]\n"
      "v_cvt_pk_bf16_f32 v78, v108, v109\n"
      "v_cvt_pk_bf16_f32 v79, v110, v111\n"
      "v_permlane16_swap_b32 v72, v74\n"
      "v_permlane16_swap_b32 v73, v75\n"
      "v_mfma_f32_16x16x32_bf16 v[136:139], a[252:255], v[184:187], v[136:139]\n"
      "ds_read_b64_tr_b16 a[240:241], v9 offset:53248\n"
      "ds_read_b64_tr_b16 a[242:243], v9 offset:53760\n"
      "v_mfma_f32_16x16x32_bf16 v[140:143], a[252:255], v[188:191], v[140:143]\n"
      "buffer_load_dwordx4 a[82:85], v1, s[48:51], s56 offen offset:1024 sc1\n"
      "v_permlane16_swap_b32 v76, v78\n"
      "v_permlane16_swap_b32 v77, v79\n"
      "s_waitcnt lgkmcnt(6)\n"
      "v_mfma_f32_16x16x32_bf16 v[128:131], a[216:219], v[192:195], v[128:131]\n"
      "ds_read_b64_tr_b16 a[244:245], v10 offset:53248\n"
      "ds_read_b64_tr_b16 a[246:247], v10 offset:53760\n"
      "v_mfma_f32_16x16x32_bf16 v[132:135], a[216:219], v[196:199], v[132:135]\n"
      "v_bfe_i32 v28, v25, 0, 1\n"
      "v_bfi_b32 v144, v28, 0, v26\n"
      "v_bfe_i32 v29, v25, 1, 1\n"
      "v_bfi_b32 v145, v29, 0, v26\n"
      "v_mfma_f32_16x16x32_bf16 v[136:139], a[220:223], v[192:195], v[136:139]\n"
      "ds_read_b64_tr_b16 a[248:249], v9 offset:57344\n"
      "ds_read_b64_tr_b16 a[250:251], v9 offset:57856\n"
      "v_mfma_f32_16x16x32_bf16 v[140:143], a[220:223], v[196:199], v[140:143]\n"
      "s_add_u32 s57, s69, 12288\n"
      "buffer_load_dwordx4 a[86:89], v2, s[48:51], s57 offen offset:0 sc1\n"
      "buffer_load_dwordx4 a[90:93], v2, s[48:51], s57 offen offset:1024 sc1\n"
      "v_mfma_f32_16x16x32_bf16 v[128:131], a[224:227], v[200:203], v[128:131]\n"
      "ds_read_b64_tr_b16 a[252:253], v10 offset:57344\n"
      "ds_read_b64_tr_b16 a[254:255], v10 offset:57856\n"
      "v_mfma_f32_16x16x32_bf16 v[132:135], a[224:227], v[204:207], v[132:135]\n"
      "v_bfi_b32 v164, v27, v28, v29\n"
      "v_bfe_i32 v28, v25, 2, 1\n"
      "v_bfi_b32 v146, v28, 0, v26\n"
      "v_bfe_i32 v29, v25, 3, 1\n"
      "v_mfma_f32_16x16x32_bf16 v[136:139], a[228:231], v[200:203], v[136:139]\n"
      "ds_read_b64_tr_b16 a[216:217], v9 offset:61440\n"
      "ds_read_b64_tr_b16 a[218:219], v9 offset:61952\n"
      "v_mfma_f32_16x16x32_bf16 v[140:143], a[228:231], v[204:207], v[140:143]\n"
      "buffer_load_dwordx4 a[94:97], v4, s[48:51], s70 offen offset:3072 sc1\n"
      "v_bfi_b32 v147, v29, 0, v26\n"
      "v_bfi_b32 v165, v27, v28, v29\n"
      "v_bfe_i32 v28, v25, 8, 1\n"
      "s_waitcnt lgkmcnt(6)\n"
      "v_mfma_f32_16x16x32_bf16 v[128:131], a[232:235], v[208:211], v[128:131]\n"
      "ds_read_b64_tr_b16 a[220:221], v10 offset:61440\n"
      "ds_read_b64_tr_b16 a[222:223], v10 offset:61952\n"
      "v_mfma_f32_16x16x32_bf16 v[132:135], a[232:235], v[212:215], v[132:135]\n"
      "v_xor_b32 v11, 0x40, v6\n"
      "v_bfi_b32 v148, v28, 0, v26\n"
      "v_bfe_i32 v29, v25, 9, 1\n"
      "v_bfi_b32 v149, v29, 0, v26\n"
      "v_mfma_f32_16x16x32_bf16 v[136:139], a[236:239], v[208:211], v[136:139]\n"
      "ds_read_b64_tr_b16 a[224:225], v11 offset:0\n"
      "ds_read_b64_tr_b16 a[226:227], v11 offset:512\n"
      "v_mfma_f32_16x16x32_bf16 v[140:143], a[236:239], v[212:215], v[140:143]\n"
      "buffer_load_dwordx4 a[98:101], v4, s[48:51], s71 offen offset:3072 sc1\n"
      "v_xor_b32 v12, 0x60, v6\n"
      "v_bfi_b32 v166, v27, v28, v29\n"
      "v_mfma_f32_16x16x32_bf16 v[128:131], a[240:243], v[216:219], v[128:131]\n"
      "ds_read_b64_tr_b16 a[228:229], v12 offset:0\n"
      "ds_read_b64_tr_b16 a[230:231], v12 offset:512\n"
      "v_mfma_f32_16x16x32_bf16 v[132:135], a[240:243], v[220:223], v[132:135]\n"
      "v_bfe_i32 v28, v25, 10, 1\n"
      "v_bfi_b32 v150, v28, 0, v26\n"
      "v_bfe_i32 v29, v25, 11, 1\n"
      "v_bfi_b32 v151, v29, 0, v26\n"
      "v_bfi_b32 v167, v27, v28, v29\n"
      "v_mfma_f32_16x16x32_bf16 v[136:139], a[244:247], v[216:219], v[136:139]\n"
      "ds_read_b64_tr_b16 a[232:233], v11 offset:4096\n"
      "ds_read_b64_tr_b16 a[234:235], v11 offset:4608\n"
      "v_mfma_f32_16x16x32_bf16 v[140:143], a[244:247], v[220:223], v[140:143]\n"
      "buffer_load_dwordx2 a[102:103], v4, s[48:51], s72 offen offset:3072 sc1\n"
      "v_bfe_i32 v28, v25, 16, 1\n"
      "v_bfi_b32 v152, v28, 0, v26\n"
      "v_bfe_i32 v29, v25, 17, 1\n"
      "s_waitcnt lgkmcnt(6)\n"
      "v_mfma_f32_16x16x32_bf16 v[128:131], a[248:251], v[224:227], v[128:131]\n"
      "ds_read_b64_tr_b16 a[236:237], v12 offset:4096\n"
      "ds_read_b64_tr_b16 a[238:239], v12 offset:4608\n"
      "v_mfma_f32_16x16x32_bf16 v[132:135], a[248:251], v[228:231], v[132:135]\n"
      "v_bfi_b32 v153, v29, 0, v26\n"
      "v_bfi_b32 v168, v27, v28, v29\n"
      "v_bfe_i32 v28, v25, 18, 1\n"
      "v_mfma_f32_16x16x32_bf16 v[136:139], a[252:255], v[224:227], v[136:139]\n"
      "v_mfma_f32_16x16x32_bf16 v[140:143], a[252:255], v[228:231], v[140:143]\n"
      "s_add_u32 s56, s68, 0\n"
      "buffer_load_dwordx4 a[104:107], v1, s[48:51], s56 offen offset:0 sc1\n"
      "v_bfi_b32 v154, v28, 0, v26\n"
      "v_bfe_i32 v29, v25, 19, 1\n"
      "v_bfi_b32 v155, v29, 0, v26\n"
      "v_bfi_b32 v169, v27, v28, v29\n"
      "v_mfma_f32_16x16x32_bf16 v[128:131], a[216:219], v[232:235], v[128:131]\n"
      "v_mfma_f32_16x16x32_bf16 v[132:135], a[216:219], v[236:239], v[132:135]\n"
      "v_bfe_i32 v28, v25, 24, 1\n"
      "v_bfi_b32 v156, v28, 0, v26\n"
      "v_bfe_i32 v29, v25, 25, 1\n"
      "v_bfi_b32 v157, v29, 0, v26\n"
      "v_bfi_b32 v170, v27, v28, v29\n"
      "v_bfe_i32 v28, v25, 26, 1\n"
      "v_bfi_b32 v158, v28, 0, v26\n"
      "v_mfma_f32_16x16x32_bf16 v[136:139], a[220:223], v[232:235], v[136:139]\n"
      "v_mfma_f32_16x16x32_bf16 v[140:143], a[220:223], v[236:239], v[140:143]\n"
      "buffer_load_dwordx4 a[108:111], v1, s[48:51], s56 offen offset:1024 sc1\n"
      "v_bfe_i32 v29, v25, 27, 1\n"
      "v_bfi_b32 v159, v29, 0, v26\n"
      "v_bfi_b32 v171, v27, v28, v29\n"
      "v_bfe_i32 v28, v24, 24, 1\n"
      "s_waitcnt lgkmcnt(0)\n"
      "v_mfma_f32_16x16x32_bf16 v[128:131], a[224:227], v[240:243], v[128:131]\n"
      "v_mfma_f32_16x16x32_bf16 v[132:135], a[224:227], v[244:247], v[132:135]\n"
      "v_bfi_b32 v160, v28, 0, v26\n"
      "v_bfe_i32 v29, v24, 25, 1\n"
      "v_bfi_b32 v161, v29, 0, v26\n"
      "v_bfi_b32 v172, v27, v28, v29\n"
      "v_bfe_i32 v28, v24, 26, 1\n"
      "v_bfi_b32 v162, v28, 0, v26\n"
      "v_mfma_f32_16x16x32_bf16 v[136:139], a[228:231], v[240:243], v[136:139]\n"
      "v_mfma_f32_16x16x32_bf16 v[140:143], a[228:231], v[244:247], v[140:143]\n"
      "s_add_u32 s57, s69, 0\n"
      "buffer_load_dwordx4 a[112:115], v2, s[48:51], s57 offen offset:0 sc1\n"
      "buffer_load_dwordx4 a[116:119], v2, s[48:51], s57 offen offset:1024 sc1\n"
      "v_bfe_i32 v29, v24, 27, 1\n"
      "v_bfi_b32 v163, v29, 0, v26\n"
      "v_mfma_f32_16x16x32_bf16 v[128:131], a[232:235], v[248:251], v[128:131]\n"
      "v_mfma_f32_16x16x32_bf16 v[132:135], a[232:235], v[252:255], v[132:135]\n"
      "v_bfi_b32 v173, v27, v28, v29\n"
      "v_and_b32 v188, v22, v64\n"
      "v_and_b32 v189, v22, v65\n"
      "v_bfi_b32 v190, v22, v66, v58\n"
      "v_bfi_b32 v191, v22, v67, v59\n"
      "v_bfi_b32 v64, v22, v72, v64\n"
      "v_mfma_f32_16x16x32_bf16 v[136:139], a[236:239], v[248:251], v[136:139]\n"
      "v_mfma_f32_16x16x32_bf16 v[140:143], a[236:239], v[252:255], v[140:143]\n"
      "v_bfi_b32 v65, v22, v73, v65\n"
      "v_and_b32 v66, v23, v66\n"
      "v_and_b32 v67, v23, v67\n"
      "v_and_b32 v58, v22, v58\n"
      "v_and_b32 v59, v22, v59\n"
      "v_and_b32 v72, v23, v72\n"
      "v_and_b32 v73, v23, v73\n"
      "buffer_load_dwordx4 a[120:123], v4, s[48:51], s70 offen offset:0 sc1\n"
      "s_waitcnt vmcnt(29)\n"
      "v_mfma_f32_32x32x16_bf16 v[80:95], a[0:3], v[52:55], v[144:159]\n"
      "s_waitcnt vmcnt(22)\n"
      "s_waitcnt vmcnt(15)\n"
      "v_cvt_pk_bf16_f32 v176, v128, v129\n"
      "v_cvt_pk_bf16_f32 v177, v130, v131\n"
      "v_cvt_pk_bf16_f32 v178, v132, v133\n"
      "v_cvt_pk_bf16_f32 v179, v134, v135\n"
      "v_mfma_f32_32x32x16_bf16 v[96:111], a[8:11], v[52:55], v[148:163]\n"
      "v_cvt_pk_bf16_f32 v180, v136, v137\n"
      "v_cvt_pk_bf16_f32 v181, v138, v139\n"
      "v_cvt_pk_bf16_f32 v182, v140, v141\n"
      "v_cvt_pk_bf16_f32 v183, v142, v143\n"
      "v_permlane16_swap_b32 v176, v178\n"
      "v_permlane16_swap_b32 v177, v179\n"
      "buffer_load_dwordx4 a[124:127], v4, s[48:51], s71 offen offset:0 sc1\n"
      "v_mfma_f32_32x32x16_bf16 v[80:95], a[4:7], v[56:59], v[80:95]\n"
      "v_permlane16_swap_b32 v180, v182\n"
      "v_permlane16_swap_b32 v181, v183\n"
      "v_and_b32 v184, v22, v36\n"
      "v_and_b32 v185, v22, v37\n"
      "v_bfi_b32 v186, v22, v38, v182\n"
      "v_bfi_b32 v187, v22, v39, v183\n"
      "v_mfma_f32_32x32x16_bf16 v[96:111], a[12:15], v[56:59], v[96:111]\n"
      "v_bfi_b32 v36, v22, v44, v36\n"
      "v_bfi_b32 v37, v22, v45, v37\n"
      "v_and_b32 v38, v23, v38\n"
      "v_and_b32 v39, v23, v39\n"
      "v_and_b32 v182, v22, v182\n"
      "v_max3_f32 v80, v80, v84, v88\n"
      "buffer_load_dwordx2 a[128:129], v4, s[48:51], s72 offen offset:0 sc1\n"
      "v_max3_f32 v81, v81, v85, v89\n"
      "v_max3_f32 v82, v82, v86, v90\n"
      "v_max3_f32 v83, v83, v87, v91\n"
      "v_mfma_f32_32x32x16_bf16 v[192:207], a[26:29], v[60:63], v[144:159]\n"
      "v_and_b32 v183, v22, v183\n"
      "v_max3_f32 v80, v80, v92, v108\n"
      "v_max3_f32 v81, v81, v93, v109\n"
      "v_max3_f32 v82, v82, v94, v110\n"
      "v_max3_f32 v83, v83, v95, v111\n"
      "v_max_f32 v86, v80, v81\n"
      "v_max3_f32 v84, v86, v82, v83\n"
      "v_mov_b32 v85, v84\n"
      "v_mov_b32 v104, 0\n"
      "v_mov_b32 v105, 0\n"
      "v_permlane32_swap_b32 v84, v85\n"
      "v_max_f32 v84, v84, v85\n"
      "s_add_u32 s56, s68, 1536\n"
      "buffer_load_dwordx4 a[130:133], v1, s[48:51], s56 offen offset:512 sc1\n"
      "v_mul_f32 v84, v84, v20\n"
      "v_fma_f32 v80, v80, v20, -v84\n"
      "v_fma_f32 v81, v81, v20, -v84\n"
      "v_fma_f32 v82, v82, v20, -v84\n"
      "v_fma_f32 v83, v83, v20, -v84\n"
      "v_exp_f32 v80, v80\n"
      "v_exp_f32 v81, v81\n"
      "v_exp_f32 v82, v82\n"
      "v_exp_f32 v83, v83\n"
      "v_add_f32 v86, v80, v81\n"
      "v_add_f32 v87, v82, v83\n"
      "buffer_load_dwordx4 a[134:137], v7, s[48:51], s56 offen offset:0 sc1\n"
      "v_add_f32 v84, v86, v87\n"
      "v_add_f32 v85, v86, v87\n"
      "v_mfma_f32_32x32x16_bf16 v[208:223], a[34:37], v[60:63], v[148:163]\n"
      "v_and_b32 v44, v23, v44\n"
      "v_permlane32_swap_b32 v84, v85\n"
      "v_add_f32 v84, v84, v85\n"
      "v_rcp_f32 v84, v84\n"
      "v_and_b32 v45, v23, v45\n"
      "v_mul_f32 v80, v80, v84\n"
      "v_mul_f32 v81, v81, v84\n"
      "v_mul_f32 v82, v82, v84\n"
      "v_mul_f32 v83, v83, v84\n"
      "v_cvt_pk_bf16_f32 v84, v80, v81\n"
      "v_cvt_pk_bf16_f32 v85, v82, v83\n"
      "v_and_b32 v96, v164, v84\n"
      "s_add_u32 s57, s69, 1536\n"
      "buffer_load_dwordx4 a[138:141], v2, s[48:51], s57 offen offset:512 sc1\n"
      "buffer_load_dwordx4 a[142:145], v8, s[48:51], s57 offen offset:0 sc1\n"
      "v_and_b32 v97, v165, v85\n"
      "v_and_b32 v98, v166, v84\n"
      "v_and_b32 v99, v167, v85\n"
      "v_and_b32 v100, v168, v84\n"
      "v_and_b32 v101, v169, v85\n"
      "v_and_b32 v102, v170, v84\n"
      "v_and_b32 v103, v171, v85\n"
      "v_and_b32 v106, v172, v84\n"
      "v_and_b32 v107, v173, v85\n"
      "s_waitcnt vmcnt(32)\n"
      "v_mfma_f32_32x32x16_bf16 v[80:95], a[16:19], v[96:99], 0\n"
      "s_waitcnt vmcnt(14)\n"
      "buffer_load_dwordx4 a[146:149], v4, s[48:51], s70 offen offset:384 sc1\n"
      "v_mfma_f32_32x32x16_bf16 v[80:95], a[20:23], v[100:103], v[80:95]\n"
      "v_mfma_f32_32x32x16_bf16 v[80:95], a[22:25], v[104:107], v[80:95]\n"
      "v_mfma_f32_32x32x16_bf16 v[192:207], a[30:33], v[188:191], v[192:207]\n"
      "buffer_load_dwordx4 a[150:153], v4, s[48:51], s71 offen offset:384 sc1\n"
      "v_mfma_f32_32x32x16_bf16 v[208:223], a[38:41], v[188:191], v[208:223]\n"
      "v_mfma_f32_32x32x16_bf16 v[224:239], a[52:55], v[68:71], v[144:159]\n"
      "buffer_load_dwordx2 a[154:155], v4, s[48:51], s72 offen offset:384 sc1\n"
      "v_mfma_f32_32x32x16_bf16 v[240:255], a[60:63], v[68:71], v[148:163]\n"
      "s_nop 5\n"
      "v_cvt_pk_bf16_f32 v52, v80, v81\n"
      "v_mfma_f32_32x32x16_bf16 v[224:239], a[56:59], v[64:67], v[224:239]\n"
      "s_add_u32 s56, s68, 3584\n"
      "buffer_load_dwordx4 a[156:159], v1, s[48:51], s56 offen offset:512 sc1\n"
      "v_cvt_pk_bf16_f32 v53, v82, v83\n"
      "v_mfma_f32_32x32x16_bf16 v[240:255], a[64:67], v[64:67], v[240:255]\n"
      "v_cvt_pk_bf16_f32 v54, v84, v85\n"
      "v_cvt_pk_bf16_f32 v55, v86, v87\n"
      "v_cvt_pk_bf16_f32 v56, v88, v89\n"
      "v_cvt_pk_bf16_f32 v57, v90, v91\n"
      "v_cvt_pk_bf16_f32 v58, v92, v93\n"
      "v_cvt_pk_bf16_f32 v59, v94, v95\n"
      "v_max3_f32 v192, v192, v196, v200\n"
      "v_max3_f32 v193, v193, v197, v201\n"
      "v_max3_f32 v194, v194, v198, v202\n"
      "v_max3_f32 v195, v195, v199, v203\n"
      "buffer_load_dwordx4 a[160:163], v7, s[48:51], s56 offen offset:0 sc1\n"
      "v_max3_f32 v192, v192, v204, v220\n"
      "v_max3_f32 v193, v193, v205, v221\n"
      "v_max3_f32 v194, v194, v206, v222\n"
      "v_max3_f32 v195, v195, v207, v223\n"
      "v_max_f32 v198, v192, v193\n"
      "v_max3_f32 v196, v198, v194, v195\n"
      "v_mov_b32 v197, v196\n"
      "v_mov_b32 v216, 0\n"
      "v_mov_b32 v217, 0\n"
      "v_permlane32_swap_b32 v196, v197\n"
      "v_max_f32 v196, v196, v197\n"
      "v_mul_f32 v196, v196, v20\n"
      "v_fma_f32 v192, v192, v20, -v196\n"
      "v_fma_f32 v193, v193, v20, -v196\n"
      "v_fma_f32 v194, v194, v20, -v196\n"
      "v_fma_f32 v195, v195, v20, -v196\n"
      "v_exp_f32 v192, v192\n"
      "s_add_u32 s57, s69, 3584\n"
      "buffer_load_dwordx4 a[164:167], v2, s[48:51], s57 offen offset:512 sc1\n"
      "buffer_load_dwordx4 a[168:171], v8, s[48:51], s57 offen offset:0 sc1\n"
      "v_exp_f32 v193, v193\n"
      "v_exp_f32 v194, v194\n"
      "v_exp_f32 v195, v195\n"
      "v_add_f32 v198, v192, v193\n"
      "v_add_f32 v199, v194, v195\n"
      "v_add_f32 v196, v198, v199\n"
      "v_add_f32 v197, v198, v199\n"
      "v_max3_f32 v224, v224, v228, v232\n"
      "v_max3_f32 v225, v225, v229, v233\n"
      "buffer_load_dwordx4 a[172:175], v4, s[48:51], s70 offen offset:896 sc1\n"
      "v_permlane32_swap_b32 v196, v197\n"
      "v_add_f32 v196, v196, v197\n"
      "v_rcp_f32 v196, v196\n"
      "v_max3_f32 v226, v226, v230, v234\n"
      "v_mul_f32 v192, v192, v196\n"
      "v_mul_f32 v193, v193, v196\n"
      "v_mul_f32 v194, v194, v196\n"
      "v_mul_f32 v195, v195, v196\n"
      ".LPAD16:\n"
      "s_cbranch_execz .LRET\n"
      "v_cvt_pk_bf16_f32 v196, v192, v193\n"
      "v_cvt_pk_bf16_f32 v197, v194, v195\n"
      "v_and_b32 v208, v164, v196\n"
      "v_and_b32 v209, v165, v197\n"
      "v_and_b32 v210, v166, v196\n"
      "v_and_b32 v211, v167, v197\n"
      "v_and_b32 v212, v168, v196\n"
      "v_and_b32 v213, v169, v197\n"
      "buffer_load_dwordx4 a[176:179], v4, s[48:51], s71 offen offset:896 sc1\n"
      "v_and_b32 v214, v170, v196\n"
      "v_and_b32 v215, v171, v197\n"
      "v_and_b32 v218, v172, v196\n"
      "v_and_b32 v219, v173, v197\n"
      "s_waitcnt vmcnt(34)\n"
      "v_mfma_f32_32x32x16_bf16 v[192:207], a[42:45], v[208:211], 0\n"
      "v_max3_f32 v227, v227, v231, v235\n"
      "v_max3_f32 v224, v224, v236, v252\n"
      "v_max3_f32 v225, v225, v237, v253\n"
      "v_max3_f32 v226, v226, v238, v254\n"
      "v_max3_f32 v227, v227, v239, v255\n"
      "v_max_f32 v230, v224, v225\n"
      "v_mfma_f32_32x32x16_bf16 v[192:207], a[46:49], v[212:215], v[192:207]\n"
      "v_max3_f32 v228, v230, v226, v227\n"
      "buffer_load_dwordx2 a[180:181], v4, s[48:51], s72 offen offset:896 sc1\n"
      "v_mov_b32 v229, v228\n"
      "v_mov_b32 v248, 0\n"
      "v_mov_b32 v249, 0\n"
      "v_mfma_f32_32x32x16_bf16 v[192:207], a[48:51], v[216:219], v[192:207]\n"
      "v_permlane32_swap_b32 v228, v229\n"
      "v_max_f32 v228, v228, v229\n"
      "v_mul_f32 v228, v228, v20\n"
      "v_fma_f32 v224, v224, v20, -v228\n"
      "v_fma_f32 v225, v225, v20, -v228\n"
      "v_fma_f32 v226, v226, v20, -v228\n"
      "v_fma_f32 v227, v227, v20, -v228\n"
      "v_exp_f32 v224, v224\n"
      "v_exp_f32 v225, v225\n"
      "v_exp_f32 v226, v226\n"
      "s_add_u32 s56, s68, 5120\n"
      "buffer_load_dwordx4 a[182:185], v1, s[48:51], s56 offen offset:0 sc1\n"
      "v_cvt_pk_bf16_f32 v60, v192, v193\n"
      "v_cvt_pk_bf16_f32 v61, v194, v195\n"
      "v_cvt_pk_bf16_f32 v62, v196, v197\n"
      "v_cvt_pk_bf16_f32 v63, v198, v199\n"
      "v_cvt_pk_bf16_f32 v188, v200, v201\n"
      "v_cvt_pk_bf16_f32 v189, v202, v203\n"
      "v_cvt_pk_bf16_f32 v190, v204, v205\n"
      "v_cvt_pk_bf16_f32 v191, v206, v207\n"
      "v_permlane32_swap_b32 v56, v60\n"
      "v_permlane32_swap_b32 v57, v61\n"
      "s_add_u32 s75, s82, 7168\n"
      "v_exp_f32 v227, v227\n"
      "v_add_f32 v230, v224, v225\n"
      "buffer_load_dwordx4 a[186:189], v1, s[48:51], s56 offen offset:1024 sc1\n"
      "v_add_f32 v231, v226, v227\n"
      "v_add_f32 v228, v230, v231\n"
      "v_add_f32 v229, v230, v231\n"
      "v_mfma_f32_32x32x16_bf16 v[112:127], a[78:81], v[72:75], v[144:159]\n"
      "s_waitcnt vmcnt(19)\n"
      "v_permlane32_swap_b32 v228, v229\n"
      "v_add_f32 v228, v228, v229\n"
      "v_rcp_f32 v228, v228\n"
      "s_nop 0\n"
      "v_mul_f32 v224, v224, v228\n"
      "v_mul_f32 v225, v225, v228\n"
      "v_mul_f32 v226, v226, v228\n"
      "v_mul_f32 v227, v227, v228\n"
      "v_cvt_pk_bf16_f32 v228, v224, v225\n"
      "v_cvt_pk_bf16_f32 v229, v226, v227\n"
      "s_add_u32 s57, s69, 5120\n"
      "buffer_load_dwordx4 a[190:193], v2, s[48:51], s57 offen offset:0 sc1\n"
      "buffer_load_dwordx4 a[194:197], v2, s[48:51], s57 offen offset:1024 sc1\n"
      "v_and_b32 v240, v164, v228\n"
      "v_and_b32 v241, v165, v229\n"
      "v_and_b32 v242, v166, v228\n"
      "v_and_b32 v243, v167, v229\n"
      "v_and_b32 v244, v168, v228\n"
      "v_and_b32 v245, v169, v229\n"
      "v_and_b32 v246, v170, v228\n"
      "v_and_b32 v247, v171, v229\n"
      "v_and_b32 v250, v172, v228\n"
      "v_and_b32 v251, v173, v229\n"
      "s_waitcnt vmcnt(32)\n"
      "v_mfma_f32_32x32x16_bf16 v[224:239], a[68:71], v[240:243], 0\n"
      "buffer_load_dwordx4 a[198:201], v4, s[48:51], s70 offen offset:1280 sc1\n"
      "v_mfma_f32_32x32x16_bf16 v[224:239], a[72:75], v[244:247], v[224:239]\n"
      "s_waitcnt vmcnt(15)\n"
      "v_mfma_f32_32x32x16_bf16 v[224:239], a[74:77], v[248:251], v[224:239]\n"
      "v_mfma_f32_32x32x16_bf16 v[128:143], a[86:89], v[72:75], v[148:163]\n"
      "buffer_load_dwordx4 a[202:205], v4, s[48:51], s71 offen offset:1280 sc1\n"
      "v_mfma_f32_32x32x16_bf16 v[112:127], a[82:85], v[76:79], v[112:127]\n"
      "v_mfma_f32_32x32x16_bf16 v[128:143], a[90:93], v[76:79], v[128:143]\n"
      "buffer_load_dwordx2 a[206:207], v4, s[48:51], s72 offen offset:1280 sc1\n"
      "buffer_store_dwordx4 v[52:55], v1, s[48:51], s75 offen offset:0 sc1\n"
      "v_mfma_f32_32x32x16_bf16 v[80:95], a[104:107], v[176:179], v[144:159]\n"
      "s_nop 4\n"
      "v_cvt_pk_bf16_f32 v68, v224, v225\n"
      "v_mfma_f32_32x32x16_bf16 v[96:111], a[112:115], v[176:179], v[148:163]\n"
      "v_cvt_pk_bf16_f32 v69, v226, v227\n"
      "v_cvt_pk_bf16_f32 v70, v228, v229\n"
      "buffer_store_dwordx4 v[56:59], v15, s[48:51], s75 offen offset:1024 sc1\n"
      "v_cvt_pk_bf16_f32 v71, v230, v231\n"
      "v_cvt_pk_bf16_f32 v64, v232, v233\n"
      "v_cvt_pk_bf16_f32 v65, v234, v235\n"
      "v_cvt_pk_bf16_f32 v66, v236, v237\n"
      "v_cvt_pk_bf16_f32 v67, v238, v239\n"
      "v_max3_f32 v112, v112, v116, v120\n"
      "v_max3_f32 v113, v113, v117, v121\n"
      "v_max3_f32 v114, v114, v118, v122\n"
      "v_max3_f32 v115, v115, v119, v123\n"
      "v_max3_f32 v112, v112, v124, v140\n"
      "v_max3_f32 v113, v113, v125, v141\n"
      "v_max3_f32 v114, v114, v126, v142\n"
      "v_max3_f32 v115, v115, v127, v143\n"
      "v_max_f32 v118, v112, v113\n"
      "v_max3_f32 v116, v118, v114, v115\n"
      "v_mov_b32 v117, v116\n"
      "v_mov_b32 v136, 0\n"
      "v_mov_b32 v137, 0\n"
      "buffer_store_dwordx4 v[60:63], v1, s[48:51], s75 offen offset:1536 sc1\n"
      "v_permlane32_swap_b32 v116, v117\n"
      "v_max_f32 v116, v116, v117\n"
      "v_mul_f32 v116, v116, v20\n"
      "v_fma_f32 v112, v112, v20, -v116\n"
      "v_fma_f32 v113, v113, v20, -v116\n"
      "v_fma_f32 v114, v114, v20, -v116\n"
      "v_fma_f32 v115, v115, v20, -v116\n"
      "v_exp_f32 v112, v112\n"
      "v_exp_f32 v113, v113\n"
      "v_exp_f32 v114, v114\n"
      "v_exp_f32 v115, v115\n"
      "v_add_f32 v118, v112, v113\n"
      "v_add_f32 v119, v114, v115\n"
      "v_add_f32 v116, v118, v119\n"
      "buffer_store_dwordx4 v[188:191], v1, s[48:51], s75 offen offset:2560 sc1\n"
      "v_add_f32 v117, v118, v119\n"
      "v_mfma_f32_32x32x16_bf16 v[80:95], a[108:111], v[180:183], v[80:95]\n"
      "s_nop 0\n"
      "v_permlane32_swap_b32 v116, v117\n"
      "v_add_f32 v116, v116, v117\n"
      "v_rcp_f32 v116, v116\n"
      "s_nop 0\n"
      "v_mul_f32 v112, v112, v116\n"
      "v_mul_f32 v113, v113, v116\n"
      "v_mul_f32 v114, v114, v116\n"
      "v_mul_f32 v115, v115, v116\n"
      "v_cvt_pk_bf16_f32 v116, v112, v113\n"
      "v_cvt_pk_bf16_f32 v117, v114, v115\n"
      "v_and_b32 v128, v164, v116\n"
      "v_and_b32 v129, v165, v117\n"
      "v_and_b32 v130, v166, v116\n"
      "v_and_b32 v131, v167, v117\n"
      "v_and_b32 v132, v168, v116\n"
      "v_and_b32 v133, v169, v117\n"
      "v_and_b32 v134, v170, v116\n"
      "v_and_b32 v135, v171, v117\n"
      "v_and_b32 v138, v172, v116\n"
      "v_and_b32 v139, v173, v117\n"
      "s_waitcnt vmcnt(32)\n"
      "v_mfma_f32_32x32x16_bf16 v[112:127], a[94:97], v[128:131], 0\n"
      "s_waitcnt vmcnt(14)\n"
      "v_mfma_f32_32x32x16_bf16 v[112:127], a[98:101], v[132:135], v[112:127]\n"
      "v_mfma_f32_32x32x16_bf16 v[112:127], a[100:103], v[136:139], v[112:127]\n"
      "v_mfma_f32_32x32x16_bf16 v[96:111], a[116:119], v[180:183], v[96:111]\n"
      "v_max3_f32 v80, v80, v84, v88\n"
      "v_max3_f32 v81, v81, v85, v89\n"
      "v_max3_f32 v82, v82, v86, v90\n"
      "v_max3_f32 v83, v83, v87, v91\n"
      "v_mfma_f32_32x32x16_bf16 v[192:207], a[130:133], v[32:35], v[144:159]\n"
      "s_nop 5\n"
      "v_cvt_pk_bf16_f32 v72, v112, v113\n"
      "v_mfma_f32_32x32x16_bf16 v[208:223], a[138:141], v[32:35], v[148:163]\n"
      "v_cvt_pk_bf16_f32 v73, v114, v115\n"
      "v_mfma_f32_32x32x16_bf16 v[192:207], a[134:137], v[184:187], v[192:207]\n"
      "v_cvt_pk_bf16_f32 v74, v116, v117\n"
      "v_cvt_pk_bf16_f32 v75, v118, v119\n"
      "v_cvt_pk_bf16_f32 v76, v120, v121\n"
      "v_cvt_pk_bf16_f32 v77, v122, v123\n"
      "v_cvt_pk_bf16_f32 v78, v124, v125\n"
      "v_cvt_pk_bf16_f32 v79, v126, v127\n"
      "v_permlane32_swap_b32 v64, v72\n"
      "v_permlane32_swap_b32 v65, v73\n"
      "s_add_u32 s75, s82, 10752\n"
      "buffer_store_dwordx4 v[68:71], v1, s[48:51], s75 offen offset:0 sc1\n"
      "v_max3_f32 v80, v80, v92, v108\n"
      "v_max3_f32 v81, v81, v93, v109\n"
      "v_max3_f32 v82, v82, v94, v110\n"
      "v_max3_f32 v83, v83, v95, v111\n"
      "v_max_f32 v86, v80, v81\n"
      "v_max3_f32 v84, v86, v82, v83\n"
      "v_mov_b32 v85, v84\n"
      "v_mov_b32 v104, 0\n"
      "v_mov_b32 v105, 0\n"
      "v_permlane32_swap_b32 v84, v85\n"
      "v_max_f32 v84, v84, v85\n"
      "v_mul_f32 v84, v84, v20\n"
      "v_fma_f32 v80, v80, v20, -v84\n"
      "v_fma_f32 v81, v81, v20, -v84\n"
      "v_fma_f32 v82, v82, v20, -v84\n"
      "v_fma_f32 v83, v83, v20, -v84\n"
      "v_exp_f32 v80, v80\n"
      "buffer_store_dwordx4 v[64:67], v15, s[48:51], s75 offen offset:1024 sc1\n"
      "v_exp_f32 v81, v81\n"
      "v_exp_f32 v82, v82\n"
      "v_exp_f32 v83, v83\n"
      "v_add_f32 v86, v80, v81\n"
      "v_add_f32 v87, v82, v83\n"
      "v_add_f32 v84, v86, v87\n"
      "v_add_f32 v85, v86, v87\n"
      "v_mfma_f32_32x32x16_bf16 v[208:223], a[142:145], v[184:187], v[208:223]\n"
      "v_max3_f32 v192, v192, v196, v200\n"
      "v_permlane32_swap_b32 v84, v85\n"
      "v_add_f32 v84, v84, v85\n"
      "v_rcp_f32 v84, v84\n"
      "v_max3_f32 v193, v193, v197, v201\n"
      "buffer_store_dwordx4 v[72:75], v1, s[48:51], s75 offen offset:1536 sc1\n"
      "v_mul_f32 v80, v80, v84\n"
      "v_mul_f32 v81, v81, v84\n"
      "v_mul_f32 v82, v82, v84\n"
      "v_mul_f32 v83, v83, v84\n"
      "v_cvt_pk_bf16_f32 v84, v80, v81\n"
      "v_cvt_pk_bf16_f32 v85, v82, v83\n"
      "v_and_b32 v96, v164, v84\n"
      "v_and_b32 v97, v165, v85\n"
      "v_and_b32 v98, v166, v84\n"
      "v_and_b32 v99, v167, v85\n"
      "v_and_b32 v100, v168, v84\n"
      "v_and_b32 v101, v169, v85\n"
      "v_and_b32 v102, v170, v84\n"
      "v_and_b32 v103, v171, v85\n"
      "v_and_b32 v106, v172, v84\n"
      "v_and_b32 v107, v173, v85\n"
      "s_waitcnt vmcnt(28)\n"
      "v_mfma_f32_32x32x16_bf16 v[80:95], a[120:123], v[96:99], 0\n"
      "buffer_store_dwordx4 v[76:79], v1, s[48:51], s75 offen offset:2560 sc1\n"
      "v_max3_f32 v194, v194, v198, v202\n"
      "v_max3_f32 v195, v195, v199, v203\n"
      "v_max3_f32 v192, v192, v204, v220\n"
      "v_max3_f32 v193, v193, v205, v221\n"
      "v_mfma_f32_32x32x16_bf16 v[80:95], a[124:127], v[100:103], v[80:95]\n"
      "v_max3_f32 v194, v194, v206, v222\n"
      "v_max3_f32 v195, v195, v207, v223\n"
      "v_max_f32 v198, v192, v193\n"
      "v_max3_f32 v196, v198, v194, v195\n"
      "v_mov_b32 v197, v196\n"
      "v_mov_b32 v216, 0\n"
      "v_mfma_f32_32x32x16_bf16 v[80:95], a[126:129], v[104:107], v[80:95]\n"
      "v_mov_b32 v217, 0\n"
      "v_permlane32_swap_b32 v196, v197\n"
      "v_max_f32 v196, v196, v197\n"
      "v_mul_f32 v196, v196, v20\n"
      "v_fma_f32 v192, v192, v20, -v196\n"
      "v_fma_f32 v193, v193, v20, -v196\n"
      "v_fma_f32 v194, v194, v20, -v196\n"
      "v_fma_f32 v195, v195, v20, -v196\n"
      "v_exp_f32 v192, v192\n"
      "v_exp_f32 v193, v193\n"
      "v_exp_f32 v194, v194\n"
      "v_exp_f32 v195, v195\n"
      "v_cvt_pk_bf16_f32 v176, v80, v81\n"
      "v_cvt_pk_bf16_f32 v177, v82, v83\n"
      "v_cvt_pk_bf16_f32 v178, v84, v85\n"
      "v_cvt_pk_bf16_f32 v179, v86, v87\n"
      "v_cvt_pk_bf16_f32 v180, v88, v89\n"
      "v_cvt_pk_bf16_f32 v181, v90, v91\n"
      "v_cvt_pk_bf16_f32 v182, v92, v93\n"
      "v_cvt_pk_bf16_f32 v183, v94, v95\n"
      "v_add_f32 v198, v192, v193\n"
      "v_add_f32 v199, v194, v195\n"
      "v_add_f32 v196, v198, v199\n"
      "v_add_f32 v197, v198, v199\n"
      "v_mfma_f32_32x32x16_bf16 v[224:239], a[156:159], v[40:43], v[144:159]\n"
      "s_waitcnt vmcnt(11)\n"
      "v_permlane32_swap_b32 v196, v197\n"
      "v_add_f32 v196, v196, v197\n"
      "v_rcp_f32 v196, v196\n"
      "s_nop 0\n"
      "v_mul_f32 v192, v192, v196\n"
      "v_mul_f32 v193, v193, v196\n"
      "v_mul_f32 v194, v194, v196\n"
      "v_mul_f32 v195, v195, v196\n"
      "v_cvt_pk_bf16_f32 v196, v192, v193\n"
      "v_cvt_pk_bf16_f32 v197, v194, v195\n"
      "v_and_b32 v208, v164, v196\n"
      "v_and_b32 v209, v165, v197\n"
      "v_and_b32 v210, v166, v196\n"
      "v_and_b32 v211, v167, v197\n"
      "v_and_b32 v212, v168, v196\n"
      "v_and_b32 v213, v169, v197\n"
      "v_and_b32 v214, v170, v196\n"
      "v_and_b32 v215, v171, v197\n"
      "v_and_b32 v218, v172, v196\n"
      "v_and_b32 v219, v173, v197\n"
      "s_waitcnt vmcnt(22)\n"
      "v_mfma_f32_32x32x16_bf16 v[192:207], a[146:149], v[208:211], 0\n"
      "v_mfma_f32_32x32x16_bf16 v[192:207], a[150:153], v[212:215], v[192:207]\n"
      "v_mfma_f32_32x32x16_bf16 v[192:207], a[152:155], v[216:219], v[192:207]\n"
      "v_mfma_f32_32x32x16_bf16 v[240:255], a[164:167], v[40:43], v[148:163]\n"
      "v_mfma_f32_32x32x16_bf16 v[224:239], a[160:163], v[36:39], v[224:239]\n"
      "v_mfma_f32_32x32x16_bf16 v[240:255], a[168:171], v[36:39], v[240:255]\n"
      "v_mfma_f32_32x32x16_bf16 v[112:127], a[182:185], v[44:47], v[144:159]\n"
      "v_mfma_f32_32x32x16_bf16 v[128:143], a[190:193], v[44:47], v[148:163]\n"
      "v_mfma_f32_32x32x16_bf16 v[112:127], a[186:189], v[48:51], v[112:127]\n"
      "s_nop 5\n"
      "v_cvt_pk_bf16_f32 v32, v192, v193\n"
      "v_mfma_f32_32x32x16_bf16 v[128:143], a[194:197], v[48:51], v[128:143]\n"
      "v_cvt_pk_bf16_f32 v33, v194, v195\n"
      "v_cvt_pk_bf16_f32 v34, v196, v197\n"
      "v_cvt_pk_bf16_f32 v35, v198, v199\n"
      "v_cvt_pk_bf16_f32 v184, v200, v201\n"
      "v_cvt_pk_bf16_f32 v185, v202, v203\n"
      "v_cvt_pk_bf16_f32 v186, v204, v205\n"
      "v_cvt_pk_bf16_f32 v187, v206, v207\n"
      "v_permlane32_swap_b32 v180, v32\n"
      "v_permlane32_swap_b32 v181, v33\n"
      "s_add_u32 s75, s82, 0\n"
      "buffer_store_dwordx4 v[176:179], v1, s[48:51], s75 offen offset:0 sc1\n"
      "v_max3_f32 v224, v224, v228, v232\n"
      "v_max3_f32 v225, v225, v229, v233\n"
      "v_max3_f32 v226, v226, v230, v234\n"
      "v_max3_f32 v227, v227, v231, v235\n"
      "v_max3_f32 v224, v224, v236, v252\n"
      "v_max3_f32 v225, v225, v237, v253\n"
      "v_max3_f32 v226, v226, v238, v254\n"
      "v_max3_f32 v227, v227, v239, v255\n"
      "v_max_f32 v230, v224, v225\n"
      "v_max3_f32 v228, v230, v226, v227\n"
      "v_mov_b32 v229, v228\n"
      "v_mov_b32 v248, 0\n"
      "v_mov_b32 v249, 0\n"
      "v_permlane32_swap_b32 v228, v229\n"
      "v_max_f32 v228, v228, v229\n"
      "v_mul_f32 v228, v228, v20\n"
      "v_fma_f32 v224, v224, v20, -v228\n"
      "v_fma_f32 v225, v225, v20, -v228\n"
      "buffer_store_dwordx4 v[180:183], v15, s[48:51], s75 offen offset:1024 sc1\n"
      "v_fma_f32 v226, v226, v20, -v228\n"
      "v_fma_f32 v227, v227, v20, -v228\n"
      "v_exp_f32 v224, v224\n"
      "v_exp_f32 v225, v225\n"
      "v_exp_f32 v226, v226\n"
      "v_exp_f32 v227, v227\n"
      "v_add_f32 v230, v224, v225\n"
      "v_add_f32 v231, v226, v227\n"
      "v_add_f32 v228, v230, v231\n"
      "v_add_f32 v229, v230, v231\n"
      "v_max3_f32 v112, v112, v116, v120\n"
      "v_max3_f32 v113, v113, v117, v121\n"
      "v_permlane32_swap_b32 v228, v229\n"
      "v_add_f32 v228, v228, v229\n"
      "buffer_store_dwordx4 v[32:35], v1, s[48:51], s75 offen offset:1536 sc1\n"
      "v_rcp_f32 v228, v228\n"
      "v_max3_f32 v114, v114, v118, v122\n"
      "v_mul_f32 v224, v224, v228\n"
      "v_mul_f32 v225, v225, v228\n"
      "v_mul_f32 v226, v226, v228\n"
      "v_mul_f32 v227, v227, v228\n"
      "v_cvt_pk_bf16_f32 v228, v224, v225\n"
      "v_cvt_pk_bf16_f32 v229, v226, v227\n"
      "v_and_b32 v240, v164, v228\n"
      "v_and_b32 v241, v165, v229\n"
      "v_and_b32 v242, v166, v228\n"
      "v_and_b32 v243, v167, v229\n"
      "v_and_b32 v244, v168, v228\n"
      "v_and_b32 v245, v169, v229\n"
      "v_and_b32 v246, v170, v228\n"
      "v_and_b32 v247, v171, v229\n"
      "v_and_b32 v250, v172, v228\n"
      "buffer_store_dwordx4 v[184:187], v1, s[48:51], s75 offen offset:2560 sc1\n"
      "v_and_b32 v251, v173, v229\n"
      "s_waitcnt vmcnt(19)\n"
      "v_mfma_f32_32x32x16_bf16 v[224:239], a[172:175], v[240:243], 0\n"
      "v_max3_f32 v115, v115, v119, v123\n"
      "v_max3_f32 v112, v112, v124, v140\n"
      "v_max3_f32 v113, v113, v125, v141\n"
      "v_max3_f32 v114, v114, v126, v142\n"
      "v_max3_f32 v115, v115, v127, v143\n"
      "v_max_f32 v118, v112, v113\n"
      "v_mfma_f32_32x32x16_bf16 v[224:239], a[176:179], v[244:247], v[224:239]\n"
      "v_max3_f32 v116, v118, v114, v115\n"
      "v_mov_b32 v117, v116\n"
      "v_mov_b32 v136, 0\n"
      "v_mov_b32 v137, 0\n"
      "v_permlane32_swap_b32 v116, v117\n"
      "v_max_f32 v116, v116, v117\n"
      "v_mfma_f32_32x32x16_bf16 v[224:239], a[178:181], v[248:251], v[224:239]\n"
      "v_mul_f32 v116, v116, v20\n"
      "v_fma_f32 v112, v112, v20, -v116\n"
      "v_fma_f32 v113, v113, v20, -v116\n"
      "v_fma_f32 v114, v114, v20, -v116\n"
      "v_fma_f32 v115, v115, v20, -v116\n"
      "v_exp_f32 v112, v112\n"
      "v_exp_f32 v113, v113\n"
      "v_exp_f32 v114, v114\n"
      "v_exp_f32 v115, v115\n"
      "v_add_f32 v118, v112, v113\n"
      "v_add_f32 v119, v114, v115\n"
      "v_add_f32 v116, v118, v119\n"
      "v_cvt_pk_bf16_f32 v40, v224, v225\n"
      "v_cvt_pk_bf16_f32 v41, v226, v227\n"
      "v_cvt_pk_bf16_f32 v42, v228, v229\n"
      "v_cvt_pk_bf16_f32 v43, v230, v231\n"
      "v_cvt_pk_bf16_f32 v36, v232, v233\n"
      "v_cvt_pk_bf16_f32 v37, v234, v235\n"
      "v_cvt_pk_bf16_f32 v38, v236, v237\n"
      "v_cvt_pk_bf16_f32 v39, v238, v239\n"
      "v_add_f32 v117, v118, v119\n"
      "s_nop 1\n"
      "v_permlane32_swap_b32 v116, v117\n"
      "v_add_f32 v116, v116, v117\n"
      "v_rcp_f32 v116, v116\n"
      "s_nop 0\n"
      "v_mul_f32 v112, v112, v116\n"
      "v_mul_f32 v113, v113, v116\n"
      "v_mul_f32 v114, v114, v116\n"
      "v_mul_f32 v115, v115, v116\n"
      "v_cvt_pk_bf16_f32 v116, v112, v113\n"
      "v_cvt_pk_bf16_f32 v117, v114, v115\n"
      "v_and_b32 v128, v164, v116\n"
      "v_and_b32 v129, v165, v117\n"
      "v_and_b32 v130, v166, v116\n"
      "v_and_b32 v131, v167, v117\n"
      "v_and_b32 v132, v168, v116\n"
      "v_and_b32 v133, v169, v117\n"
      "v_and_b32 v134, v170, v116\n"
      "v_and_b32 v135, v171, v117\n"
      "v_and_b32 v138, v172, v116\n"
      "v_and_b32 v139, v173, v117\n"
      "s_waitcnt vmcnt(12)\n"
      "v_mfma_f32_32x32x16_bf16 v[112:127], a[198:201], v[128:131], 0\n"
      "v_mfma_f32_32x32x16_bf16 v[112:127], a[202:205], v[132:135], v[112:127]\n"
      "v_mfma_f32_32x32x16_bf16 v[112:127], a[204:207], v[136:139], v[112:127]\n"
      "s_nop 11\n"
      "v_cvt_pk_bf16_f32 v44, v112, v113\n"
      "v_cvt_pk_bf16_f32 v45, v114, v115\n"
      "v_cvt_pk_bf16_f32 v46, v116, v117\n"
      "v_cvt_pk_bf16_f32 v47, v118, v119\n"
      "v_cvt_pk_bf16_f32 v48, v120, v121\n"
      "v_cvt_pk_bf16_f32 v49, v122, v123\n"
      "v_cvt_pk_bf16_f32 v50, v124, v125\n"
      "v_cvt_pk_bf16_f32 v51, v126, v127\n"
      "v_permlane32_swap_b32 v36, v44\n"
      "v_permlane32_swap_b32 v37, v45\n"
      "s_add_u32 s75, s82, 3584\n"
      "buffer_store_dwordx4 v[40:43], v1, s[48:51], s75 offen offset:0 sc1\n"
      "buffer_store_dwordx4 v[36:39], v15, s[48:51], s75 offen offset:1024 sc1\n"
      "buffer_store_dwordx4 v[44:47], v1, s[48:51], s75 offen offset:1536 sc1\n"
      "buffer_store_dwordx4 v[48:51], v1, s[48:51], s75 offen offset:2560 sc1\n"
      "s_waitcnt vmcnt(0)\n"
      "buffer_store_dwordx2 v[16:17], v13, s[48:51], s89 offen offset:896 sc1\n"
      "s_endpgm\n"
      ".LS_KVB:\n"
      "s_mov_b32 s99, 200000\n"
      ".LR_KVB:\n"
      "s_sleep 2\n"
      "v_mov_b32 v18, 0\n"
      "v_mov_b32 v19, 0\n"
      "buffer_atomic_or_x2 v[18:19], v14, s[48:51], s88 offen offset:0 sc0 sc1\n"
      "s_waitcnt vmcnt(0)\n"
      "v_cmp_ne_u64 vcc, s[96:97], v[18:19]\n"
      "s_nop 0\n"
      "s_and_b64 vcc, vcc, 0x3fff3fff\n"
      "s_cbranch_scc0 .LK_KVB\n"
      "s_sub_u32 s99, s99, 1\n"
      "s_cbranch_scc0 .LR_KVB\n"
      "s_branch .LK_KVB\n"
      ".LEND:\n"
      :
      : "{s[0:1]}"(__builtin_amdgcn_kernarg_segment_ptr()), "{s2}"(blockIdx.x), "{v0}"(threadIdx.x),
        "{s3}"((__attribute__((address_space(3))) char*)lds)
      : "v1", "v2", "v3", "v4", "v5", "v6", "v7", "v8", "v9", "v10", "v11", "v12", "v13", "v14", "v15", "v16",
      "v17", "v18", "v19", "v20", "v21", "v22", "v23", "v24", "v25", "v26", "v27", "v28", "v29", "v30", "v31", "v32",
      "v33", "v34", "v35", "v36", "v37", "v38", "v39", "v40", "v41", "v42", "v43", "v44", "v45", "v46", "v47", "v48",
      "v49", "v50", "v51", "v52", "v53", "v54", "v55", "v56", "v57", "v58", "v59", "v60", "v61", "v62", "v63", "v64",
      "v65", "v66", "v67", "v68", "v69", "v70", "v71", "v72", "v73", "v74", "v75", "v76", "v77", "v78", "v79", "v80",
      "v81", "v82", "v83", "v84", "v85", "v86", "v87", "v88", "v89", "v90", "v91", "v92", "v93", "v94", "v95", "v96",
      "v97", "v98", "v99", "v100", "v101", "v102", "v103", "v104", "v105", "v106", "v107", "v108", "v109", "v110", "v111", "v112",
      "v113", "v114", "v115", "v116", "v117", "v118", "v119", "v120", "v121", "v122", "v123", "v124", "v125", "v126", "v127", "v128",
      "v129", "v130", "v131", "v132", "v133", "v134", "v135", "v136", "v137", "v138", "v139", "v140", "v141", "v142", "v143", "v144",
      "v145", "v146", "v147", "v148", "v149", "v150", "v151", "v152", "v153", "v154", "v155", "v156", "v157", "v158", "v159", "v160",
      "v161", "v162", "v163", "v164", "v165", "v166", "v167", "v168", "v169", "v170", "v171", "v172", "v173", "v174", "v175", "v176",
      "v177", "v178", "v179", "v180", "v181", "v182", "v183", "v184", "v185", "v186", "v187", "v188", "v189", "v190", "v191", "v192",
      "v193", "v194", "v195", "v196", "v197", "v198", "v199", "v200", "v201", "v202", "v203", "v204", "v205", "v206", "v207", "v208",
      "v209", "v210", "v211", "v212", "v213", "v214", "v215", "v216", "v217", "v218", "v219", "v220", "v221", "v222", "v223", "v224",
      "v225", "v226", "v227", "v228", "v229", "v230", "v231", "v232", "v233", "v234", "v235", "v236", "v237", "v238", "v239", "v240",
      "v241", "v242", "v243", "v244", "v245", "v246", "v247", "v248", "v249", "v250", "v251", "v252", "v253", "v254", "v255", "a0",
      "a1", "a2", "a3", "a4", "a5", "a6", "a7", "a8", "a9", "a10", "a11", "a12", "a13", "a14", "a15", "a16",
      "a17", "a18", "a19", "a20", "a21", "a22", "a23", "a24", "a25", "a26", "a27", "a28", "a29", "a30", "a31", "a32",
      "a33", "a34", "a35", "a36", "a37", "a38", "a39", "a40", "a41", "a42", "a43", "a44", "a45", "a46", "a47", "a48",
      "a49", "a50", "a51", "a52", "a53", "a54", "a55", "a56", "a57", "a58", "a59", "a60", "a61", "a62", "a63", "a64",
      "a65", "a66", "a67", "a68", "a69", "a70", "a71", "a72", "a73", "a74", "a75", "a76", "a77", "a78", "a79", "a80",
      "a81", "a82", "a83", "a84", "a85", "a86", "a87", "a88", "a89", "a90", "a91", "a92", "a93", "a94", "a95", "a96",
      "a97", "a98", "a99", "a100", "a101", "a102", "a103", "a104", "a105", "a106", "a107", "a108", "a109", "a110", "a111", "a112",
      "a113", "a114", "a115", "a116", "a117", "a118", "a119", "a120", "a121", "a122", "a123", "a124", "a125", "a126", "a127", "a128",
      "a129", "a130", "a131", "a132", "a133", "a134", "a135", "a136", "a137", "a138", "a139", "a140", "a141", "a142", "a143", "a144",
      "a145", "a146", "a147", "a148", "a149", "a150", "a151", "a152", "a153", "a154", "a155", "a156", "a157", "a158", "a159", "a160",
      "a161", "a162", "a163", "a164", "a165", "a166", "a167", "a168", "a169", "a170", "a171", "a172", "a173", "a174", "a175", "a176",
      "a177", "a178", "a179", "a180", "a181", "a182", "a183", "a184", "a185", "a186", "a187", "a188", "a189", "a190", "a191", "a192",
      "a193", "a194", "a195", "a196", "a197", "a198", "a199", "a200", "a201", "a202", "a203", "a204", "a205", "a206", "a207", "a208",
      "a209", "a210", "a211", "a212", "a213", "a214", "a215", "a216", "a217", "a218", "a219", "a220", "a221", "a222", "a223", "a224",
      "a225", "a226", "a227", "a228", "a229", "a230", "a231", "a232", "a233", "a234", "a235", "a236", "a237", "a238", "a239", "a240",
      "a241", "a242", "a243", "a244", "a245", "a246", "a247", "a248", "a249", "a250", "a251", "a252", "a253", "a254", "a255", "s4",
      "s5", "s6", "s7", "s8", "s9", "s10", "s11", "s12", "s13", "s14", "s15", "s16", "s17", "s18", "s19", "s20",
      "s21", "s22", "s23", "s24", "s25", "s26", "s27", "s28", "s29", "s30", "s31", "s34", "s35", "s36", "s37", "s38",
      "s39", "s40", "s41", "s42", "s43", "s44", "s45", "s46", "s47", "s48", "s49", "s50", "s51", "s52", "s53", "s54",
      "s55", "s56", "s57", "s58", "s59", "s60", "s61", "s62", "s63", "s64", "s65", "s66", "s67", "s68", "s69", "s70",
      "s71", "s72", "s73", "s74", "s75", "s76", "s77", "s78", "s79", "s80", "s81", "s82", "s83", "s84", "s85", "s86",
      "s87", "s88", "s89", "s90", "s91", "s92", "s93", "s94", "s95", "s96", "s97", "s98", "s99", "vcc", "scc", "memory");
#endif
}

extern "C" void kernel_launch(void* const* d_in, const int* in_sizes, int n_in, void* d_out, int out_size, void* d_ws,
                              size_t ws_size, hipStream_t stream) {
  const unsigned F = 0x20000u;
  fused_block_kernel<<<256, 256, 0, stream>>>(d_in[5], 448u * 3584u, F, d_in[0], 2048u * 896u, F, d_in[1], 448u * 2688u, F, d_in[4],
                                              448u * 3584u, F, d_in[6], 896u * 896u, F, d_in[2], 224u * 896u, F, d_out, 2048u * 896u,
                                              F, d_ws, 0x1200000u, F);
}
```
